```python
import torch

def _softmax(x, axis=-1):
    return torch.softmax(x, dim=axis)
def _gelu(x):
    return torch.nn.functional.gelu(x, approximate="tanh")
def _silu(x):
    return torch.nn.functional.silu(x)
def _sigmoid(x):
    return torch.sigmoid(x)
def _layernorm(x, axis=-1, eps=1e-5):
    m = x.mean(dim=axis, keepdim=True)
    v = x.var(dim=axis, keepdim=True, unbiased=False)
    return (x - m) / torch.sqrt(v + eps)
def _scatter_add(tgt, idx, src, axis=0):
    return tgt.clone().index_add_(axis, idx, src)
def _segment_sum(src, seg, n):
    out = torch.zeros((n,) + src.shape[1:], dtype=src.dtype)
    return out.index_add_(0, seg, src)
def _pad(x, pw):
    flat = tuple(v for pair in reversed(pw) for v in pair)
    return torch.nn.functional.pad(x, flat)

kernel_name = 'standalone__gqa_block_cached__near_ridge'

def setup_inputs():

    _w = (lambda a: (lambda t: t.to(torch.bfloat16) if t.is_floating_point() else t)(torch.as_tensor(a)))
    B, Sq, D = 8, 64, 576
    Hq, Hkv, Sk, Dh = 32, 16, 512, 128
    assert Hq % Hkv == 0, (Hq, Hkv)
    return {
        "x": _w((lambda *s: torch.randn(*s, dtype=torch.bfloat16) * 0.8)(B, Sq, D)),
        "k_cache": _w((lambda *s: torch.randn(*s, dtype=torch.bfloat16) * 0.8)(B, Hkv, Sk, Dh)),
        "v_cache": _w((lambda *s: torch.randn(*s, dtype=torch.bfloat16) * 0.05)(B, Hkv, Sk, Dh)),
        "wq": _w((lambda *s: torch.randn(*s, dtype=torch.bfloat16) * 0.8)(D, Hq * Dh) * (D ** -0.5)),
        "wo": _w((lambda *s: torch.randn(*s, dtype=torch.bfloat16) * 0.05)(Hq * Dh, D) * ((Hq * Dh) ** -0.5)),
    }

def reference(x, k_cache, v_cache, wq, wo):
    B, Sq, D = x.shape
    Hq, Dh = 32, 128
    Hkv = k_cache.shape[1]
    rep = Hq // Hkv
    q = (lambda x, *d: x.permute(*d))((x @ wq).reshape(B, Sq, Hq, Dh), 0, 2, 1, 3)
    k = (lambda x, n, axis: torch.repeat_interleave(x, n, dim=axis))(k_cache, rep, axis=1)
    v = (lambda x, n, axis: torch.repeat_interleave(x, n, dim=axis))(v_cache, rep, axis=1)
    scores = torch.einsum("bhqd,bhkd->bhqk", q, k) * (Dh ** -0.5)
    p = _softmax(scores, axis=-1)
    o = torch.einsum("bhqk,bhkd->bhqd", p, v)
    return (lambda x, *d: x.permute(*d))(o, 0, 2, 1, 3).reshape(B, Sq, Hq * Dh) @ wo
```

```cpp
#include <hip/hip_runtime.h>
#include <hip/hip_bf16.h>
typedef unsigned short u16;
__device__ __forceinline__ float bf2f(u16 x) { return __uint_as_float(((unsigned)x) << 16); }
__device__ __forceinline__ u16 f2bf(float f) { unsigned u = __float_as_uint(f); u += 0x7fffu + ((u >> 16) & 1u); return (u16)(u >> 16); }
constexpr int B = 8, SQ = 64, D = 576, HQ = 32, HKV = 16, SK = 512, DH = 128;
__global__ __launch_bounds__(256) void k_q(const u16* x, const u16* wq, float* q) {
  int col = blockIdx.x * 256 + threadIdx.x; int r0 = blockIdx.y * 8;
  __shared__ float xs[8][D];
  for (int i = threadIdx.x; i < 8 * D; i += 256) xs[i / D][i % D] = bf2f(x[(r0 + i / D) * D + i % D]);
  __syncthreads();
  float acc[8] = {};
  for (int k = 0; k < D; ++k) { float w = bf2f(wq[k * 4096 + col]);
    #pragma unroll
    for (int r = 0; r < 8; ++r) acc[r] += xs[r][k] * w; }
  for (int r = 0; r < 8; ++r) q[(r0 + r) * 4096 + col] = acc[r];
}
__global__ __launch_bounds__(512) void k_attn(const float* q, const u16* kc, const u16* vc, float* o) {
  int qg = blockIdx.x, h = blockIdx.y, b = blockIdx.z; int t = threadIdx.x;
  __shared__ float qs[8][DH]; __shared__ float ps[8][SK]; __shared__ float red[8][8];
  for (int i = t; i < 8 * DH; i += 512) qs[i / DH][i % DH] = q[(b * SQ + qg * 8 + i / DH) * 4096 + h * DH + i % DH];
  __syncthreads();
  const u16* kr = kc + ((size_t)(b * HKV + h / 2) * SK + t) * DH;
  float s[8] = {};
  for (int d = 0; d < DH; ++d) { float kv = bf2f(kr[d]);
    #pragma unroll
    for (int r = 0; r < 8; ++r) s[r] += qs[r][d] * kv; }
  float m[8];
  for (int r = 0; r < 8; ++r) { s[r] *= 0.08838834764831845f; float v = s[r];
    for (int off = 32; off; off >>= 1) v = fmaxf(v, __shfl_xor(v, off));
    if ((t & 63) == 0) red[r][t >> 6] = v; }
  __syncthreads();
  for (int r = 0; r < 8; ++r) { float v = red[r][0]; for (int w = 1; w < 8; ++w) v = fmaxf(v, red[r][w]); m[r] = v; }
  __syncthreads();
  for (int r = 0; r < 8; ++r) { float p = __expf(s[r] - m[r]); ps[r][t] = p; float v = p;
    for (int off = 32; off; off >>= 1) v += __shfl_xor(v, off);
    if ((t & 63) == 0) red[r][t >> 6] = v; }
  __syncthreads();
  int d = t & 127, rg = t >> 7;
  const u16* vr = vc + (size_t)(b * HKV + h / 2) * SK * DH + d;
  float a0 = 0, a1 = 0;
  for (int k = 0; k < SK; ++k) { float v = bf2f(vr[k * DH]); a0 += ps[rg * 2][k] * v; a1 += ps[rg * 2 + 1][k] * v; }
  float l0 = 0, l1 = 0; for (int w = 0; w < 8; ++w) { l0 += red[rg * 2][w]; l1 += red[rg * 2 + 1][w]; }
  o[(b * SQ + qg * 8 + rg * 2) * 4096 + h * DH + d] = a0 / l0;
  o[(b * SQ + qg * 8 + rg * 2 + 1) * 4096 + h * DH + d] = a1 / l1;
}
__global__ __launch_bounds__(576) void k_o(const float* o, const u16* wo, u16* out) {
  int r0 = blockIdx.x * 2; int col = threadIdx.x;
  __shared__ float os[2][4096];
  for (int i = threadIdx.x; i < 2 * 4096; i += 576) os[i / 4096][i % 4096] = o[(r0 + i / 4096) * 4096 + i % 4096];
  __syncthreads();
  float a0 = 0, a1 = 0;
  for (int k = 0; k < 4096; ++k) { float w = bf2f(wo[k * D + col]); a0 += os[0][k] * w; a1 += os[1][k] * w; }
  out[r0 * D + col] = f2bf(a0); out[(r0 + 1) * D + col] = f2bf(a1);
}
extern "C" void kernel_launch(void* const* d_in, const int* in_sizes, int n_in,
                              void* d_out, int out_size, void* d_ws, size_t ws_size,
                              hipStream_t stream) {
  const u16* x = (const u16*)d_in[0]; const u16* kc = (const u16*)d_in[1]; const u16* vc = (const u16*)d_in[2];
  const u16* wq = (const u16*)d_in[3]; const u16* wo = (const u16*)d_in[4];
  float* q = (float*)d_ws; float* o = q + 512 * 4096;
  k_q<<<dim3(16, 64), 256, 0, stream>>>(x, wq, q);
  k_attn<<<dim3(8, 32, 8), 512, 0, stream>>>(q, kc, vc, o);
  k_o<<<256, 576, 0, stream>>>(o, wo, (u16*)d_out);
}
```

```cpp
#include <hip/hip_runtime.h>
#include <hip/hip_bf16.h>

typedef unsigned short u16;
typedef unsigned long long u64;
typedef short bf16x8 __attribute__((ext_vector_type(8)));
typedef short s16x4 __attribute__((ext_vector_type(4)));
typedef float f32x16 __attribute__((ext_vector_type(16)));
typedef float f32x2 __attribute__((ext_vector_type(2)));
typedef __bf16 bf16x2 __attribute__((ext_vector_type(2)));
typedef unsigned u32x4 __attribute__((ext_vector_type(4)));
typedef unsigned u32x2 __attribute__((ext_vector_type(2)));
#define LAS __attribute__((address_space(3)))
#define DI __device__ __forceinline__
typedef __amdgpu_buffer_rsrc_t rsrc_t;

constexpr int L_P = 65536, L_Q = 81920, L_ST = 98304;
constexpr int slot_addr(int g) { return (g % 7) < 3 ? 114688 + (g % 7) * 16384 : ((g % 7) - 3) * 16384; }
constexpr unsigned SLAB = 40960, S_O = 0, S_T = 16384, S_M = 24576;
constexpr unsigned W_FLAG = 16u << 20, W_EPO = W_FLAG + 65536, W_BYTES = W_EPO + 4096;
constexpr int SC1 = 16, NT = 2;
constexpr unsigned SPIN_CAP = 1u << 20;

#define ST(i) do {} while (0)

DI rsrc_t mk(const void* p, unsigned n) { return __builtin_amdgcn_make_buffer_rsrc((void*)p, 0, n, 0x00020000); }
#define DMA(R, LDSOFF, V, SOFF, POL) __builtin_amdgcn_raw_ptr_buffer_load_lds(R, (LAS void*)(sm + (LDSOFF)), 16, V, SOFF, 0, POL)
#define LBAR(N) asm volatile("s_waitcnt vmcnt(%0)\n\ts_barrier" :: "i"(N) : "memory")
#define CBAR() asm volatile("s_waitcnt lgkmcnt(0)\n\ts_barrier" ::: "memory")
#define MFMA(a, b, c) __builtin_amdgcn_mfma_f32_32x32x16_bf16(a, b, c, 0, 0, 0)
DI bf16x8 rd128(LAS char* p) { return *(LAS bf16x8*)p; }
DI bf16x8 rdtr(LAS char* p, int step) {
  s16x4 a = __builtin_amdgcn_ds_read_tr16_b64_v4i16((LAS s16x4*)p), b = __builtin_amdgcn_ds_read_tr16_b64_v4i16((LAS s16x4*)(p + step));
  return __builtin_shufflevector(a, b, 0, 1, 2, 3, 4, 5, 6, 7);
}
DI unsigned pk(float a, float b) { f32x2 v = {a, b}; return __builtin_bit_cast(unsigned, __builtin_convertvector(v, bf16x2)); }
DI u32x4 pk8(const f32x16& x, int s) { return u32x4{pk(x[8 * s], x[8 * s + 1]), pk(x[8 * s + 2], x[8 * s + 3]), pk(x[8 * s + 4], x[8 * s + 5]), pk(x[8 * s + 6], x[8 * s + 7])}; }
DI float swap32max(float v) { auto r = __builtin_amdgcn_permlane32_swap(__float_as_uint(v), __float_as_uint(v), false, false); return fmaxf(__uint_as_float(r[0]), __uint_as_float(r[1])); }
DI float swap32add(float v) { auto r = __builtin_amdgcn_permlane32_swap(__float_as_uint(v), __float_as_uint(v), false, false); return __uint_as_float(r[0]) + __uint_as_float(r[1]); }
DI unsigned umax4(u32x4 v) { return max(max(v.x, v.y), max(v.z, v.w)); }
DI void acc8(float (&a)[8], u32x4 v) {
#pragma unroll
  for (int i = 0; i < 4; ++i) { a[2 * i] += __uint_as_float(v[i] << 16); a[2 * i + 1] += __uint_as_float(v[i] & 0xffff0000u); }
}

__global__ __launch_bounds__(512, 2) void gqa_fused(const char* __restrict__ x, const char* __restrict__ kc, const char* __restrict__ vc,
                                                    const char* __restrict__ wq, const char* __restrict__ wo, char* __restrict__ out, char* ws) {
  __shared__ __attribute__((aligned(256))) char smem[163840];
  LAS char* sm = (LAS char*)smem;
  const int w = blockIdx.x, wv = __builtin_amdgcn_readfirstlane(threadIdx.x >> 6), l = threadIdx.x & 63;
  const int xcd = w & 7, j = w >> 3, b = j >> 2, hh = j & 3, h = xcd * 4 + hh;
  ST(0);
  const rsrc_t rw = mk(ws, W_BYTES);
  if (wv < 4) {
    const int lw = wv;
    const rsrc_t rx = mk(x + b * 73728, 73728), rq = mk(wq + h * 256, 576 * 8192 - h * 256);
    const unsigned vx0 = (16 * lw + (l >> 3)) * 1152 + 16 * ((l & 7) ^ (l >> 4));
    const unsigned vx1 = (16 * lw + 8 + (l >> 3)) * 1152 + 16 * ((l & 7) ^ (4 + (l >> 4)));
    const unsigned sw4 = 16 * ((l & 15) ^ ((l >> 4) << 2));
    const unsigned vq = (16 * lw + (l >> 4)) * 8192 + sw4;
    int c = (b + 2 * hh) & 7;
    auto chunk = [&](int slot) {
      DMA(rx, slot * 24576 + lw * 2048, vx0, c * 128, 0);
      DMA(rx, slot * 24576 + lw * 2048 + 1024, vx1, c * 128, 0);
#pragma unroll
      for (int p = 0; p < 4; ++p) DMA(rq, slot * 24576 + 8192 + lw * 4096 + p * 1024, vq, (c * 64 + p * 4) * 8192, NT);
      c = c == 8 ? 0 : c + 1;
    };
    chunk(0);
    asm volatile("s_barrier" ::: "memory");
    const size_t kvo = (size_t)(b * 16 + (h >> 1)) * 131072;
    const rsrc_t rk = mk(kc + kvo, 131072), rv = mk(vc + kvo, 131072), ro = mk(wo + (size_t)xcd * 512 * 1152, 512 * 1152);
    const unsigned vk = (4 * lw + (l >> 4)) * 256 + 16 * ((l & 15) ^ (4 * lw + (l >> 4)));
    const unsigned vv = lw * 4096 + (l >> 4) * 256 + sw4;
    const unsigned vo = (16 * lw + (l >> 4)) * 1152 + hh * 256 + sw4;
    const unsigned vt = (128 * hh + 32 * lw + (l >> 3)) * 1152 + 1024 + 16 * ((l & 7) ^ (((l >> 4) & 1) << 2));
    asm volatile("s_barrier" ::: "memory");
    chunk(1);
    {
      int slot = 2;
#pragma unroll 1
      for (int i = 0; i < 7; ++i) { LBAR(6); chunk(slot); slot = slot == 2 ? 0 : slot + 1; }
    }
    auto unitK = [&](int u) {
#pragma unroll
      for (int p = 0; p < 4; ++p) DMA(rk, slot_addr(u) + lw * 1024 + p * 4096, vk, u * 16384 + p * 4096, NT);
    };
    auto unitV = [&](int u) {
#pragma unroll
      for (int p = 0; p < 4; ++p) DMA(rv, slot_addr(8 + u) + lw * 4096 + p * 1024, vv, u * 16384 + p * 1024, NT);
    };
    auto unitW = [&](int i) {
      const int row0 = (((hh + (i >> 1)) & 3) * 2 + (i & 1)) * 64;
#pragma unroll
      for (int p = 0; p < 4; ++p) DMA(ro, slot_addr(17 + i) + lw * 4096 + p * 1024, vo, (row0 + p * 4) * 1152, NT);
    };
    LBAR(6); unitK(0);
    LBAR(4); unitK(1); unitK(2);
    ST(1);
#pragma unroll
    for (int u = 0; u < 8; ++u) { LBAR(8); if (u < 5) unitK(u + 3); else unitV(u - 5); }
    ST(2);
    asm volatile("s_barrier" ::: "memory");
#pragma unroll
    for (int u = 0; u < 5; ++u) { LBAR(8); unitV(u + 3); }
    LBAR(8);
    {
      const unsigned i = lw * 36 + (l < 36 ? l : 35);
      __builtin_amdgcn_raw_ptr_buffer_load_lds(ro, (LAS void*)(sm + L_ST + 4096 + lw * 256), 4, (j * 144 + i) * 128, 0, 0, NT);
#pragma unroll
      for (int p = 0; p < 4; ++p) DMA(ro, slot_addr(16) + lw * 4096 + p * 1024, vt, p * 8 * 1152, NT);
    }
    LBAR(9); unitW(0);
    LBAR(9);
    ST(3);
    unitW(1); unitW(2); unitW(3); unitW(4);
    LBAR(16); unitW(5);
    LBAR(12); unitW(6); unitW(7);
    LBAR(12);
    u32x4 o[3][4];
    unsigned oo[3];
#pragma unroll
    for (int t = 0; t < 3; ++t) oo[t] = (unsigned)(((b * 4 + ((hh + 1 + t) & 3)) * 8 + xcd)) * SLAB + S_O;
    const unsigned vl = lw * 4096 + l * 16;
#define OLOAD(t) asm volatile("buffer_load_dwordx4 %0, %4, %5, %6 offen sc1\n\tbuffer_load_dwordx4 %1, %4, %5, %6 offen offset:1024 sc1\n\t" \
                              "buffer_load_dwordx4 %2, %4, %5, %6 offen offset:2048 sc1\n\tbuffer_load_dwordx4 %3, %4, %5, %6 offen offset:3072 sc1" \
                              : "=&v"(o[t][0]), "=&v"(o[t][1]), "=&v"(o[t][2]), "=&v"(o[t][3]) : "v"(vl), "s"(rw), "s"(oo[t]) : "memory")
#define OWAIT(t, N) asm volatile("s_waitcnt vmcnt(%4)" : "+v"(o[t][0]), "+v"(o[t][1]), "+v"(o[t][2]), "+v"(o[t][3]) : "i"(N) : "memory")
    OLOAD(0); OLOAD(1); OLOAD(2);
    LBAR(12);
    ST(4);
#pragma unroll
    for (int t = 0; t < 3; ++t) {
      OWAIT(t, 8 - 4 * t);
#pragma unroll 1
      for (unsigned spin = 0; spin < SPIN_CAP; ++spin) {
        const unsigned m = max(max(umax4(o[t][0]), umax4(o[t][1])), max(umax4(o[t][2]), umax4(o[t][3])));
        if (!__any(m == 0xffffffffu)) break;
        OLOAD(t); OWAIT(t, 0);
      }
#pragma unroll
      for (int p = 0; p < 4; ++p) *(LAS u32x4*)(sm + slot_addr(25 + t) + lw * 4096 + p * 1024 + l * 16) = o[t][p];
      CBAR();
    }
    ST(5);
    if (lw == 0) {
      const unsigned frag = ((hh >> 1) * 2 + (xcd >> 2)) * 2 + (hh & 1);
      const unsigned vr = (unsigned)((b * 4 + (l >> 4)) * 8) * SLAB + S_T + frag * 1024 + (32 * ((l >> 3) & 1) + 8 * (xcd & 3) + (l & 7)) * 16;
      u32x4 v[8];
#pragma unroll 1
      for (unsigned spin = 0; spin < SPIN_CAP; ++spin) {
        asm volatile("" ::: "memory");
        unsigned m = 0;
#pragma unroll
        for (int p = 0; p < 8; ++p) v[p] = __builtin_amdgcn_raw_buffer_load_b128(rw, vr, p * SLAB, SC1);
#pragma unroll
        for (int p = 0; p < 8; ++p) m = max(m, umax4(v[p]));
        if (!__any(m == 0xffffffffu)) break;
      }
      float a[8] = {};
#pragma unroll
      for (int p = 0; p < 8; ++p) acc8(a, v[p]);
      float e[4], f[2];
#pragma unroll
      for (int i = 0; i < 4; ++i) { auto r = __builtin_amdgcn_permlane16_swap(__float_as_uint(a[i]), __float_as_uint(a[4 + i]), false, false); e[i] = __uint_as_float(r[0]) + __uint_as_float(r[1]); }
#pragma unroll
      for (int i = 0; i < 2; ++i) { auto r = __builtin_amdgcn_permlane32_swap(__float_as_uint(e[i]), __float_as_uint(e[2 + i]), false, false); f[i] = __uint_as_float(r[0]) + __uint_as_float(r[1]); }
      const int n = 512 + 16 * hh + 8 * ((l >> 4) & 1) + 4 * ((l >> 3) & 1) + 2 * (l >> 5);
      *(unsigned*)(out + (size_t)(b * 64 + 8 * xcd + (l & 7)) * 1152 + n * 2) = pk(f[0], f[1]);
    }
    ST(6);
    return;
  }
  const int cw = wv - 4, r = l & 31, hf = l >> 5, kt = cw >> 1, qt = cw & 1;
  const unsigned slab = (unsigned)w * SLAB;
  const unsigned oob = cw == 0 ? 0u : 0x40000000u;
  asm volatile("s_barrier" ::: "memory");
  u32x2 ep = __builtin_amdgcn_raw_buffer_load_b64(rw, oob, W_EPO + b * 256, SC1);
  {
    const u32x4 s = {~0u, ~0u, ~0u, ~0u};
#pragma unroll
    for (int p = 0; p < 10; ++p) __builtin_amdgcn_raw_buffer_store_b128(s, rw, slab + cw * 10240 + p * 1024 + l * 16, 0, SC1);
  }
  asm volatile("s_barrier" ::: "memory");
  const int qq = (l & 15) >> 2, pp = l & 3, blk = (l >> 4) & 1;
  const int trc = 16 * ((4 * cw + 2 * blk + (pp >> 1)) ^ (qq << 2)) + 8 * (pp & 1);
  f32x16 qa0 = {}, qa1 = {};
  {
    LAS char* aw = sm + 8192 + (8 * hf + qq) * 256 + trc;
    const int ax = r * 128 + 16 * (hf ^ ((r >> 1) & 7));
#pragma unroll 1
    for (int i3 = 0; i3 < 3; ++i3) {
#pragma unroll
      for (int sl = 0; sl < 3; ++sl) {
        CBAR();
#pragma unroll
        for (int s = 0; s < 4; ++s) {
          const bf16x8 a = rdtr(aw + sl * 24576 + s * 4096, 1024);
          const bf16x8 b0 = rd128(sm + sl * 24576 + (ax ^ (32 * s))), b1 = rd128(sm + sl * 24576 + 4096 + (ax ^ (32 * s)));
          qa0 = MFMA(a, b0, qa0); qa1 = MFMA(a, b1, qa1);
        }
      }
    }
#pragma unroll
    for (int s = 0; s < 2; ++s) {
      const u32x4 p0 = pk8(qa0, s), p1 = pk8(qa1, s);
      LAS char* q = sm + L_Q + (2 * cw + s) * 2048 + r * 16 + 8 * hf;
      *(LAS u32x2*)q = u32x2{p0.x, p0.y}; *(LAS u32x2*)(q + 512) = u32x2{p0.z, p0.w};
      *(LAS u32x2*)(q + 1024) = u32x2{p1.x, p1.y}; *(LAS u32x2*)(q + 1536) = u32x2{p1.z, p1.w};
    }
  }
  asm volatile("s_waitcnt vmcnt(0)" : "+v"(ep) :: "memory");
  ep.x += 1;
  ST(1);
  f32x16 S[8];
  float mx = -__builtin_inff();
  u32x2 fl = {};
  {
    bf16x8 qf[8];
    const int ak = (32 * kt + r) * 256 + 16 * (hf ^ (r & 15));
#pragma unroll
    for (int u = 0; u < 8; ++u) {
      CBAR();
      if (u == 0) {
        __builtin_amdgcn_raw_buffer_store_b64(ep, rw, l == 0 ? oob : 0x40000000u, W_FLAG + w * 256, SC1);
#pragma unroll
        for (int s = 0; s < 8; ++s) qf[s] = rd128(sm + L_Q + s * 2048 + qt * 1024 + l * 16);
      }
      if (u == 3) fl = __builtin_amdgcn_raw_buffer_load_b64(rw, (unsigned)(b * 32 + r) * 256 + oob, W_FLAG, SC1);
      f32x16 a = {};
#pragma unroll
      for (int s = 0; s < 8; ++s) a = MFMA(rd128(sm + slot_addr(u) + (ak ^ (32 * s))), qf[s], a);
      S[u] = a;
#pragma unroll
      for (int i = 0; i < 16; ++i) mx = fmaxf(mx, a[i]);
      if (u == 6 && cw == 0) {
#pragma unroll 1
        for (unsigned spin = 0; spin < SPIN_CAP; ++spin) {
          if (__all(fl.x == ep.x && fl.y == ep.y)) break;
          asm volatile("" ::: "memory");
          fl = __builtin_amdgcn_raw_buffer_load_b64(rw, (unsigned)(b * 32 + r) * 256, W_FLAG, SC1);
        }
        if ((w & 31) == 0 && l == 0) __builtin_amdgcn_raw_buffer_store_b64(ep, rw, 0, W_EPO + b * 256, SC1);
      }
    }
  }
  LAS float* stm = (LAS float*)(sm + L_ST);
  mx = swap32max(mx);
  stm[cw * 32 + r] = mx;
  CBAR();
  ST(2);
  constexpr float CS = 0.08838834764831845f * 1.4426950408889634f;
  const float nm = -fmaxf(mx, stm[(cw ^ 2) * 32 + r]) * CS;
  float l0 = 0.f, l1 = 0.f;
  auto softmax = [&](int u) {
    f32x16 p;
#pragma unroll
    for (int i = 0; i < 16; ++i) p[i] = __builtin_amdgcn_exp2f(__builtin_fmaf(S[u][i], CS, nm));
#pragma unroll
    for (int i = 0; i < 16; i += 2) { l0 += p[i]; l1 += p[i + 1]; }
    LAS char* d = sm + L_P + (u & 1) * 8192 + kt * 4096 + qt * 1024 + l * 16;
    *(LAS u32x4*)d = pk8(p, 0); *(LAS u32x4*)(d + 2048) = pk8(p, 1);
  };
  softmax(0);
  f32x16 o0 = {}, o1 = {};
  const int av = (4 * hf + qq) * 256 + trc;
  float ri0 = 0.f, ri1 = 0.f;
#pragma unroll
  for (int u = 0; u < 8; ++u) {
    CBAR();
    if (u == 7) { ri0 = __builtin_amdgcn_rcpf(stm[128 + r] + stm[192 + r]); ri1 = __builtin_amdgcn_rcpf(stm[160 + r] + stm[224 + r]); }
#pragma unroll
    for (int ks = 0; ks < 4; ++ks) {
      const bf16x8 a = rdtr(sm + slot_addr(8 + u) + av + ks * 4096, 2048);
      LAS char* p = sm + L_P + (u & 1) * 8192 + ks * 2048 + l * 16;
      o0 = MFMA(a, rd128(p), o0); o1 = MFMA(a, rd128(p + 1024), o1);
    }
    if (u < 7) softmax(u + 1);
    if (u == 6) stm[128 + cw * 32 + r] = swap32add(l0 + l1);
  }
  o0 *= ri0; o1 *= ri1;
#pragma unroll
  for (int s = 0; s < 2; ++s) {
    const u32x4 p0 = pk8(o0, s), p1 = pk8(o1, s);
    const int off = (2 * cw + s) * 2048 + l * 16;
    __builtin_amdgcn_raw_buffer_store_b128(p0, rw, slab + S_O + off, 0, SC1);
    __builtin_amdgcn_raw_buffer_store_b128(p1, rw, slab + S_O + off + 1024, 0, SC1);
    *(LAS u32x4*)(sm + L_Q + off) = p0; *(LAS u32x4*)(sm + L_Q + off + 1024) = p1;
  }
  ST(3);
  bf16x8 wf[32];
  auto loadW = [&](int i) {
#pragma unroll
    for (int ks = 0; ks < 4; ++ks) wf[4 * i + ks] = rdtr(sm + slot_addr(17 + i) + av + ks * 4096, 2048);
  };
  f32x16 c0 = {}, c1 = {};
  auto head = [&](int t, int base) {
#pragma unroll
    for (int ks = 0; ks < 8; ++ks) { LAS char* p = sm + base + ks * 2048 + l * 16; c0 = MFMA(wf[8 * t + ks], rd128(p), c0); c1 = MFMA(wf[8 * t + ks], rd128(p + 1024), c1); }
  };
  CBAR();
  {
    f32x16 t = {};
    const int at = (4 * hf + qq) * 128 + 16 * ((4 * kt + 2 * blk + (pp >> 1)) ^ ((qq >> 1) << 2)) + 8 * (pp & 1);
#pragma unroll
    for (int ks = 0; ks < 8; ++ks) t = MFMA(rdtr(sm + slot_addr(16) + at + ks * 2048, 1024), rd128(sm + L_Q + ks * 2048 + qt * 1024 + l * 16), t);
#pragma unroll
    for (int s = 0; s < 2; ++s) __builtin_amdgcn_raw_buffer_store_b128(pk8(t, s), rw, slab + S_T + (2 * cw + s) * 1024 + l * 16, 0, SC1);
    loadW(0);
  }
  CBAR();
  loadW(1); loadW(2);
  head(0, L_Q);
  CBAR();
  loadW(3); loadW(4);
  CBAR();
  loadW(5); loadW(6); loadW(7);
#pragma unroll
  for (int t = 0; t < 3; ++t) { CBAR(); head(t + 1, slot_addr(25 + t)); }
  ST(4);
#pragma unroll
  for (int s = 0; s < 2; ++s) {
    __builtin_amdgcn_raw_buffer_store_b128(pk8(c0, s), rw, slab + S_M + (4 * cw + s) * 1024 + l * 16, 0, SC1);
    __builtin_amdgcn_raw_buffer_store_b128(pk8(c1, s), rw, slab + S_M + (4 * cw + 2 + s) * 1024 + l * 16, 0, SC1);
  }
  asm volatile("s_waitcnt vmcnt(0)" ::: "memory");
  ST(5);
  {
    const unsigned vr = (unsigned)(j * 8 + 4 * blk) * SLAB + S_M + ((cw * 2 + (xcd >> 2)) * 2 + ((l >> 3) & 1)) * 1024 + (32 * hf + 8 * (xcd & 3) + (l & 7)) * 16;
    u32x4 v[4];
#pragma unroll 1
    for (unsigned spin = 0; spin < SPIN_CAP; ++spin) {
      asm volatile("" ::: "memory");
#pragma unroll
      for (int p = 0; p < 4; ++p) v[p] = __builtin_amdgcn_raw_buffer_load_b128(rw, vr, p * SLAB, SC1);
      const unsigned m = max(max(umax4(v[0]), umax4(v[1])), max(umax4(v[2]), umax4(v[3])));
      if (!__any(m == 0xffffffffu)) break;
    }
    float a[8] = {};
#pragma unroll
    for (int p = 0; p < 4; ++p) acc8(a, v[p]);
    float e[4];
#pragma unroll
    for (int i = 0; i < 4; ++i) { auto rr = __builtin_amdgcn_permlane16_swap(__float_as_uint(a[i]), __float_as_uint(a[4 + i]), false, false); e[i] = __uint_as_float(rr[0]) + __uint_as_float(rr[1]); }
    const int n = 128 * hh + 32 * cw + 16 * ((l >> 3) & 1) + 8 * blk + 4 * hf;
    *(u32x2*)(out + (size_t)(b * 64 + 8 * xcd + (l & 7)) * 1152 + n * 2) = u32x2{pk(e[0], e[1]), pk(e[2], e[3])};
  }
  ST(6);
}

extern "C" void kernel_launch(void* const* d_in, const int* in_sizes, int n_in, void* d_out, int out_size, void* d_ws, size_t ws_size, hipStream_t stream) {
  gqa_fused<<<256, 512, 0, stream>>>((const char*)d_in[0], (const char*)d_in[1], (const char*)d_in[2], (const char*)d_in[3], (const char*)d_in[4], (char*)d_out, (char*)d_ws);
}
```

```cpp
#include <hip/hip_runtime.h>
#include <hip/hip_bf16.h>

typedef unsigned long long u64;
typedef short bf16x8 __attribute__((ext_vector_type(8)));
typedef short s16x4 __attribute__((ext_vector_type(4)));
typedef float f32x16 __attribute__((ext_vector_type(16)));
typedef float f32x2 __attribute__((ext_vector_type(2)));
typedef __bf16 bf16x2 __attribute__((ext_vector_type(2)));
typedef unsigned u32x4 __attribute__((ext_vector_type(4)));
typedef unsigned u32x2 __attribute__((ext_vector_type(2)));
#define LAS __attribute__((address_space(3)))
#define CAS __attribute__((address_space(4)))
#define DI __device__ __forceinline__
typedef __amdgpu_buffer_rsrc_t rsrc_t;

constexpr int L_P = 65536, L_Q = 81920;
constexpr int slot_addr(int g) { return g == 24 ? L_P : (g & 7) < 4 ? 98304 + (g & 7) * 16384 : ((g & 7) - 4) * 16384; }
constexpr unsigned S_O = 0, S_OC = 16384, S_T = 17408, S_TC = 25600, S_M = 27648, S_MC = 44032, S_H = 46080, SLAB = 46592;
constexpr unsigned W_EPO = 12u << 20, W_BYTES = W_EPO + 4096;
constexpr int SC1 = 16, NT = 2;
constexpr unsigned SPIN_CAP = 1u << 20;
#define TOUCH_AT 104
#define TOUCH_POL 2
#define W_POL 2
#define RETOUCH 7
#define RE_POL 16
#define T1_AT 3
#define H2_DELAY 8
#define P2_DELAY 20
#define PREF 1
#define CODE_LINES 152

#define DG(i) do {} while (0)
#define ST(i) do {} while (0)

DI rsrc_t mk(const void* p, unsigned n) { return __builtin_amdgcn_make_buffer_rsrc((void*)p, 0, n, 0x00020000); }
#define DMA(R, LDSOFF, V, SOFF, IMM, POL) __builtin_amdgcn_raw_ptr_buffer_load_lds(R, (LAS void*)(sm + (LDSOFF)), 16, V, SOFF, IMM, POL)
#define FENCE() __builtin_amdgcn_sched_barrier(0)
#define LTS() do {} while (0)
#define LBAR(N) do { asm volatile("s_waitcnt vmcnt(%0)\n\ts_barrier" :: "i"(N) : "memory"); LTS(); } while (0)
#define CBAR() CBAR0()
#define CBAR0() do { FENCE(); asm volatile("" ::: "memory"); __builtin_amdgcn_s_waitcnt(0xC07F); __builtin_amdgcn_s_barrier(); asm volatile("" ::: "memory"); FENCE(); } while (0)
#define PIN(x) asm volatile("" : "+v"(x))
DI LAS char* opq(LAS char* p) { asm("" : "+v"(p)); return p; }
#define MFMA(a, b, c) __builtin_amdgcn_mfma_f32_32x32x16_bf16(a, b, c, 0, 0, 0)
#define MF(acc, a, b) do { FENCE(); acc = MFMA(a, b, acc); PIN(acc); FENCE(); } while (0)
#define UNROLL _Pragma("unroll")
DI bf16x8 rd128(LAS char* p) { return *(LAS bf16x8*)p; }
DI bf16x8 rdtr(LAS char* p, int step) {
  s16x4 a = __builtin_amdgcn_ds_read_tr16_b64_v4i16((LAS s16x4*)p), b = __builtin_amdgcn_ds_read_tr16_b64_v4i16((LAS s16x4*)(p + step));
  return __builtin_shufflevector(a, b, 0, 1, 2, 3, 4, 5, 6, 7);
}
DI unsigned pk(float a, float b) { f32x2 v = {a, b}; return __builtin_bit_cast(unsigned, __builtin_convertvector(v, bf16x2)); }
DI u32x4 pk8(const f32x16& x, int s) { return u32x4{pk(x[8 * s], x[8 * s + 1]), pk(x[8 * s + 2], x[8 * s + 3]), pk(x[8 * s + 4], x[8 * s + 5]), pk(x[8 * s + 6], x[8 * s + 7])}; }
DI float max2(float a, float b) { return __builtin_elementwise_maximum(a, b); }
DI float max3(float a, float b, float c) { return max2(max2(a, b), c); }
DI float min2(float a, float b) { return __builtin_elementwise_minimum(a, b); }
DI float min3(float a, float b, float c) { return min2(min2(a, b), c); }
DI float swap32min(float v) { auto r = __builtin_amdgcn_permlane32_swap(__float_as_uint(v), __float_as_uint(v), false, false); return min2(__uint_as_float(r[0]), __uint_as_float(r[1])); }
DI float swap32max(float v) { auto r = __builtin_amdgcn_permlane32_swap(__float_as_uint(v), __float_as_uint(v), false, false); return max2(__uint_as_float(r[0]), __uint_as_float(r[1])); }
DI float sum16(float a, float b) { auto r = __builtin_amdgcn_permlane16_swap(__float_as_uint(a), __float_as_uint(b), false, false); return __uint_as_float(r[0]) + __uint_as_float(r[1]); }
DI float sum32(float a, float b) { auto r = __builtin_amdgcn_permlane32_swap(__float_as_uint(a), __float_as_uint(b), false, false); return __uint_as_float(r[0]) + __uint_as_float(r[1]); }
DI unsigned csum(unsigned c, u32x4 v) { return (c + v.x + v.y) + v.z + v.w; }
DI float blo(unsigned v) { return __uint_as_float(v << 16); }
DI float bhi(unsigned v) { return __uint_as_float(v & 0xffff0000u); }
DI void st128(u32x4 v, rsrc_t r, unsigned off) { __builtin_amdgcn_raw_buffer_store_b128(v, r, off, 0, SC1); }
DI void st32(unsigned v, rsrc_t r, unsigned off) { __builtin_amdgcn_raw_buffer_store_b32(v, r, off, 0, SC1); }

__global__ __launch_bounds__(512, 2) void gqa_fused(const char* __restrict__ x, const char* __restrict__ kc, const char* __restrict__ vc,
                                                    const char* __restrict__ wq, const char* __restrict__ wo, char* __restrict__ out, char* ws) {
  __shared__ __attribute__((aligned(256))) char smem[163840];
  LAS char* sm = (LAS char*)smem;
  u64 pc;
  asm volatile("s_getpc_b64 %0" : "=s"(pc));
  const int w = blockIdx.x, wv = __builtin_amdgcn_readfirstlane(threadIdx.x >> 6), l = threadIdx.x & 63;
  const int xcd = w & 7, j = w >> 3, b = j >> 2, hh = j & 3, h = xcd * 4 + hh;
  ST(0);
  asm volatile("" :: "s"(x), "s"(kc), "s"(vc), "s"(wq), "s"(wo), "s"(out), "s"(ws));
  const rsrc_t rw = mk(ws, W_BYTES);
  const size_t kvo = (size_t)(b * 16 + (h >> 1)) * 131072;
  if (__builtin_expect(wv < 4, 1)) {
    const int lw = wv;
    LTS();
    const rsrc_t rx = mk(x + b * 73728, 73728), rq = mk(wq + h * 256, 576 * 8192 - h * 256);
    const unsigned vx0 = (16 * lw + (l >> 3)) * 1152 + 16 * ((l & 7) ^ (l >> 4));
    const unsigned vx1 = (16 * lw + 8 + (l >> 3)) * 1152 + 16 * ((l & 7) ^ (4 + (l >> 4)));
    const unsigned sw4 = 16 * ((l & 15) ^ ((l >> 4) << 2));
    const unsigned vq = (16 * lw + (l >> 4)) * 8192 + sw4;
    {
      int c = (b + 2 * hh) & 7, so = 0;
#pragma unroll 1
      for (int i = 0; i < 9; ++i) {
        if (i >= 2) LBAR(6);
        DMA(rx, so + lw * 2048, vx0, c * 128, 0, 0);
        DMA(rx, so + lw * 2048 + 1024, vx1, c * 128, 0, 0);
        UNROLL for (int p = 0; p < 4; ++p) DMA(rq, so + 8192 + lw * 4096 + p * 1024, vq, (c * 64 + p * 4) * 8192, 0, NT);
        if (i == TOUCH_AT - 100) __builtin_amdgcn_raw_ptr_buffer_load_lds(mk(wo + (size_t)xcd * 512 * 1152, 512 * 1152), (LAS void*)(sm + 74752 + lw * 256), 4, (j * 144 + lw * 36 + (l < 36 ? l : 35)) * 128, 0, 0, TOUCH_POL);
        if (i == RETOUCH) __builtin_amdgcn_raw_ptr_buffer_load_lds(mk(wo + (size_t)xcd * 512 * 1152, 512 * 1152), (LAS void*)(sm + 74752 + lw * 256), 4, (j * 144 + lw * 36 + (l < 36 ? l : 35)) * 128, 0, 0, RE_POL);
        c = c == 8 ? 0 : c + 1;
        so = so == 49152 ? 0 : so + 24576;
      }
    }
    const unsigned tag = *(const CAS unsigned*)(ws + W_EPO + b * 256) + 0x9E3779B9u;
    const rsrc_t rk = mk(kc + kvo, 131072), rv = mk(vc + kvo, 131072), ro = mk(wo + (size_t)xcd * 512 * 1152, 512 * 1152);
    const unsigned vk = (4 * lw + (l >> 4)) * 256 + 16 * ((l & 15) ^ (4 * lw + (l >> 4)));
    const unsigned vv = lw * 4096 + (l >> 4) * 256 + sw4;
    const unsigned vo = (16 * lw + (l >> 4)) * 1152 + hh * 256 + sw4;
    const unsigned vt = (128 * hh + 32 * lw + (l >> 3)) * 1152 + 1024 + 16 * ((l & 7) ^ (((l >> 4) & 1) << 2));
    auto rslot = [](int g) { return (g & 4) ? (g & 3) * 16384 : 98304 + (g & 3) * 16384; };
    auto unitK = [&](int u) { const int s = rslot(u) + lw * 1024; UNROLL for (int p = 0; p < 4; ++p) DMA(rk, s + p * 4096, vk, u * 16384 + p * 4096, 0, NT); };
    auto unitV = [&](int u) { const int s = rslot(u) + lw * 4096;
      DMA(rv, s, vv, u * 16384, 0, NT); DMA(rv, s, vv, u * 16384, 1024, NT); DMA(rv, s, vv, u * 16384, 2048, NT); DMA(rv, s, vv, u * 16384, 3072, NT); };
    auto unitW = [&](int i, int s) {
      const int row0 = ((2 * hh + i) & 7) * 64;
      UNROLL for (int p = 0; p < 4; ++p) DMA(ro, s + lw * 4096 + p * 1024, vo, (row0 + p * 4) * 1152, 0, W_POL);
    };
    LBAR(6); unitK(0);
    LBAR(4); unitK(1); unitK(2); unitK(3);
    asm volatile("" :: "s"(tag));
    ST(1);
#pragma unroll 1
    for (int u = 4; u < 8; ++u) { LBAR(12); unitK(u); }
    LBAR(12);
#pragma unroll 1
    for (int u = 0; u < 8; ++u) {
      if (u < 3) LBAR(8); else if (u > 4) LBAR(16);
      unitV(u);
      if (u == 3) { ST(2); asm volatile("s_barrier" ::: "memory"); }
    }
    LBAR(16);
    LBAR(12);
    LBAR(8);
    UNROLL for (int p = 0; p < 4; ++p) DMA(ro, slot_addr(16) + lw * 4096 + p * 1024, vt, p * 8 * 1152, 0, W_POL);
    LBAR(8); unitW(0, slot_addr(17));
    LBAR(8);
    ST(3);
    u32x4 o[3][4]; unsigned oc[3], oo[3];
    UNROLL for (int t = 0; t < 3; ++t) oo[t] = (unsigned)(((b * 4 + ((hh + 1 + t) & 3)) * 8 + xcd)) * SLAB;
    const unsigned vl = S_O + lw * 4096 + l * 16, vc2 = S_OC + lw * 256 + l * 4;
#define OLOAD(t) asm volatile("buffer_load_dwordx4 %0, %5, %7, %8 offen sc1\n\tbuffer_load_dwordx4 %1, %5, %7, %8 offen offset:1024 sc1\n\t" \
                              "buffer_load_dwordx4 %2, %5, %7, %8 offen offset:2048 sc1\n\tbuffer_load_dwordx4 %3, %5, %7, %8 offen offset:3072 sc1\n\t" \
                              "buffer_load_dword %4, %6, %7, %8 offen sc1" \
                              : "=&v"(o[t][0]), "=&v"(o[t][1]), "=&v"(o[t][2]), "=&v"(o[t][3]), "=&v"(oc[t]) : "v"(vl), "v"(vc2), "s"(rw), "s"(oo[t]) : "memory")
#define OWAIT(t, N) asm volatile("s_waitcnt vmcnt(%5)" : "+v"(o[t][0]), "+v"(o[t][1]), "+v"(o[t][2]), "+v"(o[t][3]), "+v"(oc[t]) : "i"(N) : "memory")
#define OPUT(t, N) do { OWAIT(t, N); \
      _Pragma("unroll 1") for (unsigned spin = 0; spin < SPIN_CAP; ++spin) { \
        if (__all(csum(csum(csum(csum(tag, o[t][0]), o[t][1]), o[t][2]), o[t][3]) == oc[t])) break; \
        DG(3); OLOAD(t); OWAIT(t, 0); } \
      UNROLL for (int p = 0; p < 4; ++p) asm volatile("ds_write_b128 %0, %1 offset:%2" :: "v"(od), "v"(o[t][p]), "i"(t * 16384 + p * 1024) : "memory"); } while (0)
    LAS char* od = sm + slot_addr(16) + lw * 4096 + l * 16;
    const char* hp[3];
    UNROLL for (int t = 0; t < 3; ++t) hp[t] = ws + oo[t] + S_H;
    unsigned h0, h1, h2;
#define HLOAD() asm volatile("s_load_dword %0, %3, 0x0 glc\n\ts_load_dword %1, %4, 0x0 glc\n\ts_load_dword %2, %5, 0x0 glc" : "=&s"(h0), "=&s"(h1), "=&s"(h2) : "s"(hp[0]), "s"(hp[1]), "s"(hp[2]) : "memory")
#pragma unroll 1
    for (int i = 1; i < 6; ++i) {
      unitW(i, rslot(i + 1));
      if (i == T1_AT) LBAR(4 * (T1_AT - 1));
    }
    if (lw == 0) {
      HLOAD();
      unitW(6, rslot(7)); unitW(7, L_P);
#pragma unroll 1
      for (unsigned spin = 0; spin < 4096; ++spin) {
        asm volatile("s_waitcnt lgkmcnt(0)" : "+s"(h0), "+s"(h1), "+s"(h2) :: "memory");
        DG(2);
        if (h0 == tag && h1 == tag && h2 == tag) break;
        HLOAD();
      }
      asm volatile("s_waitcnt lgkmcnt(0)" : "+s"(h0), "+s"(h1), "+s"(h2) :: "memory");
    } else { unitW(6, rslot(7)); unitW(7, L_P); }
    LBAR(8);
    ST(4);
    OLOAD(0); OLOAD(1); OLOAD(2);
    if (lw == 2) asm volatile("ds_write_b32 %0, %1" :: "v"(L_Q), "v"(0) : "memory");
    OPUT(0, 10);
    asm volatile("s_waitcnt lgkmcnt(0)\n\ts_barrier" ::: "memory"); LTS();
    OPUT(1, 5);
    asm volatile("s_waitcnt lgkmcnt(0)\n\ts_barrier" ::: "memory"); LTS();
    OPUT(2, 0);
    asm volatile("s_waitcnt lgkmcnt(0)\n\ts_barrier" ::: "memory"); LTS();
    if (lw == 1) { __builtin_amdgcn_s_sleep(H2_DELAY); if (l < 32) st32(tag, rw, (unsigned)w * SLAB + S_H + 256 + l * 4); }
    if (lw == 2) {
      __builtin_amdgcn_s_sleep(P2_DELAY);
#pragma unroll 1
      for (unsigned spin = 0; spin < 4096; ++spin) {
        asm volatile("" ::: "memory");
        if (__all(__builtin_amdgcn_raw_buffer_load_b32(rw, (unsigned)(j * 8 + (l & 7)) * SLAB + S_H + 256, 0, SC1) == tag)) break;
      }
      asm volatile("ds_write_b32 %0, %1" :: "v"(L_Q), "v"(1) : "memory");
    }
    ST(5);
    if (lw == 0) {
      const unsigned frag = ((hh >> 1) * 2 + (xcd >> 2)) * 2 + (hh & 1), pl = 32 * ((l >> 3) & 1) + 8 * (xcd & 3) + (l & 7);
      const unsigned pb = (unsigned)((b * 4 + (l >> 4)) * 8) * SLAB;
      u32x4 v[8];
#pragma unroll 1
      for (unsigned spin = 0; spin < 4096; ++spin) {
        asm volatile("" ::: "memory");
        if (__all(__builtin_amdgcn_raw_buffer_load_b32(rw, (unsigned)(b * 32 + (l & 31)) * SLAB + S_H + 128, 0, SC1) == tag)) break;
        DG(0); __builtin_amdgcn_s_sleep(2);
      }
#pragma unroll 1
      for (unsigned spin = 0; spin < SPIN_CAP; ++spin) {
        asm volatile("" ::: "memory");
        unsigned c[8]; bool ok = true; DG(1);
        UNROLL for (int p = 0; p < 8; ++p) { v[p] = __builtin_amdgcn_raw_buffer_load_b128(rw, pb + S_T + frag * 1024 + pl * 16, p * SLAB, SC1);
                                             c[p] = __builtin_amdgcn_raw_buffer_load_b32(rw, pb + S_TC + frag * 256 + (xcd & 3) * 64 + (l & 15) * 4, p * SLAB, SC1); }
        UNROLL for (int p = 0; p < 8; ++p) ok &= csum(tag, v[p]) == c[p];
        if (__all(ok)) break;
      }
      if ((w & 31) == 0 && l == 0) st32(tag, rw, W_EPO + b * 256);
      float a[8] = {};
      UNROLL for (int p = 0; p < 8; ++p) UNROLL for (int i = 0; i < 4; ++i) { a[2 * i] += blo(v[p][i]); a[2 * i + 1] += bhi(v[p][i]); }
      float e[4];
      UNROLL for (int i = 0; i < 4; ++i) e[i] = sum16(a[i], a[4 + i]);
      const int n = 512 + 16 * hh + 8 * ((l >> 4) & 1) + 4 * ((l >> 3) & 1) + 2 * (l >> 5);
      *(unsigned*)(out + (size_t)(b * 64 + 8 * xcd + (l & 7)) * 1152 + n * 2) = pk(sum32(e[0], e[2]), sum32(e[1], e[3]));
    }
    ST(6);
    return;
  }
  const int cw = wv - 4, r = l & 31, hf = l >> 5, kt = cw >> 1, qt = cw & 1;
  const unsigned slab = (unsigned)w * SLAB;
  const unsigned hoff = l < 32 && cw == 0 ? slab + S_H + l * 4 : 0x40000000u;
  if (PREF && cw == 0) {
    const char* p = (const char*)(pc & ~127ull) + ((j & 7) * (CODE_LINES / 8) + (l < CODE_LINES / 8 ? l : 0)) * 128;
    if (PREF & 2) {
      if (l == 32) p = kc + kvo;
      if (l == 33) p = vc + kvo;
      if (l == 34) p = wo + (size_t)xcd * 512 * 1152;
      if (l == 35) p = wo + (size_t)xcd * 512 * 1152 + 511 * 1152;
      if (l == 36) p = ws + b * 32 * SLAB;
      if (l == 37) p = ws + (b * 32 + 31) * SLAB + S_MC;
      if (l == 38) p = out + (size_t)b * 73728;
    }
    asm volatile("s_mov_b32 m0, %1\n\ts_nop 0\n\tglobal_load_lds_dword %0, off" :: "v"(p), "i"(73728) : "memory");
  }
  const unsigned tag = *(const CAS unsigned*)(ws + W_EPO + b * 256) + 0x9E3779B9u;
  const int qq = (l & 15) >> 2, pp = l & 3, blk = (l >> 4) & 1;
  const int trc = 16 * ((4 * cw + 2 * blk + (pp >> 1)) ^ (qq << 2)) + 8 * (pp & 1);
  asm volatile("" :: "s"(tag));
  {
    f32x16 qa0 = {}, qa1 = {};
    LAS char* aw = opq(sm + 8192 + (8 * hf + qq) * 256 + trc);
    LAS char* ax[4];
    UNROLL for (int s = 0; s < 4; ++s) ax[s] = sm + ((r * 128 + 16 * (hf ^ ((r >> 1) & 7))) ^ (32 * s));
    bf16x8 fa[2][4], f0[2][4], f1[2][4];
    UNROLL for (int i = 0; i < 9; ++i) {
      CBAR();
      const int so = (i % 3) * 24576, cur = i & 1, prv = cur ^ 1;
      UNROLL for (int s = 0; s < 4; ++s) {
        if (i > 0) { if (s & 1) qa1 = MFMA(fa[prv][s >> 1], f1[prv][s >> 1], qa1); else qa0 = MFMA(fa[prv][s >> 1], f0[prv][s >> 1], qa0); }
        FENCE();
        fa[cur][s] = rdtr(aw + so + s * 4096, 1024); f0[cur][s] = rd128(ax[s] + so); f1[cur][s] = rd128(ax[s] + so + 4096);
        FENCE();
      }
      if (i > 0) { UNROLL for (int s = 2; s < 4; ++s) { qa0 = MFMA(fa[prv][s], f0[prv][s], qa0); qa1 = MFMA(fa[prv][s], f1[prv][s], qa1); } }
    }
    FENCE();
    UNROLL for (int s = 0; s < 4; ++s) qa0 = MFMA(fa[0][s], f0[0][s], qa0);
    FENCE();
    UNROLL for (int s = 0; s < 4; ++s) qa1 = MFMA(fa[0][s], f1[0][s], qa1);
    FENCE();
    LAS char* q = opq(sm + L_Q + cw * 4096 + r * 16 + 8 * hf);
    UNROLL for (int s = 0; s < 2; ++s) { const u32x4 p0 = pk8(qa0, s); *(LAS u32x2*)(q + s * 2048) = u32x2{p0.x, p0.y}; *(LAS u32x2*)(q + s * 2048 + 512) = u32x2{p0.z, p0.w}; }
    FENCE();
    UNROLL for (int s = 0; s < 2; ++s) { const u32x4 p1 = pk8(qa1, s); *(LAS u32x2*)(q + s * 2048 + 1024) = u32x2{p1.x, p1.y}; *(LAS u32x2*)(q + s * 2048 + 1536) = u32x2{p1.z, p1.w}; }
    asm volatile("s_waitcnt vmcnt(0)" ::: "memory");
  }
  ST(1);
  constexpr float CS = 0.08838834764831845f * 1.4426950408889634f;
  f32x16 S[8];
  float mu[8], su[8];
  LAS char* pr = opq(sm + L_P + l * 16);
  LAS char* pq = opq(sm + L_Q + qt * 1024 + l * 16);
#define SM_MAX(u) do { float a_ = max3(S[u][0], S[u][1], S[u][2]), b_ = max3(S[u][3], S[u][4], S[u][5]); \
    a_ = max3(a_, S[u][6], S[u][7]); b_ = max3(b_, S[u][8], S[u][9]); a_ = max3(a_, S[u][10], S[u][11]); b_ = max3(b_, S[u][12], S[u][13]); \
    a_ = max3(a_, S[u][14], S[u][15]); mu[u] = -CS * max2(a_, b_); PIN(mu[u]); } while (0)
#define SM_F(u) do { UNROLL for (int i = 0; i < 16; i += 2) { f32x2 t_ = {S[u][i], S[u][i + 1]}; t_ = __builtin_elementwise_fma(t_, f32x2{CS, CS}, f32x2{mu[u], mu[u]}); PIN(t_); S[u][i] = t_.x; S[u][i + 1] = t_.y; } } while (0)
#define SM_E(u, i) do { f32x2 t_ = {__builtin_amdgcn_exp2f(S[u][i]), __builtin_amdgcn_exp2f(S[u][i + 1])}; PIN(t_); S[u][i] = t_.x; S[u][i + 1] = t_.y; } while (0)
#define SM_A(u) do { f32x2 a_ = f32x2{S[u][0], S[u][1]} + f32x2{S[u][2], S[u][3]}, b_ = f32x2{S[u][4], S[u][5]} + f32x2{S[u][6], S[u][7]}; \
    a_ += f32x2{S[u][8], S[u][9]}; b_ += f32x2{S[u][10], S[u][11]}; a_ += f32x2{S[u][12], S[u][13]}; b_ += f32x2{S[u][14], S[u][15]}; a_ += b_; su[u] = a_.x + a_.y; PIN(su[u]); } while (0)
  {
    bf16x8 qf[8];
    LAS char* ak[8];
    UNROLL for (int s = 0; s < 8; ++s) ak[s] = opq(sm + 98304 + (((32 * kt + r) * 256 + 16 * (hf ^ (r & 15))) ^ (32 * s)));
    UNROLL for (int u = 0; u < 8; ++u) {
      CBAR();
      bf16x8 kf[8];
      f32x16 a = {};
      const int v = u > 0 ? u - 1 : 0, v2 = u > 1 ? u - 2 : 0;
      UNROLL for (int s = 0; s < 4; ++s) { if (u == 0) qf[s] = rd128(pq + s * 2048); kf[s] = rd128(ak[s] + (u & 3) * 16384); }
      FENCE();
      if (u > 1) SM_A(v2);
      FENCE();
      if (u > 0) { SM_MAX(v); SM_F(v); }
      UNROLL for (int s = 0; s < 8; ++s) {
        MF(a, kf[s], qf[s]);
        if (s < 4) { if (u == 0) qf[s + 4] = rd128(pq + (s + 4) * 2048); kf[s + 4] = rd128(ak[s + 4] + (u & 3) * 16384); }
        if (u > 0) SM_E(v, 2 * s);
        if (u == 3 && s >= 4) { ak[2 * s - 8] = opq(ak[2 * s - 8] - 98304); ak[2 * s - 7] = opq(ak[2 * s - 7] - 98304); }
      }
      S[u] = a;
    }
  }
  LAS f32x2* stm = (LAS f32x2*)opq(sm + L_Q + r * 8);
  {
    FENCE();
    float mw = min3(min3(mu[0], mu[1], mu[2]), min3(mu[3], mu[4], mu[5]), mu[6]);
    SM_A(6);
    FENCE();
    SM_MAX(7);
    mw = swap32min(min2(mw, mu[7]));
    FENCE();
    SM_F(7);
    UNROLL for (int i = 0; i < 16; i += 2) SM_E(7, i);
    UNROLL for (int u = 0; u < 8; ++u) mu[u] = __builtin_amdgcn_exp2f(mw - mu[u]);
    FENCE();
    SM_A(7);
    float lw_ = mu[0] * su[0], lx = mu[1] * su[1];
    UNROLL for (int u = 2; u < 8; u += 2) { lw_ = __builtin_fmaf(mu[u], su[u], lw_); lx = __builtin_fmaf(mu[u + 1], su[u + 1], lx); }
    lw_ = sum32(lw_ + lx, lw_ + lx);
    stm[cw * 32] = f32x2{mw, lw_};
    CBAR();
    ST(2);
    const f32x2 ot = stm[(cw ^ 2) * 32];
    const float mg = min2(mw, ot.x), ea = __builtin_amdgcn_exp2f(mg - mw), eb = __builtin_amdgcn_exp2f(mg - ot.x);
    const float g = ea * __builtin_amdgcn_rcpf(__builtin_fmaf(ea, lw_, eb * ot.y));
    UNROLL for (int u = 0; u < 8; ++u) mu[u] *= g;
  }
  LAS char* pw = opq(sm + L_P + kt * 4096 + qt * 1024 + l * 16);
  unsigned pd[8];
#define PV_C(u, k) do { float x_ = S[u][2 * (k)] * mu[u]; PIN(x_); float y_ = S[u][2 * (k) + 1] * mu[u]; PIN(y_); pd[k] = pk(x_, y_); PIN(pd[k]); } while (0)
#define PV_C2(u, k) do { float x_ = S[u][2 * (k)] * mu[u], y_ = S[u][2 * (k) + 1] * mu[u], z_ = S[u][2 * (k) + 2] * mu[u], w_ = S[u][2 * (k) + 3] * mu[u]; \
    asm volatile("" : "+v"(x_), "+v"(z_)); asm volatile("" : "+v"(y_), "+v"(w_)); pd[k] = pk(x_, y_); pd[k + 1] = pk(z_, w_); asm volatile("" : "+v"(pd[k]), "+v"(pd[k + 1])); } while (0)
#define PV_W(u, h) do { *(LAS u32x4*)(pw + ((u) & 1) * 8192 + (h) * 2048) = u32x4{pd[4 * (h)], pd[4 * (h) + 1], pd[4 * (h) + 2], pd[4 * (h) + 3]}; FENCE(); } while (0)
  S[0] *= mu[0];
  *(LAS u32x4*)pw = pk8(S[0], 0); *(LAS u32x4*)(pw + 2048) = pk8(S[0], 1);
  FENCE();
  S[1] *= mu[1];
  UNROLL for (int k = 0; k < 8; ++k) { pd[k] = pk(S[1][2 * k], S[1][2 * k + 1]); PIN(pd[k]); }
  f32x16 o0 = {}, o1 = {};
  LAS char* av1 = opq(sm + (4 * hf + qq) * 256 + trc);
  LAS char* av0 = opq(av1 + 98304);
  UNROLL for (int u = 0; u < 7; ++u) {
    CBAR();
    bf16x8 va[4], p0[4], p1[4];
    const int v = u < 6 ? u + 2 : 7;
#define PV_RD(ks) do { va[ks] = rdtr((u < 4 ? av0 : av1) + (u & 3) * 16384 + (ks) * 4096, 2048); \
      p0[ks] = rd128(pr + (u & 1) * 8192 + (ks) * 2048); p1[ks] = rd128(pr + (u & 1) * 8192 + (ks) * 2048 + 1024); FENCE(); } while (0)
    PV_RD(0); PV_RD(1);
    MF(o0, va[0], p0[0]); PV_RD(2); MF(o1, va[0], p1[0]); PV_RD(3);
    MF(o0, va[1], p0[1]); PV_W(u + 1, 0); MF(o1, va[1], p1[1]); PV_W(u + 1, 1);
    MF(o0, va[2], p0[2]); if (u < 6) PV_C2(v, 0);
    MF(o1, va[2], p1[2]); if (u < 6) PV_C2(v, 2);
    MF(o0, va[3], p0[3]); if (u < 6) PV_C2(v, 4);
    MF(o1, va[3], p1[3]); if (u < 6) PV_C2(v, 6);
  }
  {
    CBAR();
    bf16x8 va[4], p0[4], p1[4];
    UNROLL for (int ks = 0; ks < 6; ++ks) {
      if (ks >= 2) { o0 = MFMA(va[ks - 2], p0[ks - 2], o0); FENCE(); }
      if (ks < 4) { va[ks] = rdtr(av1 + 49152 + ks * 4096, 2048); p0[ks] = rd128(pr + 8192 + ks * 2048); }
      else { p1[2 * ks - 8] = rd128(pr + 8192 + (2 * ks - 8) * 2048 + 1024); p1[2 * ks - 7] = rd128(pr + 8192 + (2 * ks - 7) * 2048 + 1024); }
      FENCE();
    }
    const int off = cw * 4096 + l * 16;
    LAS char* d = opq(sm + L_Q + off);
    o1 = MFMA(va[0], p1[0], o1); o1 = MFMA(va[1], p1[1], o1);
    FENCE();
    const u32x4 a0 = pk8(o0, 0);
    st128(a0, rw, slab + S_O + off);
    FENCE();
    o1 = MFMA(va[2], p1[2], o1);
    FENCE();
    const u32x4 a1 = pk8(o0, 1);
    st128(a1, rw, slab + S_O + off + 2048);
    FENCE();
    o1 = MFMA(va[3], p1[3], o1);
    FENCE();
    unsigned ck = csum(csum(tag, a0), a1); PIN(ck);
    *(LAS u32x4*)d = a0;
    FENCE();
    const u32x4 b0 = pk8(o1, 0), b1 = pk8(o1, 1);
    st128(b0, rw, slab + S_O + off + 1024); st128(b1, rw, slab + S_O + off + 3072);
    st32(csum(csum(ck, b0), b1), rw, slab + S_OC + cw * 256 + l * 4);
    st32(tag, rw, hoff);
    FENCE();
    *(LAS u32x4*)(d + 2048) = a1; *(LAS u32x4*)(d + 1024) = b0; *(LAS u32x4*)(d + 3072) = b1;
  }
  ST(3);
  bf16x8 wf[32];
  LAS char* av2 = opq(av1 + L_P);
  LAS char* po = opq(pr + (slot_addr(16) - L_P));
  auto loadW1 = [&](int i, int ks) { wf[4 * i + ks] = rdtr((i < 3 ? av0 + (i + 1) * 16384 : i < 7 ? av1 + (i - 3) * 16384 : av2) + ks * 4096, 2048); };
  f32x16 c0 = {}, c1 = {};
  auto head = [&](int t, LAS char* p, int wnext) {
    bf16x8 g0[8], g1[8];
    UNROLL for (int ks = 0; ks < 10; ++ks) {
      if (ks >= 2) MF(c0, wf[8 * t + ks - 2], g0[ks - 2]);
      if (ks < 8) { g0[ks] = rd128(p + ks * 2048); g1[ks] = rd128(p + ks * 2048 + 1024); FENCE(); }
      if (ks >= 2) { MF(c1, wf[8 * t + ks - 2], g1[ks - 2]);
        if (wnext >= 0) { loadW1(wnext + ((ks - 2) >> 2), (ks - 2) & 3); FENCE(); } }
    }
  };
  CBAR();
  u32x4 t0, t1;
  {
    f32x16 t = {};
    LAS char* at = opq(sm + slot_addr(16) + (4 * hf + qq) * 128 + 16 * ((4 * kt + 2 * blk + (pp >> 1)) ^ ((qq >> 1) << 2)) + 8 * (pp & 1));
    bf16x8 ta[8], tb[8];
    UNROLL for (int ks = 0; ks < 11; ++ks) {
      if (ks >= 3) MF(t, ta[ks - 3], tb[ks - 3]);
      if (ks < 8) { ta[ks] = rdtr(at + ks * 2048, 1024); tb[ks] = rd128(pq + ks * 2048); }
      if (ks >= 7) { loadW1((ks - 7) >> 1, ((ks - 7) & 1) * 2); loadW1((ks - 7) >> 1, ((ks - 7) & 1) * 2 + 1); }
      FENCE();
    }
    head(0, pr + (L_Q - L_P), -1);
    t0 = pk8(t, 0); t1 = pk8(t, 1);
  }
  CBAR();
  UNROLL for (int i = 0; i < 8; ++i) loadW1(2 + (i >> 2), i & 3);
  unsigned tc0 = csum(tag, t0), tc1 = csum(tag, t1);
  const unsigned cl = ((r >> 3) * 16 + hf * 8 + (r & 7)) * 4;
  unsigned aT = slab + S_T + cw * 2048 + l * 16, aTC = slab + S_TC + cw * 512 + cl, aM = slab + S_M + cw * 4096 + l * 16, aMC = slab + S_MC + cw * 512 + cl;
  const unsigned tile = cw * 2 + (xcd >> 2), pb = (unsigned)(j * 8 + 2 * (l >> 4)) * SLAB;
  unsigned aR = pb + S_M + tile * 2048 + (32 * ((l >> 3) & 1) + 8 * (xcd & 3) + (l & 7)) * 16, aRC = pb + S_MC + tile * 256 + (xcd & 3) * 64 + (l & 15) * 4;
  unsigned aO = (unsigned)(b * 64 + 8 * xcd + (l & 7)) * 1152 + (128 * hh + 32 * cw + 16 * blk + 8 * hf + 4 * ((l >> 3) & 1)) * 2;
  asm volatile("" : "+v"(tc0), "+v"(tc1), "+v"(aT), "+v"(aTC), "+v"(aM), "+v"(aMC), "+v"(aR), "+v"(aRC), "+v"(aO));
  CBAR();
  st128(t0, rw, aT); st128(t1, rw, aT + 1024); st32(tc0, rw, aTC); st32(tc1, rw, aTC + 256); st32(tag, rw, hoff + 128);
  FENCE();
  head(1, po, 4);
  CBAR(); head(2, po + 16384, 6);
  CBAR(); head(3, po + 32768, -1);
  ST(4);
  {
    const u32x4 a0 = pk8(c0, 0), a1 = pk8(c0, 1);
    st128(a0, rw, aM); st128(a1, rw, aM + 1024);
    FENCE();
    const u32x4 b0 = pk8(c1, 0), b1 = pk8(c1, 1);
    st128(b0, rw, aM + 2048); st128(b1, rw, aM + 3072);
    FENCE();
    st32(csum(csum(tag, a0), a1), rw, aMC); st32(csum(csum(tag, b0), b1), rw, aMC + 256);
  }
  ST(5);
  {
#pragma unroll 1
    for (unsigned spin = 0; spin < SPIN_CAP; ++spin) {
      DG(0);
      if (__builtin_amdgcn_readfirstlane(*(volatile LAS unsigned*)(sm + L_Q))) break;
    }
    ST(7);
    u32x4 v[2][2];
#pragma unroll 1
    for (unsigned spin = 0; spin < SPIN_CAP; ++spin) {
      asm volatile("" ::: "memory");
      unsigned c[2]; DG(1);
      UNROLL for (int p = 0; p < 2; ++p) {
        v[p][0] = __builtin_amdgcn_raw_buffer_load_b128(rw, aR, p * SLAB, SC1);
        v[p][1] = __builtin_amdgcn_raw_buffer_load_b128(rw, aR + 1024, p * SLAB, SC1);
        c[p] = __builtin_amdgcn_raw_buffer_load_b32(rw, aRC, p * SLAB, SC1);
      }
      if (__all(((csum(csum(tag, v[0][0]), v[0][1]) ^ c[0]) | (csum(csum(tag, v[1][0]), v[1][1]) ^ c[1])) == 0)) break;
    }
    float e[8];
    UNROLL for (int i = 0; i < 4; ++i) {
      e[2 * i] = sum16(blo(v[0][0][i]) + blo(v[1][0][i]), blo(v[0][1][i]) + blo(v[1][1][i]));
      e[2 * i + 1] = sum16(bhi(v[0][0][i]) + bhi(v[1][0][i]), bhi(v[0][1][i]) + bhi(v[1][1][i]));
    }
    *(u32x2*)(out + aO) = u32x2{pk(sum32(e[0], e[4]), sum32(e[1], e[5])), pk(sum32(e[2], e[6]), sum32(e[3], e[7]))};
  }
  ST(6);
}

extern "C" void kernel_launch(void* const* d_in, const int* in_sizes, int n_in, void* d_out, int out_size, void* d_ws, size_t ws_size, hipStream_t stream) {
  gqa_fused<<<256, 512, 0, stream>>>((const char*)d_in[0], (const char*)d_in[1], (const char*)d_in[2], (const char*)d_in[3], (const char*)d_in[4], (char*)d_out, (char*)d_ws);
}
```

```cpp
#define STAMP
#include <hip/hip_runtime.h>
#include <hip/hip_bf16.h>

typedef unsigned long long u64;
typedef short bf16x8 __attribute__((ext_vector_type(8)));
typedef short s16x4 __attribute__((ext_vector_type(4)));
typedef float f32x16 __attribute__((ext_vector_type(16)));
typedef float f32x2 __attribute__((ext_vector_type(2)));
typedef __bf16 bf16x2 __attribute__((ext_vector_type(2)));
typedef unsigned u32x4 __attribute__((ext_vector_type(4)));
typedef unsigned u32x2 __attribute__((ext_vector_type(2)));
#define LAS __attribute__((address_space(3)))
#define CAS __attribute__((address_space(4)))
#define DI __device__ __forceinline__
typedef __amdgpu_buffer_rsrc_t rsrc_t;

constexpr int L_P = 65536, L_Q = 81920;
constexpr int slot_addr(int g) { return g == 24 ? L_P : (g & 7) < 4 ? 98304 + (g & 7) * 16384 : ((g & 7) - 4) * 16384; }
constexpr unsigned S_O = 0, S_OC = 16384, S_T = 17408, S_TC = 25600, S_M = 27648, S_MC = 44032, S_H = 46080, SLAB = 46592;
constexpr unsigned W_EPO = 12u << 20, W_BYTES = W_EPO + 4096;
constexpr int SC1 = 16, NT = 2;
constexpr unsigned SPIN_CAP = 1u << 20;
#define TOUCH_AT 104
#define TOUCH_POL 2
#define W_POL 2
#define RETOUCH 7
#define RE_POL 16
#define T1_AT 3
#define H2_DELAY 8
#define P2_DELAY 4
#define C1 110
#define C2 200
#define HL_AT 2
#define PREW 1
#define BACKOFF 4
#define H2_AT 3
#define T_DELAY 14
#define PREF 1
#define CODE_LINES 152

#define DG(i) do {} while (0)
#define STAMP_OFF (32u << 20)
#define STAMP_N 256
#define STAMP_W 16
#define ST(i) do { st_[i] = __builtin_amdgcn_s_memrealtime(); } while (0)

DI rsrc_t mk(const void* p, unsigned n) { return __builtin_amdgcn_make_buffer_rsrc((void*)p, 0, n, 0x00020000); }
#define DMA(R, LDSOFF, V, SOFF, IMM, POL) __builtin_amdgcn_raw_ptr_buffer_load_lds(R, (LAS void*)(sm + (LDSOFF)), 16, V, SOFF, IMM, POL)
#define FENCE() __builtin_amdgcn_sched_barrier(0)
#define LTS() do {} while (0)
#define LBAR(N) do { asm volatile("s_waitcnt vmcnt(%0)\n\ts_barrier" :: "i"(N) : "memory"); LTS(); } while (0)
#define CBAR() CBAR0()
#define CBAR0() do { FENCE(); asm volatile("" ::: "memory"); __builtin_amdgcn_s_waitcnt(0xC07F); __builtin_amdgcn_s_barrier(); asm volatile("" ::: "memory"); FENCE(); } while (0)
#define PIN(x) asm volatile("" : "+v"(x))
DI LAS char* opq(LAS char* p) { asm("" : "+v"(p)); return p; }
#define MFMA(a, b, c) __builtin_amdgcn_mfma_f32_32x32x16_bf16(a, b, c, 0, 0, 0)
#define MF(acc, a, b) do { FENCE(); acc = MFMA(a, b, acc); PIN(acc); FENCE(); } while (0)
#define UNROLL _Pragma("unroll")
DI bf16x8 rd128(LAS char* p) { return *(LAS bf16x8*)p; }
DI bf16x8 rdtr(LAS char* p, int step) {
  s16x4 a = __builtin_amdgcn_ds_read_tr16_b64_v4i16((LAS s16x4*)p), b = __builtin_amdgcn_ds_read_tr16_b64_v4i16((LAS s16x4*)(p + step));
  return __builtin_shufflevector(a, b, 0, 1, 2, 3, 4, 5, 6, 7);
}
DI unsigned pk(float a, float b) { f32x2 v = {a, b}; return __builtin_bit_cast(unsigned, __builtin_convertvector(v, bf16x2)); }
DI u32x4 pk8(const f32x16& x, int s) { return u32x4{pk(x[8 * s], x[8 * s + 1]), pk(x[8 * s + 2], x[8 * s + 3]), pk(x[8 * s + 4], x[8 * s + 5]), pk(x[8 * s + 6], x[8 * s + 7])}; }
DI float max2(float a, float b) { return __builtin_elementwise_maximum(a, b); }
DI float max3(float a, float b, float c) { return max2(max2(a, b), c); }
DI float min2(float a, float b) { return __builtin_elementwise_minimum(a, b); }
DI float min3(float a, float b, float c) { return min2(min2(a, b), c); }
DI float swap32min(float v) { auto r = __builtin_amdgcn_permlane32_swap(__float_as_uint(v), __float_as_uint(v), false, false); return min2(__uint_as_float(r[0]), __uint_as_float(r[1])); }
DI float swap32max(float v) { auto r = __builtin_amdgcn_permlane32_swap(__float_as_uint(v), __float_as_uint(v), false, false); return max2(__uint_as_float(r[0]), __uint_as_float(r[1])); }
DI float sum16(float a, float b) { auto r = __builtin_amdgcn_permlane16_swap(__float_as_uint(a), __float_as_uint(b), false, false); return __uint_as_float(r[0]) + __uint_as_float(r[1]); }
DI float sum32(float a, float b) { auto r = __builtin_amdgcn_permlane32_swap(__float_as_uint(a), __float_as_uint(b), false, false); return __uint_as_float(r[0]) + __uint_as_float(r[1]); }
DI unsigned csum(unsigned c, u32x4 v) { return (c + v.x + v.y) + v.z + v.w; }
DI float blo(unsigned v) { return __uint_as_float(v << 16); }
DI float bhi(unsigned v) { return __uint_as_float(v & 0xffff0000u); }
DI void st128(u32x4 v, rsrc_t r, unsigned off) { __builtin_amdgcn_raw_buffer_store_b128(v, r, off, 0, SC1); }
DI void st32(unsigned v, rsrc_t r, unsigned off) { __builtin_amdgcn_raw_buffer_store_b32(v, r, off, 0, SC1); }

__global__ __launch_bounds__(512, 2) void gqa_fused(const char* __restrict__ x, const char* __restrict__ kc, const char* __restrict__ vc,
                                                    const char* __restrict__ wq, const char* __restrict__ wo, char* __restrict__ out, char* ws) {
  __shared__ __attribute__((aligned(256))) char smem[163840];
  LAS char* sm = (LAS char*)smem;
  u64 pc;
  asm volatile("s_getpc_b64 %0" : "=s"(pc));
  const int w = blockIdx.x, wv = __builtin_amdgcn_readfirstlane(threadIdx.x >> 6), l = threadIdx.x & 63;
  const int xcd = w & 7, j = w >> 3, b = j >> 2, hh = j & 3, h = xcd * 4 + hh;
  u64 st_[8] = {};
  ST(0);
  asm volatile("" :: "s"(x), "s"(kc), "s"(vc), "s"(wq), "s"(wo), "s"(out), "s"(ws));
  const rsrc_t rw = mk(ws, W_BYTES);
  const size_t kvo = (size_t)(b * 16 + (h >> 1)) * 131072;
  if (__builtin_expect(wv < 4, 1)) {
    const int lw = wv;
    LTS();
    const rsrc_t rx = mk(x + b * 73728, 73728), rq = mk(wq + h * 256, 576 * 8192 - h * 256);
    const unsigned vx0 = (16 * lw + (l >> 3)) * 1152 + 16 * ((l & 7) ^ (l >> 4));
    const unsigned vx1 = (16 * lw + 8 + (l >> 3)) * 1152 + 16 * ((l & 7) ^ (4 + (l >> 4)));
    const unsigned sw4 = 16 * ((l & 15) ^ ((l >> 4) << 2));
    const unsigned vq = (16 * lw + (l >> 4)) * 8192 + sw4;
    {
      int c = (b + 2 * hh) & 7, so = 0;
#pragma unroll 1
      for (int i = 0; i < 9; ++i) {
        if (i >= 2) LBAR(6);
        DMA(rx, so + lw * 2048, vx0, c * 128, 0, 0);
        DMA(rx, so + lw * 2048 + 1024, vx1, c * 128, 0, 0);
        UNROLL for (int p = 0; p < 4; ++p) DMA(rq, so + 8192 + lw * 4096 + p * 1024, vq, (c * 64 + p * 4) * 8192, 0, NT);
        if (i == TOUCH_AT - 100) __builtin_amdgcn_raw_ptr_buffer_load_lds(mk(wo + (size_t)xcd * 512 * 1152, 512 * 1152), (LAS void*)(sm + 74752 + lw * 256), 4, (j * 144 + lw * 36 + (l < 36 ? l : 35)) * 128, 0, 0, TOUCH_POL);
        if (i == RETOUCH) __builtin_amdgcn_raw_ptr_buffer_load_lds(mk(wo + (size_t)xcd * 512 * 1152, 512 * 1152), (LAS void*)(sm + 74752 + lw * 256), 4, (j * 144 + lw * 36 + (l < 36 ? l : 35)) * 128, 0, 0, RE_POL);
        c = c == 8 ? 0 : c + 1;
        so = so == 49152 ? 0 : so + 24576;
      }
    }
    const unsigned tag = *(const CAS unsigned*)(ws + W_EPO + b * 256) + 0x9E3779B9u;
    const rsrc_t rk = mk(kc + kvo, 131072), rv = mk(vc + kvo, 131072), ro = mk(wo + (size_t)xcd * 512 * 1152, 512 * 1152);
    const unsigned vk = (4 * lw + (l >> 4)) * 256 + 16 * ((l & 15) ^ (4 * lw + (l >> 4)));
    const unsigned vv = lw * 4096 + (l >> 4) * 256 + sw4;
    const unsigned vo = (16 * lw + (l >> 4)) * 1152 + hh * 256 + sw4;
    const unsigned vt = (128 * hh + 32 * lw + (l >> 3)) * 1152 + 1024 + 16 * ((l & 7) ^ (((l >> 4) & 1) << 2));
    auto rslot = [](int g) { return (g & 4) ? (g & 3) * 16384 : 98304 + (g & 3) * 16384; };
    auto unitK = [&](int u) { const int s = rslot(u) + lw * 1024; UNROLL for (int p = 0; p < 4; ++p) DMA(rk, s + p * 4096, vk, u * 16384 + p * 4096, 0, NT); };
    auto unitV = [&](int u) { const int s = rslot(u) + lw * 4096;
      DMA(rv, s, vv, u * 16384, 0, NT); DMA(rv, s, vv, u * 16384, 1024, NT); DMA(rv, s, vv, u * 16384, 2048, NT); DMA(rv, s, vv, u * 16384, 3072, NT); };
    auto unitW = [&](int i, int s) {
      const int row0 = ((2 * hh + i) & 7) * 64;
      UNROLL for (int p = 0; p < 4; ++p) DMA(ro, s + lw * 4096 + p * 1024, vo, (row0 + p * 4) * 1152, 0, W_POL);
    };
    LBAR(6); unitK(0);
    LBAR(4); unitK(1); unitK(2); unitK(3);
    asm volatile("" :: "s"(tag));
    ST(1);
#pragma unroll 1
    for (int u = 4; u < 8; ++u) { LBAR(12); unitK(u); }
    LBAR(12);
#pragma unroll 1
    for (int u = 0; u < 8; ++u) {
      if (u < 3) LBAR(8); else if (u > 4) LBAR(16);
      unitV(u);
      if (u == 3) { ST(2); asm volatile("s_barrier" ::: "memory"); }
    }
    LBAR(16);
    LBAR(12);
    LBAR(8);
    UNROLL for (int p = 0; p < 4; ++p) DMA(ro, slot_addr(16) + lw * 4096 + p * 1024, vt, p * 8 * 1152, 0, W_POL);
    LBAR(8); unitW(0, slot_addr(17));
    LBAR(8);
    ST(3);
    u32x4 o[3][4]; unsigned oc[3], oo[3];
    UNROLL for (int t = 0; t < 3; ++t) oo[t] = (unsigned)(((b * 4 + ((hh + 1 + t) & 3)) * 8 + xcd)) * SLAB;
    const unsigned vl = S_O + lw * 4096 + l * 16, vc2 = S_OC + lw * 256 + l * 4;
#define OLOAD(t) asm volatile("buffer_load_dwordx4 %0, %5, %7, %8 offen sc1\n\tbuffer_load_dwordx4 %1, %5, %7, %8 offen offset:1024 sc1\n\t" \
                              "buffer_load_dwordx4 %2, %5, %7, %8 offen offset:2048 sc1\n\tbuffer_load_dwordx4 %3, %5, %7, %8 offen offset:3072 sc1\n\t" \
                              "buffer_load_dword %4, %6, %7, %8 offen sc1" \
                              : "=&v"(o[t][0]), "=&v"(o[t][1]), "=&v"(o[t][2]), "=&v"(o[t][3]), "=&v"(oc[t]) : "v"(vl), "v"(vc2), "s"(rw), "s"(oo[t]) : "memory")
#define OWAIT(t, N) asm volatile("s_waitcnt vmcnt(%5)" : "+v"(o[t][0]), "+v"(o[t][1]), "+v"(o[t][2]), "+v"(o[t][3]), "+v"(oc[t]) : "i"(N) : "memory")
#define OPUT(t, N) do { OWAIT(t, N); \
      _Pragma("unroll 1") for (unsigned spin = 0; spin < SPIN_CAP; ++spin) { \
        UNROLL for (int p = 0; p < 4; ++p) asm volatile("ds_write_b128 %0, %1 offset:%2" :: "v"(od), "v"(o[t][p]), "i"(t * 16384 + p * 1024) : "memory"); \
        if (__all(csum(csum(csum(csum(tag, o[t][0]), o[t][1]), o[t][2]), o[t][3]) == oc[t])) break; \
        DG(3); asm volatile("s_waitcnt lgkmcnt(0)" ::: "memory"); OLOAD(t); OWAIT(t, 0); } } while (0)
    LAS char* od = sm + slot_addr(16) + lw * 4096 + l * 16;
    auto hint = [&](unsigned off) { const unsigned t = (unsigned)__builtin_amdgcn_s_memrealtime();
      __builtin_amdgcn_raw_buffer_store_b64(u32x2{tag, t}, rw, l < 16 && lw == 1 ? (unsigned)w * SLAB + S_H + off + l * 8 : 0x40000000u, 0, SC1); };
    auto until = [&](unsigned t) { _Pragma("unroll 1") for (int spin = 0; spin < 256; ++spin) if ((int)((unsigned)__builtin_amdgcn_s_memrealtime() - t) >= 0) break; };
    const char* hp[3];
    UNROLL for (int t = 0; t < 3; ++t) hp[t] = ws + oo[t] + S_H;
    u64 h0, h1, h2;
#define HLOAD() asm volatile("s_load_dwordx2 %0, %3, 0x0 glc\n\ts_load_dwordx2 %1, %4, 0x0 glc\n\ts_load_dwordx2 %2, %5, 0x0 glc" : "=&s"(h0), "=&s"(h1), "=&s"(h2) : "s"(hp[0]), "s"(hp[1]), "s"(hp[2]) : "memory")
#pragma unroll 1
    for (int i = 1; i < 4; ++i) unitW(i, rslot(i + 1));
    LBAR(8);
    if (lw == 0) {
      if (HL_AT == 0) HLOAD();
      unitW(4, rslot(5));
      if (HL_AT == 1) HLOAD();
      unitW(5, rslot(6));
      if (HL_AT == 2) HLOAD();
#pragma unroll 1
      for (unsigned spin = 0; spin < 4096; ++spin) {
        asm volatile("s_waitcnt lgkmcnt(0)" : "+s"(h0), "+s"(h1), "+s"(h2) :: "memory");
        DG(2);
        if ((unsigned)h0 == tag && (unsigned)h1 == tag && (unsigned)h2 == tag) break;
        HLOAD();
      }
      asm volatile("s_waitcnt lgkmcnt(0)" : "+s"(h0), "+s"(h1), "+s"(h2) :: "memory");
    } else { unitW(4, rslot(5)); unitW(5, rslot(6)); }
    LBAR(8);
    ST(4);
    OLOAD(0); OLOAD(1);
    LBAR(10);
    unitW(6, rslot(7));
    OPUT(0, 9);
    asm volatile("s_waitcnt lgkmcnt(0)\n\ts_barrier" ::: "memory"); LTS();
    unitW(7, L_P);
    OLOAD(2);
    if (H2_AT == 3) hint(128);
    OPUT(1, 13 + (H2_AT == 3));
    if (lw == 2) asm volatile("ds_write_b32 %0, %1" :: "v"(L_Q), "v"(0) : "memory");
    asm volatile("s_waitcnt vmcnt(%0) lgkmcnt(0)\n\ts_barrier" :: "i"(5 + (H2_AT == 3)) : "memory"); LTS();
    if (H2_AT == 4) hint(128);
    OPUT(2, 1);
    asm volatile("s_waitcnt lgkmcnt(0)\n\ts_barrier" ::: "memory"); LTS();
    ST(5);
    if (lw == 2) {
      const char* hb = ws + (size_t)(j * 8) * SLAB + S_H + 128;
      u64 h[8];
#pragma unroll 1
      for (unsigned spin = 0; spin < 4096; ++spin) {
        asm volatile("s_load_dwordx2 %0, %8, %9 glc\n\ts_load_dwordx2 %1, %8, %10 glc\n\ts_load_dwordx2 %2, %8, %11 glc\n\ts_load_dwordx2 %3, %8, %12 glc\n\t"
                     "s_load_dwordx2 %4, %8, %13 glc\n\ts_load_dwordx2 %5, %8, %14 glc\n\ts_load_dwordx2 %6, %8, %15 glc\n\ts_load_dwordx2 %7, %8, %16 glc\n\ts_waitcnt lgkmcnt(0)"
                     : "=&s"(h[0]), "=&s"(h[1]), "=&s"(h[2]), "=&s"(h[3]), "=&s"(h[4]), "=&s"(h[5]), "=&s"(h[6]), "=&s"(h[7])
                     : "s"(hb), "i"(0), "i"(SLAB), "i"(2 * SLAB), "i"(3 * SLAB), "i"(4 * SLAB), "i"(5 * SLAB), "i"(6 * SLAB), "i"(7 * SLAB) : "memory");
        unsigned d = 0; DG(0);
        UNROLL for (int i = 0; i < 8; ++i) d |= (unsigned)h[i] ^ tag;
        if (d == 0) break;
      }
      ST(7);
      const unsigned t0 = (unsigned)(h[0] >> 32); int m = 0;
      UNROLL for (int i = 1; i < 8; ++i) m = max(m, (int)((unsigned)(h[i] >> 32) - t0));
      until(t0 + m + C2);
      asm volatile("ds_write_b32 %0, %1" :: "v"(L_Q), "v"(1) : "memory");
    }
    if (lw == 0) {
      {
        const unsigned frag = ((hh >> 1) * 2 + (xcd >> 2)) * 2 + (hh & 1), pl = 32 * ((l >> 3) & 1) + 8 * (xcd & 3) + (l & 7);
        const unsigned pb = (unsigned)((b * 4 + (l >> 4)) * 8) * SLAB;
        u32x4 v[8];
        unsigned ad = pb + S_T + frag * 1024 + pl * 16, ac = pb + S_TC + frag * 256 + (xcd & 3) * 64 + (l & 15) * 4;
        asm volatile("s_sleep %2" : "+v"(ad), "+v"(ac) : "i"(T_DELAY) : "memory");
#pragma unroll 1
        for (unsigned spin = 0; spin < SPIN_CAP; ++spin) {
          asm volatile("" ::: "memory");
          unsigned c[8], bad = 0; DG(1);
          UNROLL for (int p = 0; p < 8; ++p) { v[p] = __builtin_amdgcn_raw_buffer_load_b128(rw, ad, p * SLAB, SC1);
                                               c[p] = __builtin_amdgcn_raw_buffer_load_b32(rw, ac, p * SLAB, SC1); }
          UNROLL for (int p = 0; p < 8; ++p) bad |= csum(tag, v[p]) ^ c[p];
          if (__all(bad == 0)) break;
          __builtin_amdgcn_s_sleep(BACKOFF);
        }
        if ((w & 31) == 0 && l == 0) st32(tag, rw, W_EPO + b * 256);
        float a[8] = {};
        UNROLL for (int p = 0; p < 8; ++p) UNROLL for (int i = 0; i < 4; ++i) { a[2 * i] += blo(v[p][i]); a[2 * i + 1] += bhi(v[p][i]); }
        float e[4];
        UNROLL for (int i = 0; i < 4; ++i) e[i] = sum16(a[i], a[4 + i]);
        const int n = 512 + 16 * hh + 8 * ((l >> 4) & 1) + 4 * ((l >> 3) & 1) + 2 * (l >> 5);
        *(unsigned*)(out + (size_t)(b * 64 + 8 * xcd + (l & 7)) * 1152 + n * 2) = pk(sum32(e[0], e[2]), sum32(e[1], e[3]));
      }
    }
    ST(6);
    if (threadIdx.x == 0) for (int i = 0; i < 7; ++i) ((u64*)(ws + STAMP_OFF))[w * STAMP_W + i] = st_[i] ? st_[i] : st_[0];
    if (threadIdx.x == 128) ((u64*)(ws + STAMP_OFF))[w * STAMP_W + 7] = st_[7];
    return;
  }
  const int cw = wv - 4, r = l & 31, hf = l >> 5, kt = cw >> 1, qt = cw & 1;
  const unsigned slab = (unsigned)w * SLAB;
  const unsigned hoff = l < 16 && cw == 0 ? slab + S_H + l * 8 : 0x40000000u;
  if (PREF && cw == 0) {
    const char* p = (const char*)(pc & ~127ull) + ((j & 7) * (CODE_LINES / 8) + (l < CODE_LINES / 8 ? l : 0)) * 128;
    if (PREF & 2) {
      if (l == 32) p = kc + kvo;
      if (l == 33) p = vc + kvo;
      if (l == 34) p = wo + (size_t)xcd * 512 * 1152;
      if (l == 35) p = wo + (size_t)xcd * 512 * 1152 + 511 * 1152;
      if (l == 36) p = ws + b * 32 * SLAB;
      if (l == 37) p = ws + (b * 32 + 31) * SLAB + S_MC;
      if (l == 38) p = out + (size_t)b * 73728;
    }
    asm volatile("s_mov_b32 m0, %1\n\ts_nop 0\n\tglobal_load_lds_dword %0, off" :: "v"(p), "i"(73728) : "memory");
  }
  const unsigned tag = *(const CAS unsigned*)(ws + W_EPO + b * 256) + 0x9E3779B9u;
  const int qq = (l & 15) >> 2, pp = l & 3, blk = (l >> 4) & 1;
  const int trc = 16 * ((4 * cw + 2 * blk + (pp >> 1)) ^ (qq << 2)) + 8 * (pp & 1);
  asm volatile("" :: "s"(tag));
  if (PREW) { __builtin_amdgcn_raw_buffer_store_b64(u32x2{~tag, ~tag}, rw, hoff, 0, SC1); __builtin_amdgcn_raw_buffer_store_b64(u32x2{~tag, ~tag}, rw, hoff + 128, 0, SC1); }
  {
    f32x16 qa0 = {}, qa1 = {};
    LAS char* aw = opq(sm + 8192 + (8 * hf + qq) * 256 + trc);
    LAS char* ax[4];
    UNROLL for (int s = 0; s < 4; ++s) ax[s] = sm + ((r * 128 + 16 * (hf ^ ((r >> 1) & 7))) ^ (32 * s));
    bf16x8 fa[2][4], f0[2][4], f1[2][4];
    UNROLL for (int i = 0; i < 9; ++i) {
      CBAR();
      const int so = (i % 3) * 24576, cur = i & 1, prv = cur ^ 1;
      UNROLL for (int s = 0; s < 4; ++s) {
        if (i > 0) { if (s & 1) qa1 = MFMA(fa[prv][s >> 1], f1[prv][s >> 1], qa1); else qa0 = MFMA(fa[prv][s >> 1], f0[prv][s >> 1], qa0); }
        FENCE();
        fa[cur][s] = rdtr(aw + so + s * 4096, 1024); f0[cur][s] = rd128(ax[s] + so); f1[cur][s] = rd128(ax[s] + so + 4096);
        FENCE();
      }
      if (i > 0) { UNROLL for (int s = 2; s < 4; ++s) { qa0 = MFMA(fa[prv][s], f0[prv][s], qa0); qa1 = MFMA(fa[prv][s], f1[prv][s], qa1); } }
    }
    FENCE();
    UNROLL for (int s = 0; s < 4; ++s) qa0 = MFMA(fa[0][s], f0[0][s], qa0);
    FENCE();
    UNROLL for (int s = 0; s < 4; ++s) qa1 = MFMA(fa[0][s], f1[0][s], qa1);
    FENCE();
    LAS char* q = opq(sm + L_Q + cw * 4096 + r * 16 + 8 * hf);
    UNROLL for (int s = 0; s < 2; ++s) { const u32x4 p0 = pk8(qa0, s); *(LAS u32x2*)(q + s * 2048) = u32x2{p0.x, p0.y}; *(LAS u32x2*)(q + s * 2048 + 512) = u32x2{p0.z, p0.w}; }
    FENCE();
    UNROLL for (int s = 0; s < 2; ++s) { const u32x4 p1 = pk8(qa1, s); *(LAS u32x2*)(q + s * 2048 + 1024) = u32x2{p1.x, p1.y}; *(LAS u32x2*)(q + s * 2048 + 1536) = u32x2{p1.z, p1.w}; }
    asm volatile("s_waitcnt vmcnt(0)" ::: "memory");
  }
  ST(1);
  constexpr float CS = 0.08838834764831845f * 1.4426950408889634f;
  f32x16 S[8];
  float mu[8], su[8];
  LAS char* pr = opq(sm + L_P + l * 16);
  LAS char* pq = opq(sm + L_Q + qt * 1024 + l * 16);
#define SM_MAX(u) do { float a_ = max3(S[u][0], S[u][1], S[u][2]), b_ = max3(S[u][3], S[u][4], S[u][5]); \
    a_ = max3(a_, S[u][6], S[u][7]); b_ = max3(b_, S[u][8], S[u][9]); a_ = max3(a_, S[u][10], S[u][11]); b_ = max3(b_, S[u][12], S[u][13]); \
    a_ = max3(a_, S[u][14], S[u][15]); mu[u] = -CS * max2(a_, b_); PIN(mu[u]); } while (0)
#define SM_F(u) do { UNROLL for (int i = 0; i < 16; i += 2) { f32x2 t_ = {S[u][i], S[u][i + 1]}; t_ = __builtin_elementwise_fma(t_, f32x2{CS, CS}, f32x2{mu[u], mu[u]}); PIN(t_); S[u][i] = t_.x; S[u][i + 1] = t_.y; } } while (0)
#define SM_E(u, i) do { f32x2 t_ = {__builtin_amdgcn_exp2f(S[u][i]), __builtin_amdgcn_exp2f(S[u][i + 1])}; PIN(t_); S[u][i] = t_.x; S[u][i + 1] = t_.y; } while (0)
#define SM_A(u) do { f32x2 a_ = f32x2{S[u][0], S[u][1]} + f32x2{S[u][2], S[u][3]}, b_ = f32x2{S[u][4], S[u][5]} + f32x2{S[u][6], S[u][7]}; \
    a_ += f32x2{S[u][8], S[u][9]}; b_ += f32x2{S[u][10], S[u][11]}; a_ += f32x2{S[u][12], S[u][13]}; b_ += f32x2{S[u][14], S[u][15]}; a_ += b_; su[u] = a_.x + a_.y; PIN(su[u]); } while (0)
  {
    bf16x8 qf[8];
    LAS char* ak[8];
    UNROLL for (int s = 0; s < 8; ++s) ak[s] = opq(sm + 98304 + (((32 * kt + r) * 256 + 16 * (hf ^ (r & 15))) ^ (32 * s)));
    UNROLL for (int u = 0; u < 8; ++u) {
      CBAR();
      bf16x8 kf[8];
      f32x16 a = {};
      const int v = u > 0 ? u - 1 : 0, v2 = u > 1 ? u - 2 : 0;
      UNROLL for (int s = 0; s < 4; ++s) { if (u == 0) qf[s] = rd128(pq + s * 2048); kf[s] = rd128(ak[s] + (u & 3) * 16384); }
      FENCE();
      if (u > 1) SM_A(v2);
      FENCE();
      if (u > 0) { SM_MAX(v); SM_F(v); }
      UNROLL for (int s = 0; s < 8; ++s) {
        MF(a, kf[s], qf[s]);
        if (s < 4) { if (u == 0) qf[s + 4] = rd128(pq + (s + 4) * 2048); kf[s + 4] = rd128(ak[s + 4] + (u & 3) * 16384); }
        if (u > 0) SM_E(v, 2 * s);
        if (u == 3 && s >= 4) { ak[2 * s - 8] = opq(ak[2 * s - 8] - 98304); ak[2 * s - 7] = opq(ak[2 * s - 7] - 98304); }
      }
      S[u] = a;
    }
  }
  LAS f32x2* stm = (LAS f32x2*)opq(sm + L_Q + r * 8);
  {
    FENCE();
    float mw = min3(min3(mu[0], mu[1], mu[2]), min3(mu[3], mu[4], mu[5]), mu[6]);
    SM_A(6);
    FENCE();
    SM_MAX(7);
    mw = swap32min(min2(mw, mu[7]));
    FENCE();
    SM_F(7);
    UNROLL for (int i = 0; i < 16; i += 2) SM_E(7, i);
    UNROLL for (int u = 0; u < 8; ++u) mu[u] = __builtin_amdgcn_exp2f(mw - mu[u]);
    FENCE();
    SM_A(7);
    float lw_ = mu[0] * su[0], lx = mu[1] * su[1];
    UNROLL for (int u = 2; u < 8; u += 2) { lw_ = __builtin_fmaf(mu[u], su[u], lw_); lx = __builtin_fmaf(mu[u + 1], su[u + 1], lx); }
    lw_ = sum32(lw_ + lx, lw_ + lx);
    stm[cw * 32] = f32x2{mw, lw_};
    CBAR();
    ST(2);
    const f32x2 ot = stm[(cw ^ 2) * 32];
    const float mg = min2(mw, ot.x), ea = __builtin_amdgcn_exp2f(mg - mw), eb = __builtin_amdgcn_exp2f(mg - ot.x);
    const float g = ea * __builtin_amdgcn_rcpf(__builtin_fmaf(ea, lw_, eb * ot.y));
    UNROLL for (int u = 0; u < 8; ++u) mu[u] *= g;
  }
  LAS char* pw = opq(sm + L_P + kt * 4096 + qt * 1024 + l * 16);
  unsigned pd[8];
#define PV_C(u, k) do { float x_ = S[u][2 * (k)] * mu[u]; PIN(x_); float y_ = S[u][2 * (k) + 1] * mu[u]; PIN(y_); pd[k] = pk(x_, y_); PIN(pd[k]); } while (0)
#define PV_C2(u, k) do { float x_ = S[u][2 * (k)] * mu[u], y_ = S[u][2 * (k) + 1] * mu[u], z_ = S[u][2 * (k) + 2] * mu[u], w_ = S[u][2 * (k) + 3] * mu[u]; \
    asm volatile("" : "+v"(x_), "+v"(z_)); asm volatile("" : "+v"(y_), "+v"(w_)); pd[k] = pk(x_, y_); pd[k + 1] = pk(z_, w_); asm volatile("" : "+v"(pd[k]), "+v"(pd[k + 1])); } while (0)
#define PV_W(u, h) do { *(LAS u32x4*)(pw + ((u) & 1) * 8192 + (h) * 2048) = u32x4{pd[4 * (h)], pd[4 * (h) + 1], pd[4 * (h) + 2], pd[4 * (h) + 3]}; FENCE(); } while (0)
  S[0] *= mu[0];
  *(LAS u32x4*)pw = pk8(S[0], 0); *(LAS u32x4*)(pw + 2048) = pk8(S[0], 1);
  FENCE();
  S[1] *= mu[1];
  UNROLL for (int k = 0; k < 8; ++k) { pd[k] = pk(S[1][2 * k], S[1][2 * k + 1]); PIN(pd[k]); }
  f32x16 o0 = {}, o1 = {};
  LAS char* av1 = opq(sm + (4 * hf + qq) * 256 + trc);
  LAS char* av0 = opq(av1 + 98304);
  UNROLL for (int u = 0; u < 7; ++u) {
    CBAR();
    bf16x8 va[4], p0[4], p1[4];
    const int v = u < 6 ? u + 2 : 7;
#define PV_RD(ks) do { va[ks] = rdtr((u < 4 ? av0 : av1) + (u & 3) * 16384 + (ks) * 4096, 2048); \
      p0[ks] = rd128(pr + (u & 1) * 8192 + (ks) * 2048); p1[ks] = rd128(pr + (u & 1) * 8192 + (ks) * 2048 + 1024); FENCE(); } while (0)
    PV_RD(0); PV_RD(1);
    MF(o0, va[0], p0[0]); PV_RD(2); MF(o1, va[0], p1[0]); PV_RD(3);
    MF(o0, va[1], p0[1]); PV_W(u + 1, 0); MF(o1, va[1], p1[1]); PV_W(u + 1, 1);
    MF(o0, va[2], p0[2]); if (u < 6) PV_C2(v, 0);
    MF(o1, va[2], p1[2]); if (u < 6) PV_C2(v, 2);
    MF(o0, va[3], p0[3]); if (u < 6) PV_C2(v, 4);
    MF(o1, va[3], p1[3]); if (u < 6) PV_C2(v, 6);
  }
  {
    CBAR();
    bf16x8 va[4], p0[4], p1[4];
    UNROLL for (int ks = 0; ks < 6; ++ks) {
      if (ks >= 2) { o0 = MFMA(va[ks - 2], p0[ks - 2], o0); FENCE(); }
      if (ks < 4) { va[ks] = rdtr(av1 + 49152 + ks * 4096, 2048); p0[ks] = rd128(pr + 8192 + ks * 2048); }
      else { p1[2 * ks - 8] = rd128(pr + 8192 + (2 * ks - 8) * 2048 + 1024); p1[2 * ks - 7] = rd128(pr + 8192 + (2 * ks - 7) * 2048 + 1024); }
      FENCE();
    }
    const int off = cw * 4096 + l * 16;
    LAS char* d = opq(sm + L_Q + off);
    o1 = MFMA(va[0], p1[0], o1); o1 = MFMA(va[1], p1[1], o1);
    FENCE();
    const u32x4 a0 = pk8(o0, 0);
    st128(a0, rw, slab + S_O + off);
    FENCE();
    o1 = MFMA(va[2], p1[2], o1);
    FENCE();
    const u32x4 a1 = pk8(o0, 1);
    st128(a1, rw, slab + S_O + off + 2048);
    FENCE();
    o1 = MFMA(va[3], p1[3], o1);
    FENCE();
    unsigned ck = csum(csum(tag, a0), a1); PIN(ck);
    *(LAS u32x4*)d = a0;
    FENCE();
    const u32x4 b0 = pk8(o1, 0), b1 = pk8(o1, 1);
    st128(b0, rw, slab + S_O + off + 1024); st128(b1, rw, slab + S_O + off + 3072);
    st32(csum(csum(ck, b0), b1), rw, slab + S_OC + cw * 256 + l * 4);
    __builtin_amdgcn_raw_buffer_store_b64(u32x2{tag, tag}, rw, hoff, 0, SC1);
    FENCE();
    *(LAS u32x4*)(d + 2048) = a1; *(LAS u32x4*)(d + 1024) = b0; *(LAS u32x4*)(d + 3072) = b1;
  }
  ST(3);
  bf16x8 wf[32];
  LAS char* av2 = opq(av1 + L_P);
  LAS char* po = opq(pr + (slot_addr(16) - L_P));
  auto loadW1 = [&](int i, int ks) { wf[4 * i + ks] = rdtr((i < 3 ? av0 + (i + 1) * 16384 : i < 7 ? av1 + (i - 3) * 16384 : av2) + ks * 4096, 2048); };
  f32x16 c0 = {}, c1 = {};
  auto head = [&](int t, LAS char* p, int wnext) {
    bf16x8 g0[8], g1[8];
    UNROLL for (int ks = 0; ks < 10; ++ks) {
      if (ks >= 2) MF(c0, wf[8 * t + ks - 2], g0[ks - 2]);
      if (ks < 8) { g0[ks] = rd128(p + ks * 2048); g1[ks] = rd128(p + ks * 2048 + 1024); FENCE(); }
      if (ks >= 2) { MF(c1, wf[8 * t + ks - 2], g1[ks - 2]);
        if (wnext >= 0) { loadW1(wnext + ((ks - 2) >> 2), (ks - 2) & 3); FENCE(); } }
    }
  };
  CBAR();
  u32x4 t0, t1;
  {
    f32x16 t = {};
    LAS char* at = opq(sm + slot_addr(16) + (4 * hf + qq) * 128 + 16 * ((4 * kt + 2 * blk + (pp >> 1)) ^ ((qq >> 1) << 2)) + 8 * (pp & 1));
    bf16x8 ta[8], tb[8];
    UNROLL for (int ks = 0; ks < 11; ++ks) {
      if (ks >= 3) MF(t, ta[ks - 3], tb[ks - 3]);
      if (ks < 8) { ta[ks] = rdtr(at + ks * 2048, 1024); tb[ks] = rd128(pq + ks * 2048); }
      if (ks >= 7) { loadW1((ks - 7) >> 1, ((ks - 7) & 1) * 2); loadW1((ks - 7) >> 1, ((ks - 7) & 1) * 2 + 1); }
      FENCE();
    }
    head(0, pr + (L_Q - L_P), -1);
    t0 = pk8(t, 0); t1 = pk8(t, 1);
  }
  CBAR();
  UNROLL for (int i = 0; i < 8; ++i) loadW1(2 + (i >> 2), i & 3);
  unsigned tc0 = csum(tag, t0), tc1 = csum(tag, t1);
  const unsigned cl = ((r >> 3) * 16 + hf * 8 + (r & 7)) * 4;
  unsigned aT = slab + S_T + cw * 2048 + l * 16, aTC = slab + S_TC + cw * 512 + cl, aM = slab + S_M + cw * 4096 + l * 16, aMC = slab + S_MC + cw * 512 + cl;
  const unsigned tile = cw * 2 + (xcd >> 2), pb = (unsigned)(j * 8 + 2 * (l >> 4)) * SLAB;
  unsigned aR = pb + S_M + tile * 2048 + (32 * ((l >> 3) & 1) + 8 * (xcd & 3) + (l & 7)) * 16, aRC = pb + S_MC + tile * 256 + (xcd & 3) * 64 + (l & 15) * 4;
  unsigned aO = (unsigned)(b * 64 + 8 * xcd + (l & 7)) * 1152 + (128 * hh + 32 * cw + 16 * blk + 8 * hf + 4 * ((l >> 3) & 1)) * 2;
  asm volatile("" : "+v"(tc0), "+v"(tc1), "+v"(aT), "+v"(aTC), "+v"(aM), "+v"(aMC), "+v"(aR), "+v"(aRC), "+v"(aO));
  CBAR();
  UNROLL for (int i = 0; i < 8; ++i) loadW1(4 + (i >> 2), i & 3);
  CBAR();
  st128(t0, rw, aT); st128(t1, rw, aT + 1024); st32(tc0, rw, aTC); st32(tc1, rw, aTC + 256);
  FENCE();
  head(1, po, -1);
  CBAR(); head(2, po + 16384, 6);
  CBAR(); head(3, po + 32768, -1);
  ST(4);
  {
    const u32x4 a0 = pk8(c0, 0), a1 = pk8(c0, 1);
    st128(a0, rw, aM); st128(a1, rw, aM + 1024);
    FENCE();
    const u32x4 b0 = pk8(c1, 0), b1 = pk8(c1, 1);
    st128(b0, rw, aM + 2048); st128(b1, rw, aM + 3072);
    FENCE();
    st32(csum(csum(tag, a0), a1), rw, aMC); st32(csum(csum(tag, b0), b1), rw, aMC + 256);
  }
  ST(5);
  {
#pragma unroll 1
    for (unsigned spin = 0; spin < SPIN_CAP; ++spin) if (__builtin_amdgcn_readfirstlane(*(volatile LAS unsigned*)(sm + L_Q))) break;
    ST(7);
    u32x4 v[2][2];
#pragma unroll 1
    for (unsigned spin = 0; spin < SPIN_CAP; ++spin) {
      asm volatile("" ::: "memory");
      unsigned c[2]; DG(1);
      UNROLL for (int p = 0; p < 2; ++p) {
        v[p][0] = __builtin_amdgcn_raw_buffer_load_b128(rw, aR, p * SLAB, SC1);
        v[p][1] = __builtin_amdgcn_raw_buffer_load_b128(rw, aR + 1024, p * SLAB, SC1);
        c[p] = __builtin_amdgcn_raw_buffer_load_b32(rw, aRC, p * SLAB, SC1);
      }
      if (__all(((csum(csum(tag, v[0][0]), v[0][1]) ^ c[0]) | (csum(csum(tag, v[1][0]), v[1][1]) ^ c[1])) == 0)) break;
      __builtin_amdgcn_s_sleep(BACKOFF);
    }
    float e[8];
    UNROLL for (int i = 0; i < 4; ++i) {
      e[2 * i] = sum16(blo(v[0][0][i]) + blo(v[1][0][i]), blo(v[0][1][i]) + blo(v[1][1][i]));
      e[2 * i + 1] = sum16(bhi(v[0][0][i]) + bhi(v[1][0][i]), bhi(v[0][1][i]) + bhi(v[1][1][i]));
    }
    *(u32x2*)(out + aO) = u32x2{pk(sum32(e[0], e[4]), sum32(e[1], e[5])), pk(sum32(e[2], e[6]), sum32(e[3], e[7]))};
  }
  ST(6);
  if (threadIdx.x == 256) for (int i = 0; i < 8; ++i) ((u64*)(ws + STAMP_OFF))[w * STAMP_W + 8 + i] = st_[i] ? st_[i] : st_[0];
}

__global__ void k_probe(char* ws) {
  const u64* st = (const u64*)(ws + STAMP_OFF); int t = threadIdx.x;
  __shared__ u64 a[256], b[256];
  a[t] = st[t * STAMP_W]; b[t] = st[t * STAMP_W + 1];
  __syncthreads();
  if (t == 0) {
    u64 mn = ~0ull, mx = 0, e = 0, sum = 0;
    for (int i = 0; i < 256; ++i) { mn = a[i] < mn ? a[i] : mn; mx = a[i] > mx ? a[i] : mx; e = b[i] > e ? b[i] : e; sum += b[i] - a[i]; }
    u64 d = sum / 256;
    if (d > 10000) d = 10000;
    u64 t0 = __builtin_amdgcn_s_memrealtime();
    while (__builtin_amdgcn_s_memrealtime() - t0 < d * 100) __builtin_amdgcn_s_sleep(4);
  }
}
extern "C" void kernel_launch(void* const* d_in, const int* in_sizes, int n_in, void* d_out, int out_size, void* d_ws, size_t ws_size, hipStream_t stream) {
  gqa_fused<<<256, 512, 0, stream>>>((const char*)d_in[0], (const char*)d_in[1], (const char*)d_in[2], (const char*)d_in[3], (const char*)d_in[4], (char*)d_out, (char*)d_ws);
  k_probe<<<1, 256, 0, stream>>>((char*)d_ws);
}
```

```cpp
#include <hip/hip_runtime.h>
#include <hip/hip_bf16.h>

typedef unsigned long long u64;
typedef short bf16x8 __attribute__((ext_vector_type(8)));
typedef short s16x4 __attribute__((ext_vector_type(4)));
typedef float f32x16 __attribute__((ext_vector_type(16)));
typedef float f32x2 __attribute__((ext_vector_type(2)));
typedef __bf16 bf16x2 __attribute__((ext_vector_type(2)));
typedef unsigned u32x4 __attribute__((ext_vector_type(4)));
typedef unsigned u32x2 __attribute__((ext_vector_type(2)));
#define LAS __attribute__((address_space(3)))
#define CAS __attribute__((address_space(4)))
#define DI __device__ __forceinline__
typedef __amdgpu_buffer_rsrc_t rsrc_t;

constexpr int L_P = 65536, L_Q = 81920;
constexpr int slot_addr(int g) { return g == 24 ? L_P : (g & 7) < 4 ? 98304 + (g & 7) * 16384 : ((g & 7) - 4) * 16384; }
constexpr unsigned S_O = 0, S_OC = 16384, S_T = 17408, S_TC = 25600, S_M = 27648, S_MC = 44032, S_H = 46080, SLAB = 46592;
constexpr unsigned W_EPO = 12u << 20, W_BYTES = W_EPO + 4096;
constexpr int SC1 = 16, NT = 2;
constexpr unsigned SPIN_CAP = 1u << 20;
#define TOUCH_AT 104
#define TOUCH_POL 2
#define W_POL 2
#define RETOUCH 7
#define RE_POL 16
#define T1_AT 3
#define H2_DELAY 8
#define P2_DELAY 4
#define C1 110
#define C2 200
#define HL_AT 2
#define PREW 1
#define BACKOFF 4
#define H2_AT 3
#define T_DELAY 14
#define PREF 3
#define CODE_LINES 152

#define DG(i) do {} while (0)
#define ST(i) do {} while (0)

DI rsrc_t mk(const void* p, unsigned n) { return __builtin_amdgcn_make_buffer_rsrc((void*)p, 0, n, 0x00020000); }
#define DMA(R, LDSOFF, V, SOFF, IMM, POL) __builtin_amdgcn_raw_ptr_buffer_load_lds(R, (LAS void*)(sm + (LDSOFF)), 16, V, SOFF, IMM, POL)
#define FENCE() __builtin_amdgcn_sched_barrier(0)
#define LTS() do {} while (0)
#define LBAR(N) do { asm volatile("s_waitcnt vmcnt(%0)\n\ts_barrier" :: "i"(N) : "memory"); LTS(); } while (0)
#define CBAR() CBAR0()
#define CBAR0() do { FENCE(); asm volatile("" ::: "memory"); __builtin_amdgcn_s_waitcnt(0xC07F); __builtin_amdgcn_s_barrier(); asm volatile("" ::: "memory"); FENCE(); } while (0)
#define PIN(x) asm volatile("" : "+v"(x))
DI LAS char* opq(LAS char* p) { asm("" : "+v"(p)); return p; }
#define MFMA(a, b, c) __builtin_amdgcn_mfma_f32_32x32x16_bf16(a, b, c, 0, 0, 0)
#define MF(acc, a, b) do { FENCE(); acc = MFMA(a, b, acc); PIN(acc); FENCE(); } while (0)
#define UNROLL _Pragma("unroll")
DI bf16x8 rd128(LAS char* p) { return *(LAS bf16x8*)p; }
DI bf16x8 rdtr(LAS char* p, int step) {
  s16x4 a = __builtin_amdgcn_ds_read_tr16_b64_v4i16((LAS s16x4*)p), b = __builtin_amdgcn_ds_read_tr16_b64_v4i16((LAS s16x4*)(p + step));
  return __builtin_shufflevector(a, b, 0, 1, 2, 3, 4, 5, 6, 7);
}
DI unsigned pk(float a, float b) { f32x2 v = {a, b}; return __builtin_bit_cast(unsigned, __builtin_convertvector(v, bf16x2)); }
DI u32x4 pk8(const f32x16& x, int s) { return u32x4{pk(x[8 * s], x[8 * s + 1]), pk(x[8 * s + 2], x[8 * s + 3]), pk(x[8 * s + 4], x[8 * s + 5]), pk(x[8 * s + 6], x[8 * s + 7])}; }
DI float max2(float a, float b) { return __builtin_elementwise_maximum(a, b); }
DI float max3(float a, float b, float c) { return max2(max2(a, b), c); }
DI float min2(float a, float b) { return __builtin_elementwise_minimum(a, b); }
DI float min3(float a, float b, float c) { return min2(min2(a, b), c); }
DI float swap32min(float v) { auto r = __builtin_amdgcn_permlane32_swap(__float_as_uint(v), __float_as_uint(v), false, false); return min2(__uint_as_float(r[0]), __uint_as_float(r[1])); }
DI float swap32max(float v) { auto r = __builtin_amdgcn_permlane32_swap(__float_as_uint(v), __float_as_uint(v), false, false); return max2(__uint_as_float(r[0]), __uint_as_float(r[1])); }
DI float sum16(float a, float b) { auto r = __builtin_amdgcn_permlane16_swap(__float_as_uint(a), __float_as_uint(b), false, false); return __uint_as_float(r[0]) + __uint_as_float(r[1]); }
DI float sum32(float a, float b) { auto r = __builtin_amdgcn_permlane32_swap(__float_as_uint(a), __float_as_uint(b), false, false); return __uint_as_float(r[0]) + __uint_as_float(r[1]); }
DI unsigned csum(unsigned c, u32x4 v) { return (c + v.x + v.y) + v.z + v.w; }
DI float blo(unsigned v) { return __uint_as_float(v << 16); }
DI float bhi(unsigned v) { return __uint_as_float(v & 0xffff0000u); }
DI void st128(u32x4 v, rsrc_t r, unsigned off) { __builtin_amdgcn_raw_buffer_store_b128(v, r, off, 0, SC1); }
DI void st32(unsigned v, rsrc_t r, unsigned off) { __builtin_amdgcn_raw_buffer_store_b32(v, r, off, 0, SC1); }

__global__ __launch_bounds__(512, 2) void gqa_fused(const char* __restrict__ x, const char* __restrict__ kc, const char* __restrict__ vc,
                                                    const char* __restrict__ wq, const char* __restrict__ wo, char* __restrict__ out, char* ws) {
  __shared__ __attribute__((aligned(256))) char smem[163840];
  LAS char* sm = (LAS char*)smem;
  u64 pc;
  asm volatile("s_getpc_b64 %0" : "=s"(pc));
  const int w = blockIdx.x, wv = __builtin_amdgcn_readfirstlane(threadIdx.x >> 6), l = threadIdx.x & 63;
  const int xcd = w & 7, j = w >> 3, b = j >> 2, hh = j & 3, h = xcd * 4 + hh;
  ST(0);
  asm volatile("" :: "s"(x), "s"(kc), "s"(vc), "s"(wq), "s"(wo), "s"(out), "s"(ws));
  const rsrc_t rw = mk(ws, W_BYTES);
  const size_t kvo = (size_t)(b * 16 + (h >> 1)) * 131072;
  if (__builtin_expect(wv < 4, 1)) {
    const int lw = wv;
    LTS();
    const rsrc_t rx = mk(x + b * 73728, 73728), rq = mk(wq + h * 256, 576 * 8192 - h * 256);
    const unsigned vx0 = (16 * lw + (l >> 3)) * 1152 + 16 * ((l & 7) ^ (l >> 4));
    const unsigned vx1 = (16 * lw + 8 + (l >> 3)) * 1152 + 16 * ((l & 7) ^ (4 + (l >> 4)));
    const unsigned sw4 = 16 * ((l & 15) ^ ((l >> 4) << 2));
    const unsigned vq = (16 * lw + (l >> 4)) * 8192 + sw4;
    {
      int c = (b + 2 * hh) & 7, so = 0;
#pragma unroll 1
      for (int i = 0; i < 9; ++i) {
        if (i >= 2) LBAR(6);
        DMA(rx, so + lw * 2048, vx0, c * 128, 0, 0);
        DMA(rx, so + lw * 2048 + 1024, vx1, c * 128, 0, 0);
        UNROLL for (int p = 0; p < 4; ++p) DMA(rq, so + 8192 + lw * 4096 + p * 1024, vq, (c * 64 + p * 4) * 8192, 0, NT);
        if (i == TOUCH_AT - 100) __builtin_amdgcn_raw_ptr_buffer_load_lds(mk(wo + (size_t)xcd * 512 * 1152, 512 * 1152), (LAS void*)(sm + 74752 + lw * 256), 4, (j * 144 + lw * 36 + (l < 36 ? l : 35)) * 128, 0, 0, TOUCH_POL);
        if (i == RETOUCH) __builtin_amdgcn_raw_ptr_buffer_load_lds(mk(wo + (size_t)xcd * 512 * 1152, 512 * 1152), (LAS void*)(sm + 74752 + lw * 256), 4, (j * 144 + lw * 36 + (l < 36 ? l : 35)) * 128, 0, 0, RE_POL);
        c = c == 8 ? 0 : c + 1;
        so = so == 49152 ? 0 : so + 24576;
      }
    }
    const unsigned tag = *(const CAS unsigned*)(ws + W_EPO + b * 256) + 0x9E3779B9u;
    const rsrc_t rk = mk(kc + kvo, 131072), rv = mk(vc + kvo, 131072), ro = mk(wo + (size_t)xcd * 512 * 1152, 512 * 1152);
    const unsigned vk = (4 * lw + (l >> 4)) * 256 + 16 * ((l & 15) ^ (4 * lw + (l >> 4)));
    const unsigned vv = lw * 4096 + (l >> 4) * 256 + sw4;
    const unsigned vo = (16 * lw + (l >> 4)) * 1152 + hh * 256 + sw4;
    const unsigned vt = (128 * hh + 32 * lw + (l >> 3)) * 1152 + 1024 + 16 * ((l & 7) ^ (((l >> 4) & 1) << 2));
    auto rslot = [](int g) { return (g & 4) ? (g & 3) * 16384 : 98304 + (g & 3) * 16384; };
    auto unitK = [&](int u) { const int s = rslot(u) + lw * 1024; UNROLL for (int p = 0; p < 4; ++p) DMA(rk, s + p * 4096, vk, u * 16384 + p * 4096, 0, NT); };
    auto unitV = [&](int u) { const int s = rslot(u) + lw * 4096;
      DMA(rv, s, vv, u * 16384, 0, NT); DMA(rv, s, vv, u * 16384, 1024, NT); DMA(rv, s, vv, u * 16384, 2048, NT); DMA(rv, s, vv, u * 16384, 3072, NT); };
    auto unitW = [&](int i, int s) {
      const int row0 = ((2 * hh + i) & 7) * 64;
      UNROLL for (int p = 0; p < 4; ++p) DMA(ro, s + lw * 4096 + p * 1024, vo, (row0 + p * 4) * 1152, 0, W_POL);
    };
    LBAR(6); unitK(0);
    LBAR(4); unitK(1); unitK(2); unitK(3);
    asm volatile("" :: "s"(tag));
    ST(1);
#pragma unroll 1
    for (int u = 4; u < 8; ++u) { LBAR(12); unitK(u); }
    LBAR(12);
#pragma unroll 1
    for (int u = 0; u < 8; ++u) {
      if (u < 3) LBAR(8); else if (u > 4) LBAR(16);
      unitV(u);
      if (u == 3) { ST(2); asm volatile("s_barrier" ::: "memory"); }
    }
    LBAR(16);
    LBAR(12);
    LBAR(8);
    UNROLL for (int p = 0; p < 4; ++p) DMA(ro, slot_addr(16) + lw * 4096 + p * 1024, vt, p * 8 * 1152, 0, W_POL);
    LBAR(8); unitW(0, slot_addr(17));
    LBAR(8);
    ST(3);
    u32x4 o[3][4]; unsigned oc[3], oo[3];
    UNROLL for (int t = 0; t < 3; ++t) oo[t] = (unsigned)(((b * 4 + ((hh + 1 + t) & 3)) * 8 + xcd)) * SLAB;
    const unsigned vl = S_O + lw * 4096 + l * 16, vc2 = S_OC + lw * 256 + l * 4;
#define OLOAD(t) asm volatile("buffer_load_dwordx4 %0, %5, %7, %8 offen sc1\n\tbuffer_load_dwordx4 %1, %5, %7, %8 offen offset:1024 sc1\n\t" \
                              "buffer_load_dwordx4 %2, %5, %7, %8 offen offset:2048 sc1\n\tbuffer_load_dwordx4 %3, %5, %7, %8 offen offset:3072 sc1\n\t" \
                              "buffer_load_dword %4, %6, %7, %8 offen sc1" \
                              : "=&v"(o[t][0]), "=&v"(o[t][1]), "=&v"(o[t][2]), "=&v"(o[t][3]), "=&v"(oc[t]) : "v"(vl), "v"(vc2), "s"(rw), "s"(oo[t]) : "memory")
#define OWAIT(t, N) asm volatile("s_waitcnt vmcnt(%5)" : "+v"(o[t][0]), "+v"(o[t][1]), "+v"(o[t][2]), "+v"(o[t][3]), "+v"(oc[t]) : "i"(N) : "memory")
#define OPUT(t, N) do { OWAIT(t, N); \
      _Pragma("unroll 1") for (unsigned spin = 0; spin < SPIN_CAP; ++spin) { \
        UNROLL for (int p = 0; p < 4; ++p) asm volatile("ds_write_b128 %0, %1 offset:%2" :: "v"(od), "v"(o[t][p]), "i"(t * 16384 + p * 1024) : "memory"); \
        if (__all(csum(csum(csum(csum(tag, o[t][0]), o[t][1]), o[t][2]), o[t][3]) == oc[t])) break; \
        DG(3); asm volatile("s_waitcnt lgkmcnt(0)" ::: "memory"); OLOAD(t); OWAIT(t, 0); } } while (0)
    LAS char* od = sm + slot_addr(16) + lw * 4096 + l * 16;
    auto hint = [&](unsigned off) { const unsigned t = (unsigned)__builtin_amdgcn_s_memrealtime();
      __builtin_amdgcn_raw_buffer_store_b64(u32x2{tag, t}, rw, l < 16 && lw == 1 ? (unsigned)w * SLAB + S_H + off + l * 8 : 0x40000000u, 0, SC1); };
    auto until = [&](unsigned t) { _Pragma("unroll 1") for (int spin = 0; spin < 256; ++spin) if ((int)((unsigned)__builtin_amdgcn_s_memrealtime() - t) >= 0) break; };
    const char* hp[3];
    UNROLL for (int t = 0; t < 3; ++t) hp[t] = ws + oo[t] + S_H;
    u64 h0, h1, h2;
#define HLOAD() asm volatile("s_load_dwordx2 %0, %3, 0x0 glc\n\ts_load_dwordx2 %1, %4, 0x0 glc\n\ts_load_dwordx2 %2, %5, 0x0 glc" : "=&s"(h0), "=&s"(h1), "=&s"(h2) : "s"(hp[0]), "s"(hp[1]), "s"(hp[2]) : "memory")
#pragma unroll 1
    for (int i = 1; i < 4; ++i) unitW(i, rslot(i + 1));
    LBAR(8);
    if (lw == 0) {
      if (HL_AT == 0) HLOAD();
      unitW(4, rslot(5));
      if (HL_AT == 1) HLOAD();
      unitW(5, rslot(6));
      if (HL_AT == 2) HLOAD();
#pragma unroll 1
      for (unsigned spin = 0; spin < 4096; ++spin) {
        asm volatile("s_waitcnt lgkmcnt(0)" : "+s"(h0), "+s"(h1), "+s"(h2) :: "memory");
        DG(2);
        if ((unsigned)h0 == tag && (unsigned)h1 == tag && (unsigned)h2 == tag) break;
        HLOAD();
      }
      asm volatile("s_waitcnt lgkmcnt(0)" : "+s"(h0), "+s"(h1), "+s"(h2) :: "memory");
    } else { unitW(4, rslot(5)); unitW(5, rslot(6)); }
    LBAR(8);
    ST(4);
    OLOAD(0); OLOAD(1);
    LBAR(10);
    unitW(6, rslot(7));
    OPUT(0, 9);
    asm volatile("s_waitcnt lgkmcnt(0)\n\ts_barrier" ::: "memory"); LTS();
    unitW(7, L_P);
    OLOAD(2);
    if (H2_AT == 3) hint(128);
    OPUT(1, 13 + (H2_AT == 3));
    if (lw == 2) asm volatile("ds_write_b32 %0, %1" :: "v"(L_Q), "v"(0) : "memory");
    asm volatile("s_waitcnt vmcnt(%0) lgkmcnt(0)\n\ts_barrier" :: "i"(5 + (H2_AT == 3)) : "memory"); LTS();
    if (H2_AT == 4) hint(128);
    OPUT(2, 1);
    asm volatile("s_waitcnt lgkmcnt(0)\n\ts_barrier" ::: "memory"); LTS();
    ST(5);
    if (lw == 2) {
      const char* hb = ws + (size_t)(j * 8) * SLAB + S_H + 128;
      u64 h[8];
#pragma unroll 1
      for (unsigned spin = 0; spin < 4096; ++spin) {
        asm volatile("s_load_dwordx2 %0, %8, %9 glc\n\ts_load_dwordx2 %1, %8, %10 glc\n\ts_load_dwordx2 %2, %8, %11 glc\n\ts_load_dwordx2 %3, %8, %12 glc\n\t"
                     "s_load_dwordx2 %4, %8, %13 glc\n\ts_load_dwordx2 %5, %8, %14 glc\n\ts_load_dwordx2 %6, %8, %15 glc\n\ts_load_dwordx2 %7, %8, %16 glc\n\ts_waitcnt lgkmcnt(0)"
                     : "=&s"(h[0]), "=&s"(h[1]), "=&s"(h[2]), "=&s"(h[3]), "=&s"(h[4]), "=&s"(h[5]), "=&s"(h[6]), "=&s"(h[7])
                     : "s"(hb), "i"(0), "i"(SLAB), "i"(2 * SLAB), "i"(3 * SLAB), "i"(4 * SLAB), "i"(5 * SLAB), "i"(6 * SLAB), "i"(7 * SLAB) : "memory");
        unsigned d = 0; DG(0);
        UNROLL for (int i = 0; i < 8; ++i) d |= (unsigned)h[i] ^ tag;
        if (d == 0) break;
      }
      ST(7);
      const unsigned t0 = (unsigned)(h[0] >> 32); int m = 0;
      UNROLL for (int i = 1; i < 8; ++i) m = max(m, (int)((unsigned)(h[i] >> 32) - t0));
      until(t0 + m + C2);
      asm volatile("ds_write_b32 %0, %1" :: "v"(L_Q), "v"(1) : "memory");
    }
    if (lw == 0) {
      {
        const unsigned frag = ((hh >> 1) * 2 + (xcd >> 2)) * 2 + (hh & 1), pl = 32 * ((l >> 3) & 1) + 8 * (xcd & 3) + (l & 7);
        const unsigned pb = (unsigned)((b * 4 + (l >> 4)) * 8) * SLAB;
        u32x4 v[8];
        unsigned ad = pb + S_T + frag * 1024 + pl * 16, ac = pb + S_TC + frag * 256 + (xcd & 3) * 64 + (l & 15) * 4;
        asm volatile("s_sleep %2" : "+v"(ad), "+v"(ac) : "i"(T_DELAY) : "memory");
#pragma unroll 1
        for (unsigned spin = 0; spin < SPIN_CAP; ++spin) {
          asm volatile("" ::: "memory");
          unsigned c[8], bad = 0; DG(1);
          UNROLL for (int p = 0; p < 8; ++p) { v[p] = __builtin_amdgcn_raw_buffer_load_b128(rw, ad, p * SLAB, SC1);
                                               c[p] = __builtin_amdgcn_raw_buffer_load_b32(rw, ac, p * SLAB, SC1); }
          UNROLL for (int p = 0; p < 8; ++p) bad |= csum(tag, v[p]) ^ c[p];
          if (__all(bad == 0)) break;
          __builtin_amdgcn_s_sleep(BACKOFF);
        }
        if ((w & 31) == 0 && l == 0) st32(tag, rw, W_EPO + b * 256);
        float a[8] = {};
        UNROLL for (int p = 0; p < 8; ++p) UNROLL for (int i = 0; i < 4; ++i) { a[2 * i] += blo(v[p][i]); a[2 * i + 1] += bhi(v[p][i]); }
        float e[4];
        UNROLL for (int i = 0; i < 4; ++i) e[i] = sum16(a[i], a[4 + i]);
        const int n = 512 + 16 * hh + 8 * ((l >> 4) & 1) + 4 * ((l >> 3) & 1) + 2 * (l >> 5);
        *(unsigned*)(out + (size_t)(b * 64 + 8 * xcd + (l & 7)) * 1152 + n * 2) = pk(sum32(e[0], e[2]), sum32(e[1], e[3]));
      }
    }
    ST(6);
    return;
  }
  const int cw = wv - 4, r = l & 31, hf = l >> 5, kt = cw >> 1, qt = cw & 1;
  const unsigned slab = (unsigned)w * SLAB;
  const unsigned hoff = l < 16 && cw == 0 ? slab + S_H + l * 8 : 0x40000000u;
  if (PREF && cw == 0) {
    const char* p = (const char*)(pc & ~127ull) + ((j & 7) * (CODE_LINES / 8) + (l < CODE_LINES / 8 ? l : 0)) * 128;
    if (PREF & 2) {
      const int e = l & 1;
      if ((l >> 1) == 16) p = kc + kvo + e * 130944;
      if ((l >> 1) == 17) p = vc + kvo + e * 130944;
      if ((l >> 1) == 18) p = wo + (size_t)xcd * 512 * 1152 + e * 511 * 1152;
      if ((l >> 1) == 19) p = ws + b * 32 * SLAB + e * (32 * SLAB - 128);
      if ((l >> 1) == 20) p = out + (size_t)(b * 64 + 8 * xcd + e * 7) * 1152;
      if ((l >> 1) == 21) p = x + b * 73728 + e * 73600;
      if (l >= 48 && l < 53) p = wq + h * 256 + (size_t)(l - 48) * 143 * 8192;
    }
    asm volatile("s_mov_b32 m0, %1\n\ts_nop 0\n\tglobal_load_lds_dword %0, off" :: "v"(p), "i"(73728) : "memory");
  }
  const unsigned tag = *(const CAS unsigned*)(ws + W_EPO + b * 256) + 0x9E3779B9u;
  const int qq = (l & 15) >> 2, pp = l & 3, blk = (l >> 4) & 1;
  const int trc = 16 * ((4 * cw + 2 * blk + (pp >> 1)) ^ (qq << 2)) + 8 * (pp & 1);
  asm volatile("" :: "s"(tag));
  if (PREW) { __builtin_amdgcn_raw_buffer_store_b64(u32x2{~tag, ~tag}, rw, hoff, 0, SC1); __builtin_amdgcn_raw_buffer_store_b64(u32x2{~tag, ~tag}, rw, hoff + 128, 0, SC1); }
  {
    f32x16 qa0 = {}, qa1 = {};
    LAS char* aw = opq(sm + 8192 + (8 * hf + qq) * 256 + trc);
    LAS char* ax[4];
    UNROLL for (int s = 0; s < 4; ++s) ax[s] = sm + ((r * 128 + 16 * (hf ^ ((r >> 1) & 7))) ^ (32 * s));
    bf16x8 fa[2][4], f0[2][4], f1[2][4];
    UNROLL for (int i = 0; i < 9; ++i) {
      CBAR();
      const int so = (i % 3) * 24576, cur = i & 1, prv = cur ^ 1;
      UNROLL for (int s = 0; s < 4; ++s) {
        if (i > 0) { if (s & 1) qa1 = MFMA(fa[prv][s >> 1], f1[prv][s >> 1], qa1); else qa0 = MFMA(fa[prv][s >> 1], f0[prv][s >> 1], qa0); }
        FENCE();
        fa[cur][s] = rdtr(aw + so + s * 4096, 1024); f0[cur][s] = rd128(ax[s] + so); f1[cur][s] = rd128(ax[s] + so + 4096);
        FENCE();
      }
      if (i > 0) { UNROLL for (int s = 2; s < 4; ++s) { qa0 = MFMA(fa[prv][s], f0[prv][s], qa0); qa1 = MFMA(fa[prv][s], f1[prv][s], qa1); } }
    }
    FENCE();
    UNROLL for (int s = 0; s < 4; ++s) qa0 = MFMA(fa[0][s], f0[0][s], qa0);
    FENCE();
    UNROLL for (int s = 0; s < 4; ++s) qa1 = MFMA(fa[0][s], f1[0][s], qa1);
    FENCE();
    LAS char* q = opq(sm + L_Q + cw * 4096 + r * 16 + 8 * hf);
    UNROLL for (int s = 0; s < 2; ++s) { const u32x4 p0 = pk8(qa0, s); *(LAS u32x2*)(q + s * 2048) = u32x2{p0.x, p0.y}; *(LAS u32x2*)(q + s * 2048 + 512) = u32x2{p0.z, p0.w}; }
    FENCE();
    UNROLL for (int s = 0; s < 2; ++s) { const u32x4 p1 = pk8(qa1, s); *(LAS u32x2*)(q + s * 2048 + 1024) = u32x2{p1.x, p1.y}; *(LAS u32x2*)(q + s * 2048 + 1536) = u32x2{p1.z, p1.w}; }
    asm volatile("s_waitcnt vmcnt(0)" ::: "memory");
  }
  ST(1);
  constexpr float CS = 0.08838834764831845f * 1.4426950408889634f;
  f32x16 S[8];
  float mu[8], su[8];
  LAS char* pr = opq(sm + L_P + l * 16);
  LAS char* pq = opq(sm + L_Q + qt * 1024 + l * 16);
#define SM_MAX(u) do { float a_ = max3(S[u][0], S[u][1], S[u][2]), b_ = max3(S[u][3], S[u][4], S[u][5]); \
    a_ = max3(a_, S[u][6], S[u][7]); b_ = max3(b_, S[u][8], S[u][9]); a_ = max3(a_, S[u][10], S[u][11]); b_ = max3(b_, S[u][12], S[u][13]); \
    a_ = max3(a_, S[u][14], S[u][15]); mu[u] = -CS * max2(a_, b_); PIN(mu[u]); } while (0)
#define SM_F(u) do { UNROLL for (int i = 0; i < 16; i += 2) { f32x2 t_ = {S[u][i], S[u][i + 1]}; t_ = __builtin_elementwise_fma(t_, f32x2{CS, CS}, f32x2{mu[u], mu[u]}); PIN(t_); S[u][i] = t_.x; S[u][i + 1] = t_.y; } } while (0)
#define SM_E(u, i) do { f32x2 t_ = {__builtin_amdgcn_exp2f(S[u][i]), __builtin_amdgcn_exp2f(S[u][i + 1])}; PIN(t_); S[u][i] = t_.x; S[u][i + 1] = t_.y; } while (0)
#define SM_A(u) do { f32x2 a_ = f32x2{S[u][0], S[u][1]} + f32x2{S[u][2], S[u][3]}, b_ = f32x2{S[u][4], S[u][5]} + f32x2{S[u][6], S[u][7]}; \
    a_ += f32x2{S[u][8], S[u][9]}; b_ += f32x2{S[u][10], S[u][11]}; a_ += f32x2{S[u][12], S[u][13]}; b_ += f32x2{S[u][14], S[u][15]}; a_ += b_; su[u] = a_.x + a_.y; PIN(su[u]); } while (0)
  {
    bf16x8 qf[8];
    LAS char* ak[8];
    UNROLL for (int s = 0; s < 8; ++s) ak[s] = opq(sm + 98304 + (((32 * kt + r) * 256 + 16 * (hf ^ (r & 15))) ^ (32 * s)));
    UNROLL for (int u = 0; u < 8; ++u) {
      CBAR();
      bf16x8 kf[8];
      f32x16 a = {};
      const int v = u > 0 ? u - 1 : 0, v2 = u > 1 ? u - 2 : 0;
      UNROLL for (int s = 0; s < 4; ++s) { if (u == 0) qf[s] = rd128(pq + s * 2048); kf[s] = rd128(ak[s] + (u & 3) * 16384); }
      FENCE();
      if (u > 1) SM_A(v2);
      FENCE();
      if (u > 0) { SM_MAX(v); SM_F(v); }
      UNROLL for (int s = 0; s < 8; ++s) {
        MF(a, kf[s], qf[s]);
        if (s < 4) { if (u == 0) qf[s + 4] = rd128(pq + (s + 4) * 2048); kf[s + 4] = rd128(ak[s + 4] + (u & 3) * 16384); }
        if (u > 0) SM_E(v, 2 * s);
        if (u == 3 && s >= 4) { ak[2 * s - 8] = opq(ak[2 * s - 8] - 98304); ak[2 * s - 7] = opq(ak[2 * s - 7] - 98304); }
      }
      S[u] = a;
    }
  }
  LAS f32x2* stm = (LAS f32x2*)opq(sm + L_Q + r * 8);
  {
    FENCE();
    float mw = min3(min3(mu[0], mu[1], mu[2]), min3(mu[3], mu[4], mu[5]), mu[6]);
    SM_A(6);
    FENCE();
    SM_MAX(7);
    mw = swap32min(min2(mw, mu[7]));
    FENCE();
    SM_F(7);
    UNROLL for (int i = 0; i < 16; i += 2) SM_E(7, i);
    UNROLL for (int u = 0; u < 8; ++u) mu[u] = __builtin_amdgcn_exp2f(mw - mu[u]);
    FENCE();
    SM_A(7);
    float lw_ = mu[0] * su[0], lx = mu[1] * su[1];
    UNROLL for (int u = 2; u < 8; u += 2) { lw_ = __builtin_fmaf(mu[u], su[u], lw_); lx = __builtin_fmaf(mu[u + 1], su[u + 1], lx); }
    lw_ = sum32(lw_ + lx, lw_ + lx);
    stm[cw * 32] = f32x2{mw, lw_};
    CBAR();
    ST(2);
    const f32x2 ot = stm[(cw ^ 2) * 32];
    const float mg = min2(mw, ot.x), ea = __builtin_amdgcn_exp2f(mg - mw), eb = __builtin_amdgcn_exp2f(mg - ot.x);
    const float g = ea * __builtin_amdgcn_rcpf(__builtin_fmaf(ea, lw_, eb * ot.y));
    UNROLL for (int u = 0; u < 8; ++u) mu[u] *= g;
  }
  LAS char* pw = opq(sm + L_P + kt * 4096 + qt * 1024 + l * 16);
  unsigned pd[8];
#define PV_C(u, k) do { float x_ = S[u][2 * (k)] * mu[u]; PIN(x_); float y_ = S[u][2 * (k) + 1] * mu[u]; PIN(y_); pd[k] = pk(x_, y_); PIN(pd[k]); } while (0)
#define PV_C2(u, k) do { float x_ = S[u][2 * (k)] * mu[u], y_ = S[u][2 * (k) + 1] * mu[u], z_ = S[u][2 * (k) + 2] * mu[u], w_ = S[u][2 * (k) + 3] * mu[u]; \
    asm volatile("" : "+v"(x_), "+v"(z_)); asm volatile("" : "+v"(y_), "+v"(w_)); pd[k] = pk(x_, y_); pd[k + 1] = pk(z_, w_); asm volatile("" : "+v"(pd[k]), "+v"(pd[k + 1])); } while (0)
#define PV_W(u, h) do { *(LAS u32x4*)(pw + ((u) & 1) * 8192 + (h) * 2048) = u32x4{pd[4 * (h)], pd[4 * (h) + 1], pd[4 * (h) + 2], pd[4 * (h) + 3]}; FENCE(); } while (0)
  S[0] *= mu[0];
  *(LAS u32x4*)pw = pk8(S[0], 0); *(LAS u32x4*)(pw + 2048) = pk8(S[0], 1);
  FENCE();
  S[1] *= mu[1];
  UNROLL for (int k = 0; k < 8; ++k) { pd[k] = pk(S[1][2 * k], S[1][2 * k + 1]); PIN(pd[k]); }
  f32x16 o0 = {}, o1 = {};
  LAS char* av1 = opq(sm + (4 * hf + qq) * 256 + trc);
  LAS char* av0 = opq(av1 + 98304);
  UNROLL for (int u = 0; u < 7; ++u) {
    CBAR();
    bf16x8 va[4], p0[4], p1[4];
    const int v = u < 6 ? u + 2 : 7;
#define PV_RD(ks) do { va[ks] = rdtr((u < 4 ? av0 : av1) + (u & 3) * 16384 + (ks) * 4096, 2048); \
      p0[ks] = rd128(pr + (u & 1) * 8192 + (ks) * 2048); p1[ks] = rd128(pr + (u & 1) * 8192 + (ks) * 2048 + 1024); FENCE(); } while (0)
    PV_RD(0); PV_RD(1);
    MF(o0, va[0], p0[0]); PV_RD(2); MF(o1, va[0], p1[0]); PV_RD(3);
    MF(o0, va[1], p0[1]); PV_W(u + 1, 0); MF(o1, va[1], p1[1]); PV_W(u + 1, 1);
    MF(o0, va[2], p0[2]); if (u < 6) PV_C2(v, 0);
    MF(o1, va[2], p1[2]); if (u < 6) PV_C2(v, 2);
    MF(o0, va[3], p0[3]); if (u < 6) PV_C2(v, 4);
    MF(o1, va[3], p1[3]); if (u < 6) PV_C2(v, 6);
  }
  {
    CBAR();
    bf16x8 va[4], p0[4], p1[4];
    UNROLL for (int ks = 0; ks < 6; ++ks) {
      if (ks >= 2) { o0 = MFMA(va[ks - 2], p0[ks - 2], o0); FENCE(); }
      if (ks < 4) { va[ks] = rdtr(av1 + 49152 + ks * 4096, 2048); p0[ks] = rd128(pr + 8192 + ks * 2048); }
      else { p1[2 * ks - 8] = rd128(pr + 8192 + (2 * ks - 8) * 2048 + 1024); p1[2 * ks - 7] = rd128(pr + 8192 + (2 * ks - 7) * 2048 + 1024); }
      FENCE();
    }
    const int off = cw * 4096 + l * 16;
    LAS char* d = opq(sm + L_Q + off);
    o1 = MFMA(va[0], p1[0], o1); o1 = MFMA(va[1], p1[1], o1);
    FENCE();
    const u32x4 a0 = pk8(o0, 0);
    st128(a0, rw, slab + S_O + off);
    FENCE();
    o1 = MFMA(va[2], p1[2], o1);
    FENCE();
    const u32x4 a1 = pk8(o0, 1);
    st128(a1, rw, slab + S_O + off + 2048);
    FENCE();
    o1 = MFMA(va[3], p1[3], o1);
    FENCE();
    unsigned ck = csum(csum(tag, a0), a1); PIN(ck);
    *(LAS u32x4*)d = a0;
    FENCE();
    const u32x4 b0 = pk8(o1, 0), b1 = pk8(o1, 1);
    st128(b0, rw, slab + S_O + off + 1024); st128(b1, rw, slab + S_O + off + 3072);
    st32(csum(csum(ck, b0), b1), rw, slab + S_OC + cw * 256 + l * 4);
    __builtin_amdgcn_raw_buffer_store_b64(u32x2{tag, tag}, rw, hoff, 0, SC1);
    FENCE();
    *(LAS u32x4*)(d + 2048) = a1; *(LAS u32x4*)(d + 1024) = b0; *(LAS u32x4*)(d + 3072) = b1;
  }
  ST(3);
  bf16x8 wf[32];
  LAS char* av2 = opq(av1 + L_P);
  LAS char* po = opq(pr + (slot_addr(16) - L_P));
  auto loadW1 = [&](int i, int ks) { wf[4 * i + ks] = rdtr((i < 3 ? av0 + (i + 1) * 16384 : i < 7 ? av1 + (i - 3) * 16384 : av2) + ks * 4096, 2048); };
  f32x16 c0 = {}, c1 = {};
  auto head = [&](int t, LAS char* p, int wnext) {
    bf16x8 g0[8], g1[8];
    UNROLL for (int ks = 0; ks < 10; ++ks) {
      if (ks >= 2) MF(c0, wf[8 * t + ks - 2], g0[ks - 2]);
      if (ks < 8) { g0[ks] = rd128(p + ks * 2048); g1[ks] = rd128(p + ks * 2048 + 1024); FENCE(); }
      if (ks >= 2) { MF(c1, wf[8 * t + ks - 2], g1[ks - 2]);
        if (wnext >= 0) { loadW1(wnext + ((ks - 2) >> 2), (ks - 2) & 3); FENCE(); } }
    }
  };
  CBAR();
  u32x4 t0, t1;
  {
    f32x16 t = {};
    LAS char* at = opq(sm + slot_addr(16) + (4 * hf + qq) * 128 + 16 * ((4 * kt + 2 * blk + (pp >> 1)) ^ ((qq >> 1) << 2)) + 8 * (pp & 1));
    bf16x8 ta[8], tb[8];
    UNROLL for (int ks = 0; ks < 11; ++ks) {
      if (ks >= 3) MF(t, ta[ks - 3], tb[ks - 3]);
      if (ks < 8) { ta[ks] = rdtr(at + ks * 2048, 1024); tb[ks] = rd128(pq + ks * 2048); }
      if (ks >= 7) { loadW1((ks - 7) >> 1, ((ks - 7) & 1) * 2); loadW1((ks - 7) >> 1, ((ks - 7) & 1) * 2 + 1); }
      FENCE();
    }
    head(0, pr + (L_Q - L_P), -1);
    t0 = pk8(t, 0); t1 = pk8(t, 1);
  }
  CBAR();
  UNROLL for (int i = 0; i < 8; ++i) loadW1(2 + (i >> 2), i & 3);
  unsigned tc0 = csum(tag, t0), tc1 = csum(tag, t1);
  const unsigned cl = ((r >> 3) * 16 + hf * 8 + (r & 7)) * 4;
  unsigned aT = slab + S_T + cw * 2048 + l * 16, aTC = slab + S_TC + cw * 512 + cl, aM = slab + S_M + cw * 4096 + l * 16, aMC = slab + S_MC + cw * 512 + cl;
  const unsigned tile = cw * 2 + (xcd >> 2), pb = (unsigned)(j * 8 + 2 * (l >> 4)) * SLAB;
  unsigned aR = pb + S_M + tile * 2048 + (32 * ((l >> 3) & 1) + 8 * (xcd & 3) + (l & 7)) * 16, aRC = pb + S_MC + tile * 256 + (xcd & 3) * 64 + (l & 15) * 4;
  unsigned aO = (unsigned)(b * 64 + 8 * xcd + (l & 7)) * 1152 + (128 * hh + 32 * cw + 16 * blk + 8 * hf + 4 * ((l >> 3) & 1)) * 2;
  asm volatile("" : "+v"(tc0), "+v"(tc1), "+v"(aT), "+v"(aTC), "+v"(aM), "+v"(aMC), "+v"(aR), "+v"(aRC), "+v"(aO));
  CBAR();
  UNROLL for (int i = 0; i < 8; ++i) loadW1(4 + (i >> 2), i & 3);
  CBAR();
  st128(t0, rw, aT); st128(t1, rw, aT + 1024); st32(tc0, rw, aTC); st32(tc1, rw, aTC + 256);
  FENCE();
  head(1, po, -1);
  CBAR(); head(2, po + 16384, 6);
  CBAR(); head(3, po + 32768, -1);
  ST(4);
  {
    const u32x4 a0 = pk8(c0, 0), a1 = pk8(c0, 1);
    st128(a0, rw, aM); st128(a1, rw, aM + 1024);
    FENCE();
    const u32x4 b0 = pk8(c1, 0), b1 = pk8(c1, 1);
    st128(b0, rw, aM + 2048); st128(b1, rw, aM + 3072);
    FENCE();
    st32(csum(csum(tag, a0), a1), rw, aMC); st32(csum(csum(tag, b0), b1), rw, aMC + 256);
  }
  ST(5);
  {
#pragma unroll 1
    for (unsigned spin = 0; spin < SPIN_CAP; ++spin) if (__builtin_amdgcn_readfirstlane(*(volatile LAS unsigned*)(sm + L_Q))) break;
    ST(7);
    u32x4 v[2][2];
#pragma unroll 1
    for (unsigned spin = 0; spin < SPIN_CAP; ++spin) {
      asm volatile("" ::: "memory");
      unsigned c[2]; DG(1);
      UNROLL for (int p = 0; p < 2; ++p) {
        v[p][0] = __builtin_amdgcn_raw_buffer_load_b128(rw, aR, p * SLAB, SC1);
        v[p][1] = __builtin_amdgcn_raw_buffer_load_b128(rw, aR + 1024, p * SLAB, SC1);
        c[p] = __builtin_amdgcn_raw_buffer_load_b32(rw, aRC, p * SLAB, SC1);
      }
      if (__all(((csum(csum(tag, v[0][0]), v[0][1]) ^ c[0]) | (csum(csum(tag, v[1][0]), v[1][1]) ^ c[1])) == 0)) break;
      __builtin_amdgcn_s_sleep(BACKOFF);
    }
    float e[8];
    UNROLL for (int i = 0; i < 4; ++i) {
      e[2 * i] = sum16(blo(v[0][0][i]) + blo(v[1][0][i]), blo(v[0][1][i]) + blo(v[1][1][i]));
      e[2 * i + 1] = sum16(bhi(v[0][0][i]) + bhi(v[1][0][i]), bhi(v[0][1][i]) + bhi(v[1][1][i]));
    }
    *(u32x2*)(out + aO) = u32x2{pk(sum32(e[0], e[4]), sum32(e[1], e[5])), pk(sum32(e[2], e[6]), sum32(e[3], e[7]))};
  }
  ST(6);
}

extern "C" void kernel_launch(void* const* d_in, const int* in_sizes, int n_in, void* d_out, int out_size, void* d_ws, size_t ws_size, hipStream_t stream) {
  gqa_fused<<<256, 512, 0, stream>>>((const char*)d_in[0], (const char*)d_in[1], (const char*)d_in[2], (const char*)d_in[3], (const char*)d_in[4], (char*)d_out, (char*)d_ws);
}
```

```cpp
#define STAMP
#include <hip/hip_runtime.h>
#include <hip/hip_bf16.h>

typedef unsigned long long u64;
typedef short bf16x8 __attribute__((ext_vector_type(8)));
typedef short s16x4 __attribute__((ext_vector_type(4)));
typedef float f32x16 __attribute__((ext_vector_type(16)));
typedef float f32x2 __attribute__((ext_vector_type(2)));
typedef __bf16 bf16x2 __attribute__((ext_vector_type(2)));
typedef unsigned u32x4 __attribute__((ext_vector_type(4)));
typedef unsigned u32x2 __attribute__((ext_vector_type(2)));
#define LAS __attribute__((address_space(3)))
#define CAS __attribute__((address_space(4)))
#define DI __device__ __forceinline__
typedef __amdgpu_buffer_rsrc_t rsrc_t;

constexpr int L_P = 65536, L_Q = 81920;
constexpr int slot_addr(int g) { return g == 24 ? L_P : (g & 7) < 4 ? 98304 + (g & 7) * 16384 : ((g & 7) - 4) * 16384; }
constexpr unsigned S_O = 0, S_OC = 16384, S_T = 17408, S_TC = 25600, S_M = 27648, S_MC = 44032, S_H = 46080, SLAB = 46592;
constexpr unsigned W_EPO = 12u << 20, W_BYTES = W_EPO + 4096;
constexpr int SC1 = 16, NT = 2;
constexpr unsigned SPIN_CAP = 1u << 20;
#define TOUCH_AT 104
#define TOUCH_POL 2
#define W_POL 2
#define RETOUCH 7
#define RE_POL 16
#define T1_AT 3
#define H2_DELAY 8
#define P2_DELAY 4
#define C1 110
#define C2 200
#define HL_AT 2
#define PREW 1
#define BACKOFF 4
#define H2_AT 3
#define T_DELAY 14
#define PREF 3
#define CODE_LINES 152

#define DG(i) do {} while (0)
#define STAMP_OFF (32u << 20)
#define STAMP_N 256
#define STAMP_W 16
#define ST(i) do { st_[i] = __builtin_amdgcn_s_memrealtime(); } while (0)

DI rsrc_t mk(const void* p, unsigned n) { return __builtin_amdgcn_make_buffer_rsrc((void*)p, 0, n, 0x00020000); }
#define DMA(R, LDSOFF, V, SOFF, IMM, POL) __builtin_amdgcn_raw_ptr_buffer_load_lds(R, (LAS void*)(sm + (LDSOFF)), 16, V, SOFF, IMM, POL)
#define FENCE() __builtin_amdgcn_sched_barrier(0)
#define LTS() do {} while (0)
#define LBAR(N) do { asm volatile("s_waitcnt vmcnt(%0)\n\ts_barrier" :: "i"(N) : "memory"); LTS(); } while (0)
#define CBAR() CBAR0()
#define CBAR0() do { FENCE(); asm volatile("" ::: "memory"); __builtin_amdgcn_s_waitcnt(0xC07F); __builtin_amdgcn_s_barrier(); asm volatile("" ::: "memory"); FENCE(); } while (0)
#define PIN(x) asm volatile("" : "+v"(x))
DI LAS char* opq(LAS char* p) { asm("" : "+v"(p)); return p; }
#define MFMA(a, b, c) __builtin_amdgcn_mfma_f32_32x32x16_bf16(a, b, c, 0, 0, 0)
#define MF(acc, a, b) do { FENCE(); acc = MFMA(a, b, acc); PIN(acc); FENCE(); } while (0)
#define UNROLL _Pragma("unroll")
DI bf16x8 rd128(LAS char* p) { return *(LAS bf16x8*)p; }
DI bf16x8 rdtr(LAS char* p, int step) {
  s16x4 a = __builtin_amdgcn_ds_read_tr16_b64_v4i16((LAS s16x4*)p), b = __builtin_amdgcn_ds_read_tr16_b64_v4i16((LAS s16x4*)(p + step));
  return __builtin_shufflevector(a, b, 0, 1, 2, 3, 4, 5, 6, 7);
}
DI unsigned pk(float a, float b) { f32x2 v = {a, b}; return __builtin_bit_cast(unsigned, __builtin_convertvector(v, bf16x2)); }
DI u32x4 pk8(const f32x16& x, int s) { return u32x4{pk(x[8 * s], x[8 * s + 1]), pk(x[8 * s + 2], x[8 * s + 3]), pk(x[8 * s + 4], x[8 * s + 5]), pk(x[8 * s + 6], x[8 * s + 7])}; }
DI float max2(float a, float b) { return __builtin_elementwise_maximum(a, b); }
DI float max3(float a, float b, float c) { return max2(max2(a, b), c); }
DI float min2(float a, float b) { return __builtin_elementwise_minimum(a, b); }
DI float min3(float a, float b, float c) { return min2(min2(a, b), c); }
DI float swap32min(float v) { auto r = __builtin_amdgcn_permlane32_swap(__float_as_uint(v), __float_as_uint(v), false, false); return min2(__uint_as_float(r[0]), __uint_as_float(r[1])); }
DI float swap32max(float v) { auto r = __builtin_amdgcn_permlane32_swap(__float_as_uint(v), __float_as_uint(v), false, false); return max2(__uint_as_float(r[0]), __uint_as_float(r[1])); }
DI float sum16(float a, float b) { auto r = __builtin_amdgcn_permlane16_swap(__float_as_uint(a), __float_as_uint(b), false, false); return __uint_as_float(r[0]) + __uint_as_float(r[1]); }
DI float sum32(float a, float b) { auto r = __builtin_amdgcn_permlane32_swap(__float_as_uint(a), __float_as_uint(b), false, false); return __uint_as_float(r[0]) + __uint_as_float(r[1]); }
DI unsigned csum(unsigned c, u32x4 v) { return (c + v.x + v.y) + v.z + v.w; }
DI float blo(unsigned v) { return __uint_as_float(v << 16); }
DI float bhi(unsigned v) { return __uint_as_float(v & 0xffff0000u); }
DI void st128(u32x4 v, rsrc_t r, unsigned off) { __builtin_amdgcn_raw_buffer_store_b128(v, r, off, 0, SC1); }
DI void st32(unsigned v, rsrc_t r, unsigned off) { __builtin_amdgcn_raw_buffer_store_b32(v, r, off, 0, SC1); }

__global__ __launch_bounds__(512, 2) void gqa_fused(const char* __restrict__ x, const char* __restrict__ kc, const char* __restrict__ vc,
                                                    const char* __restrict__ wq, const char* __restrict__ wo, char* __restrict__ out, char* ws) {
  __shared__ __attribute__((aligned(256))) char smem[163840];
  LAS char* sm = (LAS char*)smem;
  u64 pc;
  asm volatile("s_getpc_b64 %0" : "=s"(pc));
  const int w = blockIdx.x, wv = __builtin_amdgcn_readfirstlane(threadIdx.x >> 6), l = threadIdx.x & 63;
  const int xcd = w & 7, j = w >> 3, b = j >> 2, hh = j & 3, h = xcd * 4 + hh;
  u64 st_[8] = {};
  ST(0);
  asm volatile("" :: "s"(x), "s"(kc), "s"(vc), "s"(wq), "s"(wo), "s"(out), "s"(ws));
  const rsrc_t rw = mk(ws, W_BYTES);
  const size_t kvo = (size_t)(b * 16 + (h >> 1)) * 131072;
  if (__builtin_expect(wv < 4, 1)) {
    const int lw = wv;
    LTS();
    const rsrc_t rx = mk(x + b * 73728, 73728), rq = mk(wq + h * 256, 576 * 8192 - h * 256);
    const unsigned vx0 = (16 * lw + (l >> 3)) * 1152 + 16 * ((l & 7) ^ (l >> 4));
    const unsigned vx1 = (16 * lw + 8 + (l >> 3)) * 1152 + 16 * ((l & 7) ^ (4 + (l >> 4)));
    const unsigned sw4 = 16 * ((l & 15) ^ ((l >> 4) << 2));
    const unsigned vq = (16 * lw + (l >> 4)) * 8192 + sw4;
    {
      int c = (b + 2 * hh) & 7, so = 0;
#pragma unroll 1
      for (int i = 0; i < 9; ++i) {
        if (i >= 2) LBAR(6);
        DMA(rx, so + lw * 2048, vx0, c * 128, 0, 0);
        DMA(rx, so + lw * 2048 + 1024, vx1, c * 128, 0, 0);
        UNROLL for (int p = 0; p < 4; ++p) DMA(rq, so + 8192 + lw * 4096 + p * 1024, vq, (c * 64 + p * 4) * 8192, 0, NT);
        if (i == TOUCH_AT - 100) __builtin_amdgcn_raw_ptr_buffer_load_lds(mk(wo + (size_t)xcd * 512 * 1152, 512 * 1152), (LAS void*)(sm + 74752 + lw * 256), 4, (j * 144 + lw * 36 + (l < 36 ? l : 35)) * 128, 0, 0, TOUCH_POL);
        if (i == RETOUCH) __builtin_amdgcn_raw_ptr_buffer_load_lds(mk(wo + (size_t)xcd * 512 * 1152, 512 * 1152), (LAS void*)(sm + 74752 + lw * 256), 4, (j * 144 + lw * 36 + (l < 36 ? l : 35)) * 128, 0, 0, RE_POL);
        c = c == 8 ? 0 : c + 1;
        so = so == 49152 ? 0 : so + 24576;
      }
    }
    const unsigned tag = *(const CAS unsigned*)(ws + W_EPO + b * 256) + 0x9E3779B9u;
    const rsrc_t rk = mk(kc + kvo, 131072), rv = mk(vc + kvo, 131072), ro = mk(wo + (size_t)xcd * 512 * 1152, 512 * 1152);
    const unsigned vk = (4 * lw + (l >> 4)) * 256 + 16 * ((l & 15) ^ (4 * lw + (l >> 4)));
    const unsigned vv = lw * 4096 + (l >> 4) * 256 + sw4;
    const unsigned vo = (16 * lw + (l >> 4)) * 1152 + hh * 256 + sw4;
    const unsigned vt = (128 * hh + 32 * lw + (l >> 3)) * 1152 + 1024 + 16 * ((l & 7) ^ (((l >> 4) & 1) << 2));
    auto rslot = [](int g) { return (g & 4) ? (g & 3) * 16384 : 98304 + (g & 3) * 16384; };
    auto unitK = [&](int u) { const int s = rslot(u) + lw * 1024; UNROLL for (int p = 0; p < 4; ++p) DMA(rk, s + p * 4096, vk, u * 16384 + p * 4096, 0, NT); };
    auto unitV = [&](int u) { const int s = rslot(u) + lw * 4096;
      DMA(rv, s, vv, u * 16384, 0, NT); DMA(rv, s, vv, u * 16384, 1024, NT); DMA(rv, s, vv, u * 16384, 2048, NT); DMA(rv, s, vv, u * 16384, 3072, NT); };
    auto unitW = [&](int i, int s) {
      const int row0 = ((2 * hh + i) & 7) * 64;
      UNROLL for (int p = 0; p < 4; ++p) DMA(ro, s + lw * 4096 + p * 1024, vo, (row0 + p * 4) * 1152, 0, W_POL);
    };
    LBAR(6); unitK(0);
    LBAR(4); unitK(1); unitK(2); unitK(3);
    asm volatile("" :: "s"(tag));
    ST(1);
#pragma unroll 1
    for (int u = 4; u < 8; ++u) { LBAR(12); unitK(u); }
    LBAR(12);
#pragma unroll 1
    for (int u = 0; u < 8; ++u) {
      if (u < 3) LBAR(8); else if (u > 4) LBAR(16);
      unitV(u);
      if (u == 3) { ST(2); asm volatile("s_barrier" ::: "memory"); }
    }
    LBAR(16);
    LBAR(12);
    LBAR(8);
    UNROLL for (int p = 0; p < 4; ++p) DMA(ro, slot_addr(16) + lw * 4096 + p * 1024, vt, p * 8 * 1152, 0, W_POL);
    LBAR(8); unitW(0, slot_addr(17));
    LBAR(8);
    ST(3);
    u32x4 o[3][4]; unsigned oc[3], oo[3];
    UNROLL for (int t = 0; t < 3; ++t) oo[t] = (unsigned)(((b * 4 + ((hh + 1 + t) & 3)) * 8 + xcd)) * SLAB;
    const unsigned vl = S_O + lw * 4096 + l * 16, vc2 = S_OC + lw * 256 + l * 4;
#define OLOAD(t) asm volatile("buffer_load_dwordx4 %0, %5, %7, %8 offen sc1\n\tbuffer_load_dwordx4 %1, %5, %7, %8 offen offset:1024 sc1\n\t" \
                              "buffer_load_dwordx4 %2, %5, %7, %8 offen offset:2048 sc1\n\tbuffer_load_dwordx4 %3, %5, %7, %8 offen offset:3072 sc1\n\t" \
                              "buffer_load_dword %4, %6, %7, %8 offen sc1" \
                              : "=&v"(o[t][0]), "=&v"(o[t][1]), "=&v"(o[t][2]), "=&v"(o[t][3]), "=&v"(oc[t]) : "v"(vl), "v"(vc2), "s"(rw), "s"(oo[t]) : "memory")
#define OWAIT(t, N) asm volatile("s_waitcnt vmcnt(%5)" : "+v"(o[t][0]), "+v"(o[t][1]), "+v"(o[t][2]), "+v"(o[t][3]), "+v"(oc[t]) : "i"(N) : "memory")
#define OPUT(t, N) do { OWAIT(t, N); \
      _Pragma("unroll 1") for (unsigned spin = 0; spin < SPIN_CAP; ++spin) { \
        UNROLL for (int p = 0; p < 4; ++p) asm volatile("ds_write_b128 %0, %1 offset:%2" :: "v"(od), "v"(o[t][p]), "i"(t * 16384 + p * 1024) : "memory"); \
        if (__all(csum(csum(csum(csum(tag, o[t][0]), o[t][1]), o[t][2]), o[t][3]) == oc[t])) break; \
        DG(3); asm volatile("s_waitcnt lgkmcnt(0)" ::: "memory"); OLOAD(t); OWAIT(t, 0); } } while (0)
    LAS char* od = sm + slot_addr(16) + lw * 4096 + l * 16;
    auto hint = [&](unsigned off) { const unsigned t = (unsigned)__builtin_amdgcn_s_memrealtime();
      __builtin_amdgcn_raw_buffer_store_b64(u32x2{tag, t}, rw, l < 16 && lw == 1 ? (unsigned)w * SLAB + S_H + off + l * 8 : 0x40000000u, 0, SC1); };
    auto until = [&](unsigned t) { _Pragma("unroll 1") for (int spin = 0; spin < 256; ++spin) if ((int)((unsigned)__builtin_amdgcn_s_memrealtime() - t) >= 0) break; };
    const char* hp[3];
    UNROLL for (int t = 0; t < 3; ++t) hp[t] = ws + oo[t] + S_H;
    u64 h0, h1, h2;
#define HLOAD() asm volatile("s_load_dwordx2 %0, %3, 0x0 glc\n\ts_load_dwordx2 %1, %4, 0x0 glc\n\ts_load_dwordx2 %2, %5, 0x0 glc" : "=&s"(h0), "=&s"(h1), "=&s"(h2) : "s"(hp[0]), "s"(hp[1]), "s"(hp[2]) : "memory")
#pragma unroll 1
    for (int i = 1; i < 4; ++i) unitW(i, rslot(i + 1));
    LBAR(8);
    if (lw == 0) {
      if (HL_AT == 0) HLOAD();
      unitW(4, rslot(5));
      if (HL_AT == 1) HLOAD();
      unitW(5, rslot(6));
      if (HL_AT == 2) HLOAD();
#pragma unroll 1
      for (unsigned spin = 0; spin < 4096; ++spin) {
        asm volatile("s_waitcnt lgkmcnt(0)" : "+s"(h0), "+s"(h1), "+s"(h2) :: "memory");
        DG(2);
        if ((unsigned)h0 == tag && (unsigned)h1 == tag && (unsigned)h2 == tag) break;
        HLOAD();
      }
      asm volatile("s_waitcnt lgkmcnt(0)" : "+s"(h0), "+s"(h1), "+s"(h2) :: "memory");
    } else { unitW(4, rslot(5)); unitW(5, rslot(6)); }
    LBAR(8);
    ST(4);
    OLOAD(0); OLOAD(1);
    LBAR(10);
    unitW(6, rslot(7));
    OPUT(0, 9);
    asm volatile("s_waitcnt lgkmcnt(0)\n\ts_barrier" ::: "memory"); LTS();
    unitW(7, L_P);
    OLOAD(2);
    if (H2_AT == 3) hint(128);
    OPUT(1, 13 + (H2_AT == 3));
    if (lw == 2) asm volatile("ds_write_b32 %0, %1" :: "v"(L_Q), "v"(0) : "memory");
    asm volatile("s_waitcnt vmcnt(%0) lgkmcnt(0)\n\ts_barrier" :: "i"(5 + (H2_AT == 3)) : "memory"); LTS();
    if (H2_AT == 4) hint(128);
    OPUT(2, 1);
    asm volatile("s_waitcnt lgkmcnt(0)\n\ts_barrier" ::: "memory"); LTS();
    ST(5);
    if (lw == 2) {
      const char* hb = ws + (size_t)(j * 8) * SLAB + S_H + 128;
      u64 h[8];
#pragma unroll 1
      for (unsigned spin = 0; spin < 4096; ++spin) {
        asm volatile("s_load_dwordx2 %0, %8, %9 glc\n\ts_load_dwordx2 %1, %8, %10 glc\n\ts_load_dwordx2 %2, %8, %11 glc\n\ts_load_dwordx2 %3, %8, %12 glc\n\t"
                     "s_load_dwordx2 %4, %8, %13 glc\n\ts_load_dwordx2 %5, %8, %14 glc\n\ts_load_dwordx2 %6, %8, %15 glc\n\ts_load_dwordx2 %7, %8, %16 glc\n\ts_waitcnt lgkmcnt(0)"
                     : "=&s"(h[0]), "=&s"(h[1]), "=&s"(h[2]), "=&s"(h[3]), "=&s"(h[4]), "=&s"(h[5]), "=&s"(h[6]), "=&s"(h[7])
                     : "s"(hb), "i"(0), "i"(SLAB), "i"(2 * SLAB), "i"(3 * SLAB), "i"(4 * SLAB), "i"(5 * SLAB), "i"(6 * SLAB), "i"(7 * SLAB) : "memory");
        unsigned d = 0; DG(0);
        UNROLL for (int i = 0; i < 8; ++i) d |= (unsigned)h[i] ^ tag;
        if (d == 0) break;
      }
      ST(7);
      const unsigned t0 = (unsigned)(h[0] >> 32); int m = 0;
      UNROLL for (int i = 1; i < 8; ++i) m = max(m, (int)((unsigned)(h[i] >> 32) - t0));
      until(t0 + m + C2);
      asm volatile("ds_write_b32 %0, %1" :: "v"(L_Q), "v"(1) : "memory");
    }
    if (lw == 0) {
      {
        const unsigned frag = ((hh >> 1) * 2 + (xcd >> 2)) * 2 + (hh & 1), pl = 32 * ((l >> 3) & 1) + 8 * (xcd & 3) + (l & 7);
        const unsigned pb = (unsigned)((b * 4 + (l >> 4)) * 8) * SLAB;
        u32x4 v[8];
        unsigned ad = pb + S_T + frag * 1024 + pl * 16, ac = pb + S_TC + frag * 256 + (xcd & 3) * 64 + (l & 15) * 4;
        asm volatile("s_sleep %2" : "+v"(ad), "+v"(ac) : "i"(T_DELAY) : "memory");
#pragma unroll 1
        for (unsigned spin = 0; spin < SPIN_CAP; ++spin) {
          asm volatile("" ::: "memory");
          unsigned c[8], bad = 0; DG(1);
          UNROLL for (int p = 0; p < 8; ++p) { v[p] = __builtin_amdgcn_raw_buffer_load_b128(rw, ad, p * SLAB, SC1);
                                               c[p] = __builtin_amdgcn_raw_buffer_load_b32(rw, ac, p * SLAB, SC1); }
          UNROLL for (int p = 0; p < 8; ++p) bad |= csum(tag, v[p]) ^ c[p];
          if (__all(bad == 0)) break;
          __builtin_amdgcn_s_sleep(BACKOFF);
        }
        if ((w & 31) == 0 && l == 0) st32(tag, rw, W_EPO + b * 256);
        float a[8] = {};
        UNROLL for (int p = 0; p < 8; ++p) UNROLL for (int i = 0; i < 4; ++i) { a[2 * i] += blo(v[p][i]); a[2 * i + 1] += bhi(v[p][i]); }
        float e[4];
        UNROLL for (int i = 0; i < 4; ++i) e[i] = sum16(a[i], a[4 + i]);
        const int n = 512 + 16 * hh + 8 * ((l >> 4) & 1) + 4 * ((l >> 3) & 1) + 2 * (l >> 5);
        *(unsigned*)(out + (size_t)(b * 64 + 8 * xcd + (l & 7)) * 1152 + n * 2) = pk(sum32(e[0], e[2]), sum32(e[1], e[3]));
      }
    }
    ST(6);
    if (threadIdx.x == 0) for (int i = 0; i < 7; ++i) ((u64*)(ws + STAMP_OFF))[w * STAMP_W + i] = st_[i] ? st_[i] : st_[0];
    if (threadIdx.x == 128) ((u64*)(ws + STAMP_OFF))[w * STAMP_W + 7] = st_[7];
    return;
  }
  const int cw = wv - 4, r = l & 31, hf = l >> 5, kt = cw >> 1, qt = cw & 1;
  const unsigned slab = (unsigned)w * SLAB;
  const unsigned hoff = l < 16 && cw == 0 ? slab + S_H + l * 8 : 0x40000000u;
  if (PREF && cw == 0) {
    const char* p = (const char*)(pc & ~127ull) + ((j & 7) * (CODE_LINES / 8) + (l < CODE_LINES / 8 ? l : 0)) * 128;
    if (PREF & 2) {
      const int e = l & 1;
      if ((l >> 1) == 16) p = kc + kvo + e * 130944;
      if ((l >> 1) == 17) p = vc + kvo + e * 130944;
      if ((l >> 1) == 18) p = wo + (size_t)xcd * 512 * 1152 + e * 511 * 1152;
      if ((l >> 1) == 19) p = ws + b * 32 * SLAB + e * (32 * SLAB - 128);
      if ((l >> 1) == 20) p = out + (size_t)(b * 64 + 8 * xcd + e * 7) * 1152;
      if ((l >> 1) == 21) p = x + b * 73728 + e * 73600;
      if (l >= 48 && l < 53) p = wq + h * 256 + (size_t)(l - 48) * 143 * 8192;
    }
    asm volatile("s_mov_b32 m0, %1\n\ts_nop 0\n\tglobal_load_lds_dword %0, off" :: "v"(p), "i"(73728) : "memory");
  }
  const unsigned tag = *(const CAS unsigned*)(ws + W_EPO + b * 256) + 0x9E3779B9u;
  const int qq = (l & 15) >> 2, pp = l & 3, blk = (l >> 4) & 1;
  const int trc = 16 * ((4 * cw + 2 * blk + (pp >> 1)) ^ (qq << 2)) + 8 * (pp & 1);
  asm volatile("" :: "s"(tag));
  if (PREW) { __builtin_amdgcn_raw_buffer_store_b64(u32x2{~tag, ~tag}, rw, hoff, 0, SC1); __builtin_amdgcn_raw_buffer_store_b64(u32x2{~tag, ~tag}, rw, hoff + 128, 0, SC1); }
  {
    f32x16 qa0 = {}, qa1 = {};
    LAS char* aw = opq(sm + 8192 + (8 * hf + qq) * 256 + trc);
    LAS char* ax[4];
    UNROLL for (int s = 0; s < 4; ++s) ax[s] = sm + ((r * 128 + 16 * (hf ^ ((r >> 1) & 7))) ^ (32 * s));
    bf16x8 fa[2][4], f0[2][4], f1[2][4];
    UNROLL for (int i = 0; i < 9; ++i) {
      CBAR();
      const int so = (i % 3) * 24576, cur = i & 1, prv = cur ^ 1;
      UNROLL for (int s = 0; s < 4; ++s) {
        if (i > 0) { if (s & 1) qa1 = MFMA(fa[prv][s >> 1], f1[prv][s >> 1], qa1); else qa0 = MFMA(fa[prv][s >> 1], f0[prv][s >> 1], qa0); }
        FENCE();
        fa[cur][s] = rdtr(aw + so + s * 4096, 1024); f0[cur][s] = rd128(ax[s] + so); f1[cur][s] = rd128(ax[s] + so + 4096);
        FENCE();
      }
      if (i > 0) { UNROLL for (int s = 2; s < 4; ++s) { qa0 = MFMA(fa[prv][s], f0[prv][s], qa0); qa1 = MFMA(fa[prv][s], f1[prv][s], qa1); } }
    }
    FENCE();
    UNROLL for (int s = 0; s < 4; ++s) qa0 = MFMA(fa[0][s], f0[0][s], qa0);
    FENCE();
    UNROLL for (int s = 0; s < 4; ++s) qa1 = MFMA(fa[0][s], f1[0][s], qa1);
    FENCE();
    LAS char* q = opq(sm + L_Q + cw * 4096 + r * 16 + 8 * hf);
    UNROLL for (int s = 0; s < 2; ++s) { const u32x4 p0 = pk8(qa0, s); *(LAS u32x2*)(q + s * 2048) = u32x2{p0.x, p0.y}; *(LAS u32x2*)(q + s * 2048 + 512) = u32x2{p0.z, p0.w}; }
    FENCE();
    UNROLL for (int s = 0; s < 2; ++s) { const u32x4 p1 = pk8(qa1, s); *(LAS u32x2*)(q + s * 2048 + 1024) = u32x2{p1.x, p1.y}; *(LAS u32x2*)(q + s * 2048 + 1536) = u32x2{p1.z, p1.w}; }
    asm volatile("s_waitcnt vmcnt(0)" ::: "memory");
  }
  ST(1);
  constexpr float CS = 0.08838834764831845f * 1.4426950408889634f;
  f32x16 S[8];
  float mu[8], su[8];
  LAS char* pr = opq(sm + L_P + l * 16);
  LAS char* pq = opq(sm + L_Q + qt * 1024 + l * 16);
#define SM_MAX(u) do { float a_ = max3(S[u][0], S[u][1], S[u][2]), b_ = max3(S[u][3], S[u][4], S[u][5]); \
    a_ = max3(a_, S[u][6], S[u][7]); b_ = max3(b_, S[u][8], S[u][9]); a_ = max3(a_, S[u][10], S[u][11]); b_ = max3(b_, S[u][12], S[u][13]); \
    a_ = max3(a_, S[u][14], S[u][15]); mu[u] = -CS * max2(a_, b_); PIN(mu[u]); } while (0)
#define SM_F(u) do { UNROLL for (int i = 0; i < 16; i += 2) { f32x2 t_ = {S[u][i], S[u][i + 1]}; t_ = __builtin_elementwise_fma(t_, f32x2{CS, CS}, f32x2{mu[u], mu[u]}); PIN(t_); S[u][i] = t_.x; S[u][i + 1] = t_.y; } } while (0)
#define SM_E(u, i) do { f32x2 t_ = {__builtin_amdgcn_exp2f(S[u][i]), __builtin_amdgcn_exp2f(S[u][i + 1])}; PIN(t_); S[u][i] = t_.x; S[u][i + 1] = t_.y; } while (0)
#define SM_A(u) do { f32x2 a_ = f32x2{S[u][0], S[u][1]} + f32x2{S[u][2], S[u][3]}, b_ = f32x2{S[u][4], S[u][5]} + f32x2{S[u][6], S[u][7]}; \
    a_ += f32x2{S[u][8], S[u][9]}; b_ += f32x2{S[u][10], S[u][11]}; a_ += f32x2{S[u][12], S[u][13]}; b_ += f32x2{S[u][14], S[u][15]}; a_ += b_; su[u] = a_.x + a_.y; PIN(su[u]); } while (0)
  {
    bf16x8 qf[8];
    LAS char* ak[8];
    UNROLL for (int s = 0; s < 8; ++s) ak[s] = opq(sm + 98304 + (((32 * kt + r) * 256 + 16 * (hf ^ (r & 15))) ^ (32 * s)));
    UNROLL for (int u = 0; u < 8; ++u) {
      CBAR();
      bf16x8 kf[8];
      f32x16 a = {};
      const int v = u > 0 ? u - 1 : 0, v2 = u > 1 ? u - 2 : 0;
      UNROLL for (int s = 0; s < 4; ++s) { if (u == 0) qf[s] = rd128(pq + s * 2048); kf[s] = rd128(ak[s] + (u & 3) * 16384); }
      FENCE();
      if (u > 1) SM_A(v2);
      FENCE();
      if (u > 0) { SM_MAX(v); SM_F(v); }
      UNROLL for (int s = 0; s < 8; ++s) {
        MF(a, kf[s], qf[s]);
        if (s < 4) { if (u == 0) qf[s + 4] = rd128(pq + (s + 4) * 2048); kf[s + 4] = rd128(ak[s + 4] + (u & 3) * 16384); }
        if (u > 0) SM_E(v, 2 * s);
        if (u == 3 && s >= 4) { ak[2 * s - 8] = opq(ak[2 * s - 8] - 98304); ak[2 * s - 7] = opq(ak[2 * s - 7] - 98304); }
      }
      S[u] = a;
    }
  }
  LAS f32x2* stm = (LAS f32x2*)opq(sm + L_Q + r * 8);
  {
    FENCE();
    float mw = min3(min3(mu[0], mu[1], mu[2]), min3(mu[3], mu[4], mu[5]), mu[6]);
    SM_A(6);
    FENCE();
    SM_MAX(7);
    mw = swap32min(min2(mw, mu[7]));
    FENCE();
    SM_F(7);
    UNROLL for (int i = 0; i < 16; i += 2) SM_E(7, i);
    UNROLL for (int u = 0; u < 8; ++u) mu[u] = __builtin_amdgcn_exp2f(mw - mu[u]);
    FENCE();
    SM_A(7);
    float lw_ = mu[0] * su[0], lx = mu[1] * su[1];
    UNROLL for (int u = 2; u < 8; u += 2) { lw_ = __builtin_fmaf(mu[u], su[u], lw_); lx = __builtin_fmaf(mu[u + 1], su[u + 1], lx); }
    lw_ = sum32(lw_ + lx, lw_ + lx);
    stm[cw * 32] = f32x2{mw, lw_};
    CBAR();
    ST(2);
    const f32x2 ot = stm[(cw ^ 2) * 32];
    const float mg = min2(mw, ot.x), ea = __builtin_amdgcn_exp2f(mg - mw), eb = __builtin_amdgcn_exp2f(mg - ot.x);
    const float g = ea * __builtin_amdgcn_rcpf(__builtin_fmaf(ea, lw_, eb * ot.y));
    UNROLL for (int u = 0; u < 8; ++u) mu[u] *= g;
  }
  LAS char* pw = opq(sm + L_P + kt * 4096 + qt * 1024 + l * 16);
  unsigned pd[8];
#define PV_C(u, k) do { float x_ = S[u][2 * (k)] * mu[u]; PIN(x_); float y_ = S[u][2 * (k) + 1] * mu[u]; PIN(y_); pd[k] = pk(x_, y_); PIN(pd[k]); } while (0)
#define PV_C2(u, k) do { float x_ = S[u][2 * (k)] * mu[u], y_ = S[u][2 * (k) + 1] * mu[u], z_ = S[u][2 * (k) + 2] * mu[u], w_ = S[u][2 * (k) + 3] * mu[u]; \
    asm volatile("" : "+v"(x_), "+v"(z_)); asm volatile("" : "+v"(y_), "+v"(w_)); pd[k] = pk(x_, y_); pd[k + 1] = pk(z_, w_); asm volatile("" : "+v"(pd[k]), "+v"(pd[k + 1])); } while (0)
#define PV_W(u, h) do { *(LAS u32x4*)(pw + ((u) & 1) * 8192 + (h) * 2048) = u32x4{pd[4 * (h)], pd[4 * (h) + 1], pd[4 * (h) + 2], pd[4 * (h) + 3]}; FENCE(); } while (0)
  S[0] *= mu[0];
  *(LAS u32x4*)pw = pk8(S[0], 0); *(LAS u32x4*)(pw + 2048) = pk8(S[0], 1);
  FENCE();
  S[1] *= mu[1];
  UNROLL for (int k = 0; k < 8; ++k) { pd[k] = pk(S[1][2 * k], S[1][2 * k + 1]); PIN(pd[k]); }
  f32x16 o0 = {}, o1 = {};
  LAS char* av1 = opq(sm + (4 * hf + qq) * 256 + trc);
  LAS char* av0 = opq(av1 + 98304);
  UNROLL for (int u = 0; u < 7; ++u) {
    CBAR();
    bf16x8 va[4], p0[4], p1[4];
    const int v = u < 6 ? u + 2 : 7;
#define PV_RD(ks) do { va[ks] = rdtr((u < 4 ? av0 : av1) + (u & 3) * 16384 + (ks) * 4096, 2048); \
      p0[ks] = rd128(pr + (u & 1) * 8192 + (ks) * 2048); p1[ks] = rd128(pr + (u & 1) * 8192 + (ks) * 2048 + 1024); FENCE(); } while (0)
    PV_RD(0); PV_RD(1);
    MF(o0, va[0], p0[0]); PV_RD(2); MF(o1, va[0], p1[0]); PV_RD(3);
    MF(o0, va[1], p0[1]); PV_W(u + 1, 0); MF(o1, va[1], p1[1]); PV_W(u + 1, 1);
    MF(o0, va[2], p0[2]); if (u < 6) PV_C2(v, 0);
    MF(o1, va[2], p1[2]); if (u < 6) PV_C2(v, 2);
    MF(o0, va[3], p0[3]); if (u < 6) PV_C2(v, 4);
    MF(o1, va[3], p1[3]); if (u < 6) PV_C2(v, 6);
  }
  {
    CBAR();
    bf16x8 va[4], p0[4], p1[4];
    UNROLL for (int ks = 0; ks < 6; ++ks) {
      if (ks >= 2) { o0 = MFMA(va[ks - 2], p0[ks - 2], o0); FENCE(); }
      if (ks < 4) { va[ks] = rdtr(av1 + 49152 + ks * 4096, 2048); p0[ks] = rd128(pr + 8192 + ks * 2048); }
      else { p1[2 * ks - 8] = rd128(pr + 8192 + (2 * ks - 8) * 2048 + 1024); p1[2 * ks - 7] = rd128(pr + 8192 + (2 * ks - 7) * 2048 + 1024); }
      FENCE();
    }
    const int off = cw * 4096 + l * 16;
    LAS char* d = opq(sm + L_Q + off);
    o1 = MFMA(va[0], p1[0], o1); o1 = MFMA(va[1], p1[1], o1);
    FENCE();
    const u32x4 a0 = pk8(o0, 0);
    st128(a0, rw, slab + S_O + off);
    FENCE();
    o1 = MFMA(va[2], p1[2], o1);
    FENCE();
    const u32x4 a1 = pk8(o0, 1);
    st128(a1, rw, slab + S_O + off + 2048);
    FENCE();
    o1 = MFMA(va[3], p1[3], o1);
    FENCE();
    unsigned ck = csum(csum(tag, a0), a1); PIN(ck);
    *(LAS u32x4*)d = a0;
    FENCE();
    const u32x4 b0 = pk8(o1, 0), b1 = pk8(o1, 1);
    st128(b0, rw, slab + S_O + off + 1024); st128(b1, rw, slab + S_O + off + 3072);
    st32(csum(csum(ck, b0), b1), rw, slab + S_OC + cw * 256 + l * 4);
    __builtin_amdgcn_raw_buffer_store_b64(u32x2{tag, tag}, rw, hoff, 0, SC1);
    FENCE();
    *(LAS u32x4*)(d + 2048) = a1; *(LAS u32x4*)(d + 1024) = b0; *(LAS u32x4*)(d + 3072) = b1;
  }
  ST(3);
  bf16x8 wf[32];
  LAS char* av2 = opq(av1 + L_P);
  LAS char* po = opq(pr + (slot_addr(16) - L_P));
  auto loadW1 = [&](int i, int ks) { wf[4 * i + ks] = rdtr((i < 3 ? av0 + (i + 1) * 16384 : i < 7 ? av1 + (i - 3) * 16384 : av2) + ks * 4096, 2048); };
  f32x16 c0 = {}, c1 = {};
  auto head = [&](int t, LAS char* p, int wnext) {
    bf16x8 g0[8], g1[8];
    UNROLL for (int ks = 0; ks < 10; ++ks) {
      if (ks >= 2) MF(c0, wf[8 * t + ks - 2], g0[ks - 2]);
      if (ks < 8) { g0[ks] = rd128(p + ks * 2048); g1[ks] = rd128(p + ks * 2048 + 1024); FENCE(); }
      if (ks >= 2) { MF(c1, wf[8 * t + ks - 2], g1[ks - 2]);
        if (wnext >= 0) { loadW1(wnext + ((ks - 2) >> 2), (ks - 2) & 3); FENCE(); } }
    }
  };
  CBAR();
  u32x4 t0, t1;
  {
    f32x16 t = {};
    LAS char* at = opq(sm + slot_addr(16) + (4 * hf + qq) * 128 + 16 * ((4 * kt + 2 * blk + (pp >> 1)) ^ ((qq >> 1) << 2)) + 8 * (pp & 1));
    bf16x8 ta[8], tb[8];
    UNROLL for (int ks = 0; ks < 11; ++ks) {
      if (ks >= 3) MF(t, ta[ks - 3], tb[ks - 3]);
      if (ks < 8) { ta[ks] = rdtr(at + ks * 2048, 1024); tb[ks] = rd128(pq + ks * 2048); }
      if (ks >= 7) { loadW1((ks - 7) >> 1, ((ks - 7) & 1) * 2); loadW1((ks - 7) >> 1, ((ks - 7) & 1) * 2 + 1); }
      FENCE();
    }
    head(0, pr + (L_Q - L_P), -1);
    t0 = pk8(t, 0); t1 = pk8(t, 1);
  }
  CBAR();
  UNROLL for (int i = 0; i < 8; ++i) loadW1(2 + (i >> 2), i & 3);
  unsigned tc0 = csum(tag, t0), tc1 = csum(tag, t1);
  const unsigned cl = ((r >> 3) * 16 + hf * 8 + (r & 7)) * 4;
  unsigned aT = slab + S_T + cw * 2048 + l * 16, aTC = slab + S_TC + cw * 512 + cl, aM = slab + S_M + cw * 4096 + l * 16, aMC = slab + S_MC + cw * 512 + cl;
  const unsigned tile = cw * 2 + (xcd >> 2), pb = (unsigned)(j * 8 + 2 * (l >> 4)) * SLAB;
  unsigned aR = pb + S_M + tile * 2048 + (32 * ((l >> 3) & 1) + 8 * (xcd & 3) + (l & 7)) * 16, aRC = pb + S_MC + tile * 256 + (xcd & 3) * 64 + (l & 15) * 4;
  unsigned aO = (unsigned)(b * 64 + 8 * xcd + (l & 7)) * 1152 + (128 * hh + 32 * cw + 16 * blk + 8 * hf + 4 * ((l >> 3) & 1)) * 2;
  asm volatile("" : "+v"(tc0), "+v"(tc1), "+v"(aT), "+v"(aTC), "+v"(aM), "+v"(aMC), "+v"(aR), "+v"(aRC), "+v"(aO));
  CBAR();
  UNROLL for (int i = 0; i < 8; ++i) loadW1(4 + (i >> 2), i & 3);
  CBAR();
  st128(t0, rw, aT); st128(t1, rw, aT + 1024); st32(tc0, rw, aTC); st32(tc1, rw, aTC + 256);
  FENCE();
  head(1, po, -1);
  CBAR(); head(2, po + 16384, 6);
  CBAR(); head(3, po + 32768, -1);
  ST(4);
  {
    const u32x4 a0 = pk8(c0, 0), a1 = pk8(c0, 1);
    st128(a0, rw, aM); st128(a1, rw, aM + 1024);
    FENCE();
    const u32x4 b0 = pk8(c1, 0), b1 = pk8(c1, 1);
    st128(b0, rw, aM + 2048); st128(b1, rw, aM + 3072);
    FENCE();
    st32(csum(csum(tag, a0), a1), rw, aMC); st32(csum(csum(tag, b0), b1), rw, aMC + 256);
  }
  ST(5);
  {
#pragma unroll 1
    for (unsigned spin = 0; spin < SPIN_CAP; ++spin) if (__builtin_amdgcn_readfirstlane(*(volatile LAS unsigned*)(sm + L_Q))) break;
    ST(7);
    u32x4 v[2][2];
#pragma unroll 1
    for (unsigned spin = 0; spin < SPIN_CAP; ++spin) {
      asm volatile("" ::: "memory");
      unsigned c[2]; DG(1);
      UNROLL for (int p = 0; p < 2; ++p) {
        v[p][0] = __builtin_amdgcn_raw_buffer_load_b128(rw, aR, p * SLAB, SC1);
        v[p][1] = __builtin_amdgcn_raw_buffer_load_b128(rw, aR + 1024, p * SLAB, SC1);
        c[p] = __builtin_amdgcn_raw_buffer_load_b32(rw, aRC, p * SLAB, SC1);
      }
      if (__all(((csum(csum(tag, v[0][0]), v[0][1]) ^ c[0]) | (csum(csum(tag, v[1][0]), v[1][1]) ^ c[1])) == 0)) break;
      __builtin_amdgcn_s_sleep(BACKOFF);
    }
    float e[8];
    UNROLL for (int i = 0; i < 4; ++i) {
      e[2 * i] = sum16(blo(v[0][0][i]) + blo(v[1][0][i]), blo(v[0][1][i]) + blo(v[1][1][i]));
      e[2 * i + 1] = sum16(bhi(v[0][0][i]) + bhi(v[1][0][i]), bhi(v[0][1][i]) + bhi(v[1][1][i]));
    }
    *(u32x2*)(out + aO) = u32x2{pk(sum32(e[0], e[4]), sum32(e[1], e[5])), pk(sum32(e[2], e[6]), sum32(e[3], e[7]))};
  }
  ST(6);
  if (threadIdx.x == 256) for (int i = 0; i < 8; ++i) ((u64*)(ws + STAMP_OFF))[w * STAMP_W + 8 + i] = st_[i] ? st_[i] : st_[0];
}

__global__ void k_probe(char* ws) {
  const u64* st = (const u64*)(ws + STAMP_OFF); int t = threadIdx.x;
  __shared__ u64 a[256], b[256];
  a[t] = st[t * STAMP_W]; b[t] = st[t * STAMP_W + 14];
  __syncthreads();
  if (t == 0) {
    u64 mn = ~0ull, mx = 0, e = 0, sum = 0;
    for (int i = 0; i < 256; ++i) { mn = a[i] < mn ? a[i] : mn; mx = a[i] > mx ? a[i] : mx; e = b[i] > e ? b[i] : e; sum += b[i] - a[i]; }
    u64 d = e - mn;
    if (d > 10000) d = 10000;
    u64 t0 = __builtin_amdgcn_s_memrealtime();
    while (__builtin_amdgcn_s_memrealtime() - t0 < d * 100) __builtin_amdgcn_s_sleep(4);
  }
}
extern "C" void kernel_launch(void* const* d_in, const int* in_sizes, int n_in, void* d_out, int out_size, void* d_ws, size_t ws_size, hipStream_t stream) {
  gqa_fused<<<256, 512, 0, stream>>>((const char*)d_in[0], (const char*)d_in[1], (const char*)d_in[2], (const char*)d_in[3], (const char*)d_in[4], (char*)d_out, (char*)d_ws);
  k_probe<<<1, 256, 0, stream>>>((char*)d_ws);
}
```

```cpp
#define Q_POL 0
#define TOUCH_POL 0
#define RETOUCH 99
#define W_POL 0
#define TOUCH_AT 101
#include <hip/hip_runtime.h>
#include <hip/hip_bf16.h>

typedef unsigned long long u64;
typedef short bf16x8 __attribute__((ext_vector_type(8)));
typedef short s16x4 __attribute__((ext_vector_type(4)));
typedef float f32x16 __attribute__((ext_vector_type(16)));
typedef float f32x2 __attribute__((ext_vector_type(2)));
typedef __bf16 bf16x2 __attribute__((ext_vector_type(2)));
typedef unsigned u32x4 __attribute__((ext_vector_type(4)));
typedef unsigned u32x2 __attribute__((ext_vector_type(2)));
#define LAS __attribute__((address_space(3)))
#define CAS __attribute__((address_space(4)))
#define DI __device__ __forceinline__
typedef __amdgpu_buffer_rsrc_t rsrc_t;

constexpr int L_P = 65536, L_Q = 81920;
constexpr int slot_addr(int g) { return g == 24 ? L_P : (g & 7) < 4 ? 98304 + (g & 7) * 16384 : ((g & 7) - 4) * 16384; }
constexpr unsigned S_O = 0, S_OC = 16384, S_T = 17408, S_TC = 25600, S_M = 27648, S_MC = 44032, S_H = 46080, SLAB = 46592;
constexpr unsigned W_EPO = 12u << 20, W_BYTES = W_EPO + 4096;
constexpr int SC1 = 16, NT = 2;
constexpr unsigned SPIN_CAP = 1u << 20;
#define RE_POL 16
#define T1_AT 3
#define H2_DELAY 8
#define P2_DELAY 4
#define C1 110
#define C2 200
#define HL_AT 2
#define PREW 1
#define BACKOFF 4
#define H2_AT 3
#define T_DELAY 14
#define KV_POL 2
#define PREF 1
#define CODE_LINES 152

#define DG(i) do {} while (0)
#define ST(i) do {} while (0)

DI rsrc_t mk(const void* p, unsigned n) { return __builtin_amdgcn_make_buffer_rsrc((void*)p, 0, n, 0x00020000); }
#define DMA(R, LDSOFF, V, SOFF, IMM, POL) __builtin_amdgcn_raw_ptr_buffer_load_lds(R, (LAS void*)(sm + (LDSOFF)), 16, V, SOFF, IMM, POL)
#define FENCE() __builtin_amdgcn_sched_barrier(0)
#define LTS() do {} while (0)
#define LBAR(N) do { asm volatile("s_waitcnt vmcnt(%0)\n\ts_barrier" :: "i"(N) : "memory"); LTS(); } while (0)
#define CBAR() CBAR0()
#define CBAR0() do { FENCE(); asm volatile("" ::: "memory"); __builtin_amdgcn_s_waitcnt(0xC07F); __builtin_amdgcn_s_barrier(); asm volatile("" ::: "memory"); FENCE(); } while (0)
#define PIN(x) asm volatile("" : "+v"(x))
DI LAS char* opq(LAS char* p) { asm("" : "+v"(p)); return p; }
#define MFMA(a, b, c) __builtin_amdgcn_mfma_f32_32x32x16_bf16(a, b, c, 0, 0, 0)
#define MF(acc, a, b) do { FENCE(); acc = MFMA(a, b, acc); PIN(acc); FENCE(); } while (0)
#define UNROLL _Pragma("unroll")
DI bf16x8 rd128(LAS char* p) { return *(LAS bf16x8*)p; }
DI bf16x8 rdtr(LAS char* p, int step) {
  s16x4 a = __builtin_amdgcn_ds_read_tr16_b64_v4i16((LAS s16x4*)p), b = __builtin_amdgcn_ds_read_tr16_b64_v4i16((LAS s16x4*)(p + step));
  return __builtin_shufflevector(a, b, 0, 1, 2, 3, 4, 5, 6, 7);
}
DI unsigned pk(float a, float b) { f32x2 v = {a, b}; return __builtin_bit_cast(unsigned, __builtin_convertvector(v, bf16x2)); }
DI u32x4 pk8(const f32x16& x, int s) { return u32x4{pk(x[8 * s], x[8 * s + 1]), pk(x[8 * s + 2], x[8 * s + 3]), pk(x[8 * s + 4], x[8 * s + 5]), pk(x[8 * s + 6], x[8 * s + 7])}; }
DI float max2(float a, float b) { return __builtin_elementwise_maximum(a, b); }
DI float max3(float a, float b, float c) { return max2(max2(a, b), c); }
DI float min2(float a, float b) { return __builtin_elementwise_minimum(a, b); }
DI float min3(float a, float b, float c) { return min2(min2(a, b), c); }
DI float swap32min(float v) { auto r = __builtin_amdgcn_permlane32_swap(__float_as_uint(v), __float_as_uint(v), false, false); return min2(__uint_as_float(r[0]), __uint_as_float(r[1])); }
DI float swap32max(float v) { auto r = __builtin_amdgcn_permlane32_swap(__float_as_uint(v), __float_as_uint(v), false, false); return max2(__uint_as_float(r[0]), __uint_as_float(r[1])); }
DI float sum16(float a, float b) { auto r = __builtin_amdgcn_permlane16_swap(__float_as_uint(a), __float_as_uint(b), false, false); return __uint_as_float(r[0]) + __uint_as_float(r[1]); }
DI float sum32(float a, float b) { auto r = __builtin_amdgcn_permlane32_swap(__float_as_uint(a), __float_as_uint(b), false, false); return __uint_as_float(r[0]) + __uint_as_float(r[1]); }
DI unsigned csum(unsigned c, u32x4 v) { return (c + v.x + v.y) + v.z + v.w; }
DI float blo(unsigned v) { return __uint_as_float(v << 16); }
DI float bhi(unsigned v) { return __uint_as_float(v & 0xffff0000u); }
DI void st128(u32x4 v, rsrc_t r, unsigned off) { __builtin_amdgcn_raw_buffer_store_b128(v, r, off, 0, SC1); }
DI void st32(unsigned v, rsrc_t r, unsigned off) { __builtin_amdgcn_raw_buffer_store_b32(v, r, off, 0, SC1); }

__global__ __launch_bounds__(512, 2) void gqa_fused(const char* __restrict__ x, const char* __restrict__ kc, const char* __restrict__ vc,
                                                    const char* __restrict__ wq, const char* __restrict__ wo, char* __restrict__ out, char* ws) {
  __shared__ __attribute__((aligned(256))) char smem[163840];
  LAS char* sm = (LAS char*)smem;
  u64 pc;
  asm volatile("s_getpc_b64 %0" : "=s"(pc));
  const int w = blockIdx.x, wv = __builtin_amdgcn_readfirstlane(threadIdx.x >> 6), l = threadIdx.x & 63;
  const int xcd = w & 7, j = w >> 3, b = j >> 2, hh = j & 3, h = xcd * 4 + hh;
  ST(0);
  asm volatile("" :: "s"(x), "s"(kc), "s"(vc), "s"(wq), "s"(wo), "s"(out), "s"(ws));
  const rsrc_t rw = mk(ws, W_BYTES);
  const size_t kvo = (size_t)(b * 16 + (h >> 1)) * 131072;
  if (__builtin_expect(wv < 4, 1)) {
    const int lw = wv;
    LTS();
    const rsrc_t rx = mk(x + b * 73728, 73728), rq = mk(wq + h * 256, 576 * 8192 - h * 256);
    const unsigned vx0 = (16 * lw + (l >> 3)) * 1152 + 16 * ((l & 7) ^ (l >> 4));
    const unsigned vx1 = (16 * lw + 8 + (l >> 3)) * 1152 + 16 * ((l & 7) ^ (4 + (l >> 4)));
    const unsigned sw4 = 16 * ((l & 15) ^ ((l >> 4) << 2));
    const unsigned vq = (16 * lw + (l >> 4)) * 8192 + sw4;
    {
      int c = (b + 2 * hh) & 7, so = 0;
#pragma unroll 1
      for (int i = 0; i < 9; ++i) {
        if (i >= 2) LBAR(6);
        DMA(rx, so + lw * 2048, vx0, c * 128, 0, 0);
        DMA(rx, so + lw * 2048 + 1024, vx1, c * 128, 0, 0);
        UNROLL for (int p = 0; p < 4; ++p) DMA(rq, so + 8192 + lw * 4096 + p * 1024, vq, (c * 64 + p * 4) * 8192, 0, Q_POL);
        if (i == TOUCH_AT - 100) __builtin_amdgcn_raw_ptr_buffer_load_lds(mk(wo + (size_t)xcd * 512 * 1152, 512 * 1152), (LAS void*)(sm + 74752 + lw * 256), 4, (j * 144 + lw * 36 + (l < 36 ? l : 35)) * 128, 0, 0, TOUCH_POL);
        if (i == RETOUCH) __builtin_amdgcn_raw_ptr_buffer_load_lds(mk(wo + (size_t)xcd * 512 * 1152, 512 * 1152), (LAS void*)(sm + 74752 + lw * 256), 4, (j * 144 + lw * 36 + (l < 36 ? l : 35)) * 128, 0, 0, RE_POL);
        c = c == 8 ? 0 : c + 1;
        so = so == 49152 ? 0 : so + 24576;
      }
    }
    const unsigned tag = *(const CAS unsigned*)(ws + W_EPO + b * 256) + 0x9E3779B9u;
    const rsrc_t rk = mk(kc + kvo, 131072), rv = mk(vc + kvo, 131072), ro = mk(wo + (size_t)xcd * 512 * 1152, 512 * 1152);
    const unsigned vk = (4 * lw + (l >> 4)) * 256 + 16 * ((l & 15) ^ (4 * lw + (l >> 4)));
    const unsigned vv = lw * 4096 + (l >> 4) * 256 + sw4;
    const unsigned vo = (16 * lw + (l >> 4)) * 1152 + hh * 256 + sw4;
    const unsigned vt = (128 * hh + 32 * lw + (l >> 3)) * 1152 + 1024 + 16 * ((l & 7) ^ (((l >> 4) & 1) << 2));
    auto rslot = [](int g) { return (g & 4) ? (g & 3) * 16384 : 98304 + (g & 3) * 16384; };
    auto unitK = [&](int u) { const int s = rslot(u) + lw * 1024; UNROLL for (int p = 0; p < 4; ++p) DMA(rk, s + p * 4096, vk, u * 16384 + p * 4096, 0, KV_POL); };
    auto unitV = [&](int u) { const int s = rslot(u) + lw * 4096;
      DMA(rv, s, vv, u * 16384, 0, KV_POL); DMA(rv, s, vv, u * 16384, 1024, KV_POL); DMA(rv, s, vv, u * 16384, 2048, KV_POL); DMA(rv, s, vv, u * 16384, 3072, KV_POL); };
    auto unitW = [&](int i, int s) {
      const int row0 = ((2 * hh + i) & 7) * 64;
      UNROLL for (int p = 0; p < 4; ++p) DMA(ro, s + lw * 4096 + p * 1024, vo, (row0 + p * 4) * 1152, 0, W_POL);
    };
    LBAR(6); unitK(0);
    LBAR(4); unitK(1); unitK(2); unitK(3);
    asm volatile("" :: "s"(tag));
    ST(1);
#pragma unroll 1
    for (int u = 4; u < 8; ++u) { LBAR(12); unitK(u); }
    LBAR(12);
#pragma unroll 1
    for (int u = 0; u < 8; ++u) {
      if (u < 3) LBAR(8); else if (u > 4) LBAR(16);
      unitV(u);
      if (u == 3) { ST(2); asm volatile("s_barrier" ::: "memory"); }
    }
    LBAR(16);
    LBAR(12);
    LBAR(8);
    UNROLL for (int p = 0; p < 4; ++p) DMA(ro, slot_addr(16) + lw * 4096 + p * 1024, vt, p * 8 * 1152, 0, W_POL);
    LBAR(8); unitW(0, slot_addr(17));
    LBAR(8);
    ST(3);
    u32x4 o[3][4]; unsigned oc[3], oo[3];
    UNROLL for (int t = 0; t < 3; ++t) oo[t] = (unsigned)(((b * 4 + ((hh + 1 + t) & 3)) * 8 + xcd)) * SLAB;
    const unsigned vl = S_O + lw * 4096 + l * 16, vc2 = S_OC + lw * 256 + l * 4;
#define OLOAD(t) asm volatile("buffer_load_dwordx4 %0, %5, %7, %8 offen sc1\n\tbuffer_load_dwordx4 %1, %5, %7, %8 offen offset:1024 sc1\n\t" \
                              "buffer_load_dwordx4 %2, %5, %7, %8 offen offset:2048 sc1\n\tbuffer_load_dwordx4 %3, %5, %7, %8 offen offset:3072 sc1\n\t" \
                              "buffer_load_dword %4, %6, %7, %8 offen sc1" \
                              : "=&v"(o[t][0]), "=&v"(o[t][1]), "=&v"(o[t][2]), "=&v"(o[t][3]), "=&v"(oc[t]) : "v"(vl), "v"(vc2), "s"(rw), "s"(oo[t]) : "memory")
#define OWAIT(t, N) asm volatile("s_waitcnt vmcnt(%5)" : "+v"(o[t][0]), "+v"(o[t][1]), "+v"(o[t][2]), "+v"(o[t][3]), "+v"(oc[t]) : "i"(N) : "memory")
#define OPUT(t, N) do { OWAIT(t, N); \
      _Pragma("unroll 1") for (unsigned spin = 0; spin < SPIN_CAP; ++spin) { \
        UNROLL for (int p = 0; p < 4; ++p) asm volatile("ds_write_b128 %0, %1 offset:%2" :: "v"(od), "v"(o[t][p]), "i"(t * 16384 + p * 1024) : "memory"); \
        if (__all(csum(csum(csum(csum(tag, o[t][0]), o[t][1]), o[t][2]), o[t][3]) == oc[t])) break; \
        DG(3); asm volatile("s_waitcnt lgkmcnt(0)" ::: "memory"); OLOAD(t); OWAIT(t, 0); } } while (0)
    LAS char* od = sm + slot_addr(16) + lw * 4096 + l * 16;
    auto hint = [&](unsigned off) { const unsigned t = (unsigned)__builtin_amdgcn_s_memrealtime();
      __builtin_amdgcn_raw_buffer_store_b64(u32x2{tag, t}, rw, l < 16 && lw == 1 ? (unsigned)w * SLAB + S_H + off + l * 8 : 0x40000000u, 0, SC1); };
    auto until = [&](unsigned t) { _Pragma("unroll 1") for (int spin = 0; spin < 256; ++spin) if ((int)((unsigned)__builtin_amdgcn_s_memrealtime() - t) >= 0) break; };
    const char* hp[3];
    UNROLL for (int t = 0; t < 3; ++t) hp[t] = ws + oo[t] + S_H;
    u64 h0, h1, h2;
#define HLOAD() asm volatile("s_load_dwordx2 %0, %3, 0x0 glc\n\ts_load_dwordx2 %1, %4, 0x0 glc\n\ts_load_dwordx2 %2, %5, 0x0 glc" : "=&s"(h0), "=&s"(h1), "=&s"(h2) : "s"(hp[0]), "s"(hp[1]), "s"(hp[2]) : "memory")
#pragma unroll 1
    for (int i = 1; i < 4; ++i) unitW(i, rslot(i + 1));
    LBAR(8);
    if (lw == 0) {
      if (HL_AT == 0) HLOAD();
      unitW(4, rslot(5));
      if (HL_AT == 1) HLOAD();
      unitW(5, rslot(6));
      if (HL_AT == 2) HLOAD();
#pragma unroll 1
      for (unsigned spin = 0; spin < 4096; ++spin) {
        asm volatile("s_waitcnt lgkmcnt(0)" : "+s"(h0), "+s"(h1), "+s"(h2) :: "memory");
        DG(2);
        if ((unsigned)h0 == tag && (unsigned)h1 == tag && (unsigned)h2 == tag) break;
        HLOAD();
      }
      asm volatile("s_waitcnt lgkmcnt(0)" : "+s"(h0), "+s"(h1), "+s"(h2) :: "memory");
    } else { unitW(4, rslot(5)); unitW(5, rslot(6)); }
    LBAR(8);
    ST(4);
    OLOAD(0); OLOAD(1);
    LBAR(10);
    unitW(6, rslot(7));
    OPUT(0, 9);
    asm volatile("s_waitcnt lgkmcnt(0)\n\ts_barrier" ::: "memory"); LTS();
    unitW(7, L_P);
    OLOAD(2);
    if (H2_AT == 3) hint(128);
    OPUT(1, 13 + (H2_AT == 3));
    if (lw == 2) asm volatile("ds_write_b32 %0, %1" :: "v"(L_Q), "v"(0) : "memory");
    asm volatile("s_waitcnt vmcnt(%0) lgkmcnt(0)\n\ts_barrier" :: "i"(5 + (H2_AT == 3)) : "memory"); LTS();
    if (H2_AT == 4) hint(128);
    OPUT(2, 1);
    asm volatile("s_waitcnt lgkmcnt(0)\n\ts_barrier" ::: "memory"); LTS();
    ST(5);
    if (lw == 2) {
      const char* hb = ws + (size_t)(j * 8) * SLAB + S_H + 128;
      u64 h[8];
#pragma unroll 1
      for (unsigned spin = 0; spin < 4096; ++spin) {
        asm volatile("s_load_dwordx2 %0, %8, %9 glc\n\ts_load_dwordx2 %1, %8, %10 glc\n\ts_load_dwordx2 %2, %8, %11 glc\n\ts_load_dwordx2 %3, %8, %12 glc\n\t"
                     "s_load_dwordx2 %4, %8, %13 glc\n\ts_load_dwordx2 %5, %8, %14 glc\n\ts_load_dwordx2 %6, %8, %15 glc\n\ts_load_dwordx2 %7, %8, %16 glc\n\ts_waitcnt lgkmcnt(0)"
                     : "=&s"(h[0]), "=&s"(h[1]), "=&s"(h[2]), "=&s"(h[3]), "=&s"(h[4]), "=&s"(h[5]), "=&s"(h[6]), "=&s"(h[7])
                     : "s"(hb), "i"(0), "i"(SLAB), "i"(2 * SLAB), "i"(3 * SLAB), "i"(4 * SLAB), "i"(5 * SLAB), "i"(6 * SLAB), "i"(7 * SLAB) : "memory");
        unsigned d = 0; DG(0);
        UNROLL for (int i = 0; i < 8; ++i) d |= (unsigned)h[i] ^ tag;
        if (d == 0) break;
      }
      ST(7);
      const unsigned t0 = (unsigned)(h[0] >> 32); int m = 0;
      UNROLL for (int i = 1; i < 8; ++i) m = max(m, (int)((unsigned)(h[i] >> 32) - t0));
      until(t0 + m + C2);
      asm volatile("ds_write_b32 %0, %1" :: "v"(L_Q), "v"(1) : "memory");
    }
    if (lw == 0) {
      {
        const unsigned frag = ((hh >> 1) * 2 + (xcd >> 2)) * 2 + (hh & 1), pl = 32 * ((l >> 3) & 1) + 8 * (xcd & 3) + (l & 7);
        const unsigned pb = (unsigned)((b * 4 + (l >> 4)) * 8) * SLAB;
        u32x4 v[8];
        unsigned ad = pb + S_T + frag * 1024 + pl * 16, ac = pb + S_TC + frag * 256 + (xcd & 3) * 64 + (l & 15) * 4;
        asm volatile("s_sleep %2" : "+v"(ad), "+v"(ac) : "i"(T_DELAY) : "memory");
#pragma unroll 1
        for (unsigned spin = 0; spin < SPIN_CAP; ++spin) {
          asm volatile("" ::: "memory");
          unsigned c[8], bad = 0; DG(1);
          UNROLL for (int p = 0; p < 8; ++p) { v[p] = __builtin_amdgcn_raw_buffer_load_b128(rw, ad, p * SLAB, SC1);
                                               c[p] = __builtin_amdgcn_raw_buffer_load_b32(rw, ac, p * SLAB, SC1); }
          UNROLL for (int p = 0; p < 8; ++p) bad |= csum(tag, v[p]) ^ c[p];
          if (__all(bad == 0)) break;
          __builtin_amdgcn_s_sleep(BACKOFF);
        }
        if ((w & 31) == 0 && l == 0) st32(tag, rw, W_EPO + b * 256);
        float a[8] = {};
        UNROLL for (int p = 0; p < 8; ++p) UNROLL for (int i = 0; i < 4; ++i) { a[2 * i] += blo(v[p][i]); a[2 * i + 1] += bhi(v[p][i]); }
        float e[4];
        UNROLL for (int i = 0; i < 4; ++i) e[i] = sum16(a[i], a[4 + i]);
        const int n = 512 + 16 * hh + 8 * ((l >> 4) & 1) + 4 * ((l >> 3) & 1) + 2 * (l >> 5);
        *(unsigned*)(out + (size_t)(b * 64 + 8 * xcd + (l & 7)) * 1152 + n * 2) = pk(sum32(e[0], e[2]), sum32(e[1], e[3]));
      }
    }
    ST(6);
    return;
  }
  const int cw = wv - 4, r = l & 31, hf = l >> 5, kt = cw >> 1, qt = cw & 1;
  const unsigned slab = (unsigned)w * SLAB;
  const unsigned hoff = l < 16 && cw == 0 ? slab + S_H + l * 8 : 0x40000000u;
  if (PREF && cw == 0) {
    const char* p = (const char*)(pc & ~127ull) + ((j & 7) * (CODE_LINES / 8) + (l < CODE_LINES / 8 ? l : 0)) * 128;
    if (PREF & 2) {
      const int e = l & 1;
      if ((l >> 1) == 16) p = kc + kvo + e * 130944;
      if ((l >> 1) == 17) p = vc + kvo + e * 130944;
      if ((l >> 1) == 18) p = wo + (size_t)xcd * 512 * 1152 + e * 511 * 1152;
      if ((l >> 1) == 19) p = ws + b * 32 * SLAB + e * (32 * SLAB - 128);
      if ((l >> 1) == 20) p = out + (size_t)(b * 64 + 8 * xcd + e * 7) * 1152;
      if ((l >> 1) == 21) p = x + b * 73728 + e * 73600;
      if (l >= 48 && l < 53) p = wq + h * 256 + (size_t)(l - 48) * 143 * 8192;
    }
    asm volatile("s_mov_b32 m0, %1\n\ts_nop 0\n\tglobal_load_lds_dword %0, off" :: "v"(p), "i"(73728) : "memory");
  }
  const unsigned tag = *(const CAS unsigned*)(ws + W_EPO + b * 256) + 0x9E3779B9u;
  const int qq = (l & 15) >> 2, pp = l & 3, blk = (l >> 4) & 1;
  const int trc = 16 * ((4 * cw + 2 * blk + (pp >> 1)) ^ (qq << 2)) + 8 * (pp & 1);
  asm volatile("" :: "s"(tag));
  if (PREW) { __builtin_amdgcn_raw_buffer_store_b64(u32x2{~tag, ~tag}, rw, hoff, 0, SC1); __builtin_amdgcn_raw_buffer_store_b64(u32x2{~tag, ~tag}, rw, hoff + 128, 0, SC1); }
  {
    f32x16 qa0 = {}, qa1 = {};
    LAS char* aw = opq(sm + 8192 + (8 * hf + qq) * 256 + trc);
    LAS char* ax[4];
    UNROLL for (int s = 0; s < 4; ++s) ax[s] = sm + ((r * 128 + 16 * (hf ^ ((r >> 1) & 7))) ^ (32 * s));
    bf16x8 fa[2][4], f0[2][4], f1[2][4];
    UNROLL for (int i = 0; i < 9; ++i) {
      CBAR();
      const int so = (i % 3) * 24576, cur = i & 1, prv = cur ^ 1;
      UNROLL for (int s = 0; s < 4; ++s) {
        if (i > 0) { if (s & 1) qa1 = MFMA(fa[prv][s >> 1], f1[prv][s >> 1], qa1); else qa0 = MFMA(fa[prv][s >> 1], f0[prv][s >> 1], qa0); }
        FENCE();
        fa[cur][s] = rdtr(aw + so + s * 4096, 1024); f0[cur][s] = rd128(ax[s] + so); f1[cur][s] = rd128(ax[s] + so + 4096);
        FENCE();
      }
      if (i > 0) { UNROLL for (int s = 2; s < 4; ++s) { qa0 = MFMA(fa[prv][s], f0[prv][s], qa0); qa1 = MFMA(fa[prv][s], f1[prv][s], qa1); } }
    }
    FENCE();
    UNROLL for (int s = 0; s < 4; ++s) qa0 = MFMA(fa[0][s], f0[0][s], qa0);
    FENCE();
    UNROLL for (int s = 0; s < 4; ++s) qa1 = MFMA(fa[0][s], f1[0][s], qa1);
    FENCE();
    LAS char* q = opq(sm + L_Q + cw * 4096 + r * 16 + 8 * hf);
    UNROLL for (int s = 0; s < 2; ++s) { const u32x4 p0 = pk8(qa0, s); *(LAS u32x2*)(q + s * 2048) = u32x2{p0.x, p0.y}; *(LAS u32x2*)(q + s * 2048 + 512) = u32x2{p0.z, p0.w}; }
    FENCE();
    UNROLL for (int s = 0; s < 2; ++s) { const u32x4 p1 = pk8(qa1, s); *(LAS u32x2*)(q + s * 2048 + 1024) = u32x2{p1.x, p1.y}; *(LAS u32x2*)(q + s * 2048 + 1536) = u32x2{p1.z, p1.w}; }
    asm volatile("s_waitcnt vmcnt(0)" ::: "memory");
  }
  ST(1);
  constexpr float CS = 0.08838834764831845f * 1.4426950408889634f;
  f32x16 S[8];
  float mu[8], su[8];
  LAS char* pr = opq(sm + L_P + l * 16);
  LAS char* pq = opq(sm + L_Q + qt * 1024 + l * 16);
#define SM_MAX(u) do { float a_ = max3(S[u][0], S[u][1], S[u][2]), b_ = max3(S[u][3], S[u][4], S[u][5]); \
    a_ = max3(a_, S[u][6], S[u][7]); b_ = max3(b_, S[u][8], S[u][9]); a_ = max3(a_, S[u][10], S[u][11]); b_ = max3(b_, S[u][12], S[u][13]); \
    a_ = max3(a_, S[u][14], S[u][15]); mu[u] = -CS * max2(a_, b_); PIN(mu[u]); } while (0)
#define SM_F(u) do { UNROLL for (int i = 0; i < 16; i += 2) { f32x2 t_ = {S[u][i], S[u][i + 1]}; t_ = __builtin_elementwise_fma(t_, f32x2{CS, CS}, f32x2{mu[u], mu[u]}); PIN(t_); S[u][i] = t_.x; S[u][i + 1] = t_.y; } } while (0)
#define SM_E(u, i) do { f32x2 t_ = {__builtin_amdgcn_exp2f(S[u][i]), __builtin_amdgcn_exp2f(S[u][i + 1])}; PIN(t_); S[u][i] = t_.x; S[u][i + 1] = t_.y; } while (0)
#define SM_A(u) do { f32x2 a_ = f32x2{S[u][0], S[u][1]} + f32x2{S[u][2], S[u][3]}, b_ = f32x2{S[u][4], S[u][5]} + f32x2{S[u][6], S[u][7]}; \
    a_ += f32x2{S[u][8], S[u][9]}; b_ += f32x2{S[u][10], S[u][11]}; a_ += f32x2{S[u][12], S[u][13]}; b_ += f32x2{S[u][14], S[u][15]}; a_ += b_; su[u] = a_.x + a_.y; PIN(su[u]); } while (0)
  {
    bf16x8 qf[8];
    LAS char* ak[8];
    UNROLL for (int s = 0; s < 8; ++s) ak[s] = opq(sm + 98304 + (((32 * kt + r) * 256 + 16 * (hf ^ (r & 15))) ^ (32 * s)));
    UNROLL for (int u = 0; u < 8; ++u) {
      CBAR();
      bf16x8 kf[8];
      f32x16 a = {};
      const int v = u > 0 ? u - 1 : 0, v2 = u > 1 ? u - 2 : 0;
      UNROLL for (int s = 0; s < 4; ++s) { if (u == 0) qf[s] = rd128(pq + s * 2048); kf[s] = rd128(ak[s] + (u & 3) * 16384); }
      FENCE();
      if (u > 1) SM_A(v2);
      FENCE();
      if (u > 0) { SM_MAX(v); SM_F(v); }
      UNROLL for (int s = 0; s < 8; ++s) {
        MF(a, kf[s], qf[s]);
        if (s < 4) { if (u == 0) qf[s + 4] = rd128(pq + (s + 4) * 2048); kf[s + 4] = rd128(ak[s + 4] + (u & 3) * 16384); }
        if (u > 0) SM_E(v, 2 * s);
        if (u == 3 && s >= 4) { ak[2 * s - 8] = opq(ak[2 * s - 8] - 98304); ak[2 * s - 7] = opq(ak[2 * s - 7] - 98304); }
      }
      S[u] = a;
    }
  }
  LAS f32x2* stm = (LAS f32x2*)opq(sm + L_Q + r * 8);
  {
    FENCE();
    float mw = min3(min3(mu[0], mu[1], mu[2]), min3(mu[3], mu[4], mu[5]), mu[6]);
    SM_A(6);
    FENCE();
    SM_MAX(7);
    mw = swap32min(min2(mw, mu[7]));
    FENCE();
    SM_F(7);
    UNROLL for (int i = 0; i < 16; i += 2) SM_E(7, i);
    UNROLL for (int u = 0; u < 8; ++u) mu[u] = __builtin_amdgcn_exp2f(mw - mu[u]);
    FENCE();
    SM_A(7);
    float lw_ = mu[0] * su[0], lx = mu[1] * su[1];
    UNROLL for (int u = 2; u < 8; u += 2) { lw_ = __builtin_fmaf(mu[u], su[u], lw_); lx = __builtin_fmaf(mu[u + 1], su[u + 1], lx); }
    lw_ = sum32(lw_ + lx, lw_ + lx);
    stm[cw * 32] = f32x2{mw, lw_};
    CBAR();
    ST(2);
    const f32x2 ot = stm[(cw ^ 2) * 32];
    const float mg = min2(mw, ot.x), ea = __builtin_amdgcn_exp2f(mg - mw), eb = __builtin_amdgcn_exp2f(mg - ot.x);
    const float g = ea * __builtin_amdgcn_rcpf(__builtin_fmaf(ea, lw_, eb * ot.y));
    UNROLL for (int u = 0; u < 8; ++u) mu[u] *= g;
  }
  LAS char* pw = opq(sm + L_P + kt * 4096 + qt * 1024 + l * 16);
  unsigned pd[8];
#define PV_C(u, k) do { float x_ = S[u][2 * (k)] * mu[u]; PIN(x_); float y_ = S[u][2 * (k) + 1] * mu[u]; PIN(y_); pd[k] = pk(x_, y_); PIN(pd[k]); } while (0)
#define PV_C2(u, k) do { float x_ = S[u][2 * (k)] * mu[u], y_ = S[u][2 * (k) + 1] * mu[u], z_ = S[u][2 * (k) + 2] * mu[u], w_ = S[u][2 * (k) + 3] * mu[u]; \
    asm volatile("" : "+v"(x_), "+v"(z_)); asm volatile("" : "+v"(y_), "+v"(w_)); pd[k] = pk(x_, y_); pd[k + 1] = pk(z_, w_); asm volatile("" : "+v"(pd[k]), "+v"(pd[k + 1])); } while (0)
#define PV_W(u, h) do { *(LAS u32x4*)(pw + ((u) & 1) * 8192 + (h) * 2048) = u32x4{pd[4 * (h)], pd[4 * (h) + 1], pd[4 * (h) + 2], pd[4 * (h) + 3]}; FENCE(); } while (0)
  S[0] *= mu[0];
  *(LAS u32x4*)pw = pk8(S[0], 0); *(LAS u32x4*)(pw + 2048) = pk8(S[0], 1);
  FENCE();
  S[1] *= mu[1];
  UNROLL for (int k = 0; k < 8; ++k) { pd[k] = pk(S[1][2 * k], S[1][2 * k + 1]); PIN(pd[k]); }
  f32x16 o0 = {}, o1 = {};
  LAS char* av1 = opq(sm + (4 * hf + qq) * 256 + trc);
  LAS char* av0 = opq(av1 + 98304);
  UNROLL for (int u = 0; u < 7; ++u) {
    CBAR();
    bf16x8 va[4], p0[4], p1[4];
    const int v = u < 6 ? u + 2 : 7;
#define PV_RD(ks) do { va[ks] = rdtr((u < 4 ? av0 : av1) + (u & 3) * 16384 + (ks) * 4096, 2048); \
      p0[ks] = rd128(pr + (u & 1) * 8192 + (ks) * 2048); p1[ks] = rd128(pr + (u & 1) * 8192 + (ks) * 2048 + 1024); FENCE(); } while (0)
    PV_RD(0); PV_RD(1);
    MF(o0, va[0], p0[0]); PV_RD(2); MF(o1, va[0], p1[0]); PV_RD(3);
    MF(o0, va[1], p0[1]); PV_W(u + 1, 0); MF(o1, va[1], p1[1]); PV_W(u + 1, 1);
    MF(o0, va[2], p0[2]); if (u < 6) PV_C2(v, 0);
    MF(o1, va[2], p1[2]); if (u < 6) PV_C2(v, 2);
    MF(o0, va[3], p0[3]); if (u < 6) PV_C2(v, 4);
    MF(o1, va[3], p1[3]); if (u < 6) PV_C2(v, 6);
  }
  {
    CBAR();
    bf16x8 va[4], p0[4], p1[4];
    UNROLL for (int ks = 0; ks < 6; ++ks) {
      if (ks >= 2) { o0 = MFMA(va[ks - 2], p0[ks - 2], o0); FENCE(); }
      if (ks < 4) { va[ks] = rdtr(av1 + 49152 + ks * 4096, 2048); p0[ks] = rd128(pr + 8192 + ks * 2048); }
      else { p1[2 * ks - 8] = rd128(pr + 8192 + (2 * ks - 8) * 2048 + 1024); p1[2 * ks - 7] = rd128(pr + 8192 + (2 * ks - 7) * 2048 + 1024); }
      FENCE();
    }
    const int off = cw * 4096 + l * 16;
    LAS char* d = opq(sm + L_Q + off);
    o1 = MFMA(va[0], p1[0], o1); o1 = MFMA(va[1], p1[1], o1);
    FENCE();
    const u32x4 a0 = pk8(o0, 0);
    st128(a0, rw, slab + S_O + off);
    FENCE();
    o1 = MFMA(va[2], p1[2], o1);
    FENCE();
    const u32x4 a1 = pk8(o0, 1);
    st128(a1, rw, slab + S_O + off + 2048);
    FENCE();
    o1 = MFMA(va[3], p1[3], o1);
    FENCE();
    unsigned ck = csum(csum(tag, a0), a1); PIN(ck);
    *(LAS u32x4*)d = a0;
    FENCE();
    const u32x4 b0 = pk8(o1, 0), b1 = pk8(o1, 1);
    st128(b0, rw, slab + S_O + off + 1024); st128(b1, rw, slab + S_O + off + 3072);
    st32(csum(csum(ck, b0), b1), rw, slab + S_OC + cw * 256 + l * 4);
    __builtin_amdgcn_raw_buffer_store_b64(u32x2{tag, tag}, rw, hoff, 0, SC1);
    FENCE();
    *(LAS u32x4*)(d + 2048) = a1; *(LAS u32x4*)(d + 1024) = b0; *(LAS u32x4*)(d + 3072) = b1;
  }
  ST(3);
  bf16x8 wf[32];
  LAS char* av2 = opq(av1 + L_P);
  LAS char* po = opq(pr + (slot_addr(16) - L_P));
  auto loadW1 = [&](int i, int ks) { wf[4 * i + ks] = rdtr((i < 3 ? av0 + (i + 1) * 16384 : i < 7 ? av1 + (i - 3) * 16384 : av2) + ks * 4096, 2048); };
  f32x16 c0 = {}, c1 = {};
  auto head = [&](int t, LAS char* p, int wnext) {
    bf16x8 g0[8], g1[8];
    UNROLL for (int ks = 0; ks < 10; ++ks) {
      if (ks >= 2) MF(c0, wf[8 * t + ks - 2], g0[ks - 2]);
      if (ks < 8) { g0[ks] = rd128(p + ks * 2048); g1[ks] = rd128(p + ks * 2048 + 1024); FENCE(); }
      if (ks >= 2) { MF(c1, wf[8 * t + ks - 2], g1[ks - 2]);
        if (wnext >= 0) { loadW1(wnext + ((ks - 2) >> 2), (ks - 2) & 3); FENCE(); } }
    }
  };
  CBAR();
  u32x4 t0, t1;
  {
    f32x16 t = {};
    LAS char* at = opq(sm + slot_addr(16) + (4 * hf + qq) * 128 + 16 * ((4 * kt + 2 * blk + (pp >> 1)) ^ ((qq >> 1) << 2)) + 8 * (pp & 1));
    bf16x8 ta[8], tb[8];
    UNROLL for (int ks = 0; ks < 11; ++ks) {
      if (ks >= 3) MF(t, ta[ks - 3], tb[ks - 3]);
      if (ks < 8) { ta[ks] = rdtr(at + ks * 2048, 1024); tb[ks] = rd128(pq + ks * 2048); }
      if (ks >= 7) { loadW1((ks - 7) >> 1, ((ks - 7) & 1) * 2); loadW1((ks - 7) >> 1, ((ks - 7) & 1) * 2 + 1); }
      FENCE();
    }
    head(0, pr + (L_Q - L_P), -1);
    t0 = pk8(t, 0); t1 = pk8(t, 1);
  }
  CBAR();
  UNROLL for (int i = 0; i < 8; ++i) loadW1(2 + (i >> 2), i & 3);
  unsigned tc0 = csum(tag, t0), tc1 = csum(tag, t1);
  const unsigned cl = ((r >> 3) * 16 + hf * 8 + (r & 7)) * 4;
  unsigned aT = slab + S_T + cw * 2048 + l * 16, aTC = slab + S_TC + cw * 512 + cl, aM = slab + S_M + cw * 4096 + l * 16, aMC = slab + S_MC + cw * 512 + cl;
  const unsigned tile = cw * 2 + (xcd >> 2), pb = (unsigned)(j * 8 + 2 * (l >> 4)) * SLAB;
  unsigned aR = pb + S_M + tile * 2048 + (32 * ((l >> 3) & 1) + 8 * (xcd & 3) + (l & 7)) * 16, aRC = pb + S_MC + tile * 256 + (xcd & 3) * 64 + (l & 15) * 4;
  unsigned aO = (unsigned)(b * 64 + 8 * xcd + (l & 7)) * 1152 + (128 * hh + 32 * cw + 16 * blk + 8 * hf + 4 * ((l >> 3) & 1)) * 2;
  asm volatile("" : "+v"(tc0), "+v"(tc1), "+v"(aT), "+v"(aTC), "+v"(aM), "+v"(aMC), "+v"(aR), "+v"(aRC), "+v"(aO));
  CBAR();
  UNROLL for (int i = 0; i < 8; ++i) loadW1(4 + (i >> 2), i & 3);
  CBAR();
  st128(t0, rw, aT); st128(t1, rw, aT + 1024); st32(tc0, rw, aTC); st32(tc1, rw, aTC + 256);
  FENCE();
  head(1, po, -1);
  CBAR(); head(2, po + 16384, 6);
  CBAR(); head(3, po + 32768, -1);
  ST(4);
  {
    const u32x4 a0 = pk8(c0, 0), a1 = pk8(c0, 1);
    st128(a0, rw, aM); st128(a1, rw, aM + 1024);
    FENCE();
    const u32x4 b0 = pk8(c1, 0), b1 = pk8(c1, 1);
    st128(b0, rw, aM + 2048); st128(b1, rw, aM + 3072);
    FENCE();
    st32(csum(csum(tag, a0), a1), rw, aMC); st32(csum(csum(tag, b0), b1), rw, aMC + 256);
  }
  ST(5);
  {
#pragma unroll 1
    for (unsigned spin = 0; spin < SPIN_CAP; ++spin) if (__builtin_amdgcn_readfirstlane(*(volatile LAS unsigned*)(sm + L_Q))) break;
    ST(7);
    u32x4 v[2][2];
#pragma unroll 1
    for (unsigned spin = 0; spin < SPIN_CAP; ++spin) {
      asm volatile("" ::: "memory");
      unsigned c[2]; DG(1);
      UNROLL for (int p = 0; p < 2; ++p) {
        v[p][0] = __builtin_amdgcn_raw_buffer_load_b128(rw, aR, p * SLAB, SC1);
        v[p][1] = __builtin_amdgcn_raw_buffer_load_b128(rw, aR + 1024, p * SLAB, SC1);
        c[p] = __builtin_amdgcn_raw_buffer_load_b32(rw, aRC, p * SLAB, SC1);
      }
      if (__all(((csum(csum(tag, v[0][0]), v[0][1]) ^ c[0]) | (csum(csum(tag, v[1][0]), v[1][1]) ^ c[1])) == 0)) break;
      __builtin_amdgcn_s_sleep(BACKOFF);
    }
    float e[8];
    UNROLL for (int i = 0; i < 4; ++i) {
      e[2 * i] = sum16(blo(v[0][0][i]) + blo(v[1][0][i]), blo(v[0][1][i]) + blo(v[1][1][i]));
      e[2 * i + 1] = sum16(bhi(v[0][0][i]) + bhi(v[1][0][i]), bhi(v[0][1][i]) + bhi(v[1][1][i]));
    }
    *(u32x2*)(out + aO) = u32x2{pk(sum32(e[0], e[4]), sum32(e[1], e[5])), pk(sum32(e[2], e[6]), sum32(e[3], e[7]))};
  }
  ST(6);
}

extern "C" void kernel_launch(void* const* d_in, const int* in_sizes, int n_in, void* d_out, int out_size, void* d_ws, size_t ws_size, hipStream_t stream) {
  gqa_fused<<<256, 512, 0, stream>>>((const char*)d_in[0], (const char*)d_in[1], (const char*)d_in[2], (const char*)d_in[3], (const char*)d_in[4], (char*)d_out, (char*)d_ws);
}
```

```cpp
#define STAMP
#include <hip/hip_runtime.h>
#include <hip/hip_bf16.h>

typedef unsigned long long u64;
typedef short bf16x8 __attribute__((ext_vector_type(8)));
typedef short s16x4 __attribute__((ext_vector_type(4)));
typedef float f32x16 __attribute__((ext_vector_type(16)));
typedef float f32x2 __attribute__((ext_vector_type(2)));
typedef __bf16 bf16x2 __attribute__((ext_vector_type(2)));
typedef unsigned u32x4 __attribute__((ext_vector_type(4)));
typedef unsigned u32x2 __attribute__((ext_vector_type(2)));
#define LAS __attribute__((address_space(3)))
#define CAS __attribute__((address_space(4)))
#define DI __device__ __forceinline__
typedef __amdgpu_buffer_rsrc_t rsrc_t;

constexpr int L_P = 65536, L_Q = 81920;
constexpr int slot_addr(int g) { return g == 24 ? L_P : (g & 7) < 4 ? 98304 + (g & 7) * 16384 : ((g & 7) - 4) * 16384; }
constexpr unsigned S_O = 0, S_OC = 16384, S_T = 17408, S_TC = 25600, S_M = 27648, S_MC = 44032, S_H = 46080, SLAB = 46592;
constexpr unsigned W_EPO = 12u << 20, W_BYTES = W_EPO + 4096;
constexpr int SC1 = 16, NT = 2;
constexpr unsigned SPIN_CAP = 1u << 20;
#define TOUCH_AT 104
#define TOUCH_POL 2
#define W_POL 2
#define RETOUCH 7
#define RE_POL 16
#define T1_AT 3
#define H2_DELAY 8
#define P2_DELAY 4
#define C1 110
#define C2 200
#define HL_AT 2
#define PREW 1
#define BACKOFF 4
#define H2_AT 3
#define T_DELAY 14
#define PREF 3
#define CODE_LINES 152

#define DG(i) do {} while (0)
#define STAMP_OFF (32u << 20)
#define STAMP_N 256
#define STAMP_W 16
#define ST(i) do { st_[i] = __builtin_amdgcn_s_memrealtime(); } while (0)

DI rsrc_t mk(const void* p, unsigned n) { return __builtin_amdgcn_make_buffer_rsrc((void*)p, 0, n, 0x00020000); }
#define DMA(R, LDSOFF, V, SOFF, IMM, POL) __builtin_amdgcn_raw_ptr_buffer_load_lds(R, (LAS void*)(sm + (LDSOFF)), 16, V, SOFF, IMM, POL)
#define FENCE() __builtin_amdgcn_sched_barrier(0)
#define LTS() do {} while (0)
#define LBAR(N) do { asm volatile("s_waitcnt vmcnt(%0)\n\ts_barrier" :: "i"(N) : "memory"); LTS(); } while (0)
#define CBAR() CBAR0()
#define CBAR0() do { FENCE(); asm volatile("" ::: "memory"); __builtin_amdgcn_s_waitcnt(0xC07F); __builtin_amdgcn_s_barrier(); asm volatile("" ::: "memory"); FENCE(); } while (0)
#define PIN(x) asm volatile("" : "+v"(x))
DI LAS char* opq(LAS char* p) { asm("" : "+v"(p)); return p; }
#define MFMA(a, b, c) __builtin_amdgcn_mfma_f32_32x32x16_bf16(a, b, c, 0, 0, 0)
#define MF(acc, a, b) do { FENCE(); acc = MFMA(a, b, acc); PIN(acc); FENCE(); } while (0)
#define UNROLL _Pragma("unroll")
DI bf16x8 rd128(LAS char* p) { return *(LAS bf16x8*)p; }
DI bf16x8 rdtr(LAS char* p, int step) {
  s16x4 a = __builtin_amdgcn_ds_read_tr16_b64_v4i16((LAS s16x4*)p), b = __builtin_amdgcn_ds_read_tr16_b64_v4i16((LAS s16x4*)(p + step));
  return __builtin_shufflevector(a, b, 0, 1, 2, 3, 4, 5, 6, 7);
}
DI unsigned pk(float a, float b) { f32x2 v = {a, b}; return __builtin_bit_cast(unsigned, __builtin_convertvector(v, bf16x2)); }
DI u32x4 pk8(const f32x16& x, int s) { return u32x4{pk(x[8 * s], x[8 * s + 1]), pk(x[8 * s + 2], x[8 * s + 3]), pk(x[8 * s + 4], x[8 * s + 5]), pk(x[8 * s + 6], x[8 * s + 7])}; }
DI float max2(float a, float b) { return __builtin_elementwise_maximum(a, b); }
DI float max3(float a, float b, float c) { return max2(max2(a, b), c); }
DI float min2(float a, float b) { return __builtin_elementwise_minimum(a, b); }
DI float min3(float a, float b, float c) { return min2(min2(a, b), c); }
DI float swap32min(float v) { auto r = __builtin_amdgcn_permlane32_swap(__float_as_uint(v), __float_as_uint(v), false, false); return min2(__uint_as_float(r[0]), __uint_as_float(r[1])); }
DI float swap32max(float v) { auto r = __builtin_amdgcn_permlane32_swap(__float_as_uint(v), __float_as_uint(v), false, false); return max2(__uint_as_float(r[0]), __uint_as_float(r[1])); }
DI float sum16(float a, float b) { auto r = __builtin_amdgcn_permlane16_swap(__float_as_uint(a), __float_as_uint(b), false, false); return __uint_as_float(r[0]) + __uint_as_float(r[1]); }
DI float sum32(float a, float b) { auto r = __builtin_amdgcn_permlane32_swap(__float_as_uint(a), __float_as_uint(b), false, false); return __uint_as_float(r[0]) + __uint_as_float(r[1]); }
DI unsigned csum(unsigned c, u32x4 v) { return (c + v.x + v.y) + v.z + v.w; }
DI float blo(unsigned v) { return __uint_as_float(v << 16); }
DI float bhi(unsigned v) { return __uint_as_float(v & 0xffff0000u); }
DI void st128(u32x4 v, rsrc_t r, unsigned off) { __builtin_amdgcn_raw_buffer_store_b128(v, r, off, 0, SC1); }
DI void st32(unsigned v, rsrc_t r, unsigned off) { __builtin_amdgcn_raw_buffer_store_b32(v, r, off, 0, SC1); }

__global__ __launch_bounds__(512, 2) void gqa_fused(const char* __restrict__ x, const char* __restrict__ kc, const char* __restrict__ vc,
                                                    const char* __restrict__ wq, const char* __restrict__ wo, char* __restrict__ out, char* ws) {
  __shared__ __attribute__((aligned(256))) char smem[163840];
  LAS char* sm = (LAS char*)smem;
  u64 pc;
  asm volatile("s_getpc_b64 %0" : "=s"(pc));
  const int w = blockIdx.x, wv = __builtin_amdgcn_readfirstlane(threadIdx.x >> 6), l = threadIdx.x & 63;
  const int xcd = w & 7, j = w >> 3, b = j >> 2, hh = j & 3, h = xcd * 4 + hh;
  u64 st_[8] = {};
  ST(0);
  asm volatile("" :: "s"(x), "s"(kc), "s"(vc), "s"(wq), "s"(wo), "s"(out), "s"(ws));
  const rsrc_t rw = mk(ws, W_BYTES);
  const size_t kvo = (size_t)(b * 16 + (h >> 1)) * 131072;
  if (__builtin_expect(wv < 4, 1)) {
    const int lw = wv;
    LTS();
    const rsrc_t rx = mk(x + b * 73728, 73728), rq = mk(wq + h * 256, 576 * 8192 - h * 256);
    const unsigned vx0 = (16 * lw + (l >> 3)) * 1152 + 16 * ((l & 7) ^ (l >> 4));
    const unsigned vx1 = (16 * lw + 8 + (l >> 3)) * 1152 + 16 * ((l & 7) ^ (4 + (l >> 4)));
    const unsigned sw4 = 16 * ((l & 15) ^ ((l >> 4) << 2));
    const unsigned vq = (16 * lw + (l >> 4)) * 8192 + sw4;
    {
      int c = (b + 2 * hh) & 7, so = 0;
#pragma unroll 1
      for (int i = 0; i < 9; ++i) {
        if (i >= 2) LBAR(6);
        DMA(rx, so + lw * 2048, vx0, c * 128, 0, 0);
        DMA(rx, so + lw * 2048 + 1024, vx1, c * 128, 0, 0);
        UNROLL for (int p = 0; p < 4; ++p) DMA(rq, so + 8192 + lw * 4096 + p * 1024, vq, (c * 64 + p * 4) * 8192, 0, NT);
        if (i == TOUCH_AT - 100) __builtin_amdgcn_raw_ptr_buffer_load_lds(mk(wo + (size_t)xcd * 512 * 1152, 512 * 1152), (LAS void*)(sm + 74752 + lw * 256), 4, (j * 144 + lw * 36 + (l < 36 ? l : 35)) * 128, 0, 0, TOUCH_POL);
        if (i == RETOUCH) __builtin_amdgcn_raw_ptr_buffer_load_lds(mk(wo + (size_t)xcd * 512 * 1152, 512 * 1152), (LAS void*)(sm + 74752 + lw * 256), 4, (j * 144 + lw * 36 + (l < 36 ? l : 35)) * 128, 0, 0, RE_POL);
        c = c == 8 ? 0 : c + 1;
        so = so == 49152 ? 0 : so + 24576;
      }
    }
    const unsigned tag = *(const CAS unsigned*)(ws + W_EPO + b * 256) + 0x9E3779B9u;
    const rsrc_t rk = mk(kc + kvo, 131072), rv = mk(vc + kvo, 131072), ro = mk(wo + (size_t)xcd * 512 * 1152, 512 * 1152);
    const unsigned vk = (4 * lw + (l >> 4)) * 256 + 16 * ((l & 15) ^ (4 * lw + (l >> 4)));
    const unsigned vv = lw * 4096 + (l >> 4) * 256 + sw4;
    const unsigned vo = (16 * lw + (l >> 4)) * 1152 + hh * 256 + sw4;
    const unsigned vt = (128 * hh + 32 * lw + (l >> 3)) * 1152 + 1024 + 16 * ((l & 7) ^ (((l >> 4) & 1) << 2));
    auto rslot = [](int g) { return (g & 4) ? (g & 3) * 16384 : 98304 + (g & 3) * 16384; };
    auto unitK = [&](int u) { const int s = rslot(u) + lw * 1024; UNROLL for (int p = 0; p < 4; ++p) DMA(rk, s + p * 4096, vk, u * 16384 + p * 4096, 0, NT); };
    auto unitV = [&](int u) { const int s = rslot(u) + lw * 4096;
      DMA(rv, s, vv, u * 16384, 0, NT); DMA(rv, s, vv, u * 16384, 1024, NT); DMA(rv, s, vv, u * 16384, 2048, NT); DMA(rv, s, vv, u * 16384, 3072, NT); };
    auto unitW = [&](int i, int s) {
      const int row0 = ((2 * hh + i) & 7) * 64;
      UNROLL for (int p = 0; p < 4; ++p) DMA(ro, s + lw * 4096 + p * 1024, vo, (row0 + p * 4) * 1152, 0, W_POL);
    };
    LBAR(6); unitK(0);
    LBAR(4); unitK(1); unitK(2); unitK(3);
    asm volatile("" :: "s"(tag));
    ST(1);
#pragma unroll 1
    for (int u = 4; u < 8; ++u) { LBAR(12); unitK(u); }
    LBAR(12);
#pragma unroll 1
    for (int u = 0; u < 8; ++u) {
      if (u < 3) LBAR(8); else if (u > 4) LBAR(16);
      unitV(u);
      if (u == 3) { ST(2); asm volatile("s_barrier" ::: "memory"); }
    }
    LBAR(16);
    LBAR(12);
    LBAR(8);
    UNROLL for (int p = 0; p < 4; ++p) DMA(ro, slot_addr(16) + lw * 4096 + p * 1024, vt, p * 8 * 1152, 0, W_POL);
    LBAR(8); unitW(0, slot_addr(17));
    LBAR(8);
    ST(3);
    u32x4 o[3][4]; unsigned oc[3], oo[3];
    UNROLL for (int t = 0; t < 3; ++t) oo[t] = (unsigned)(((b * 4 + ((hh + 1 + t) & 3)) * 8 + xcd)) * SLAB;
    const unsigned vl = S_O + lw * 4096 + l * 16, vc2 = S_OC + lw * 256 + l * 4;
#define OLOAD(t) asm volatile("buffer_load_dwordx4 %0, %5, %7, %8 offen sc1\n\tbuffer_load_dwordx4 %1, %5, %7, %8 offen offset:1024 sc1\n\t" \
                              "buffer_load_dwordx4 %2, %5, %7, %8 offen offset:2048 sc1\n\tbuffer_load_dwordx4 %3, %5, %7, %8 offen offset:3072 sc1\n\t" \
                              "buffer_load_dword %4, %6, %7, %8 offen sc1" \
                              : "=&v"(o[t][0]), "=&v"(o[t][1]), "=&v"(o[t][2]), "=&v"(o[t][3]), "=&v"(oc[t]) : "v"(vl), "v"(vc2), "s"(rw), "s"(oo[t]) : "memory")
#define OWAIT(t, N) asm volatile("s_waitcnt vmcnt(%5)" : "+v"(o[t][0]), "+v"(o[t][1]), "+v"(o[t][2]), "+v"(o[t][3]), "+v"(oc[t]) : "i"(N) : "memory")
#define OPUT(t, N) do { OWAIT(t, N); \
      _Pragma("unroll 1") for (unsigned spin = 0; spin < SPIN_CAP; ++spin) { \
        UNROLL for (int p = 0; p < 4; ++p) asm volatile("ds_write_b128 %0, %1 offset:%2" :: "v"(od), "v"(o[t][p]), "i"(t * 16384 + p * 1024) : "memory"); \
        if (__all(csum(csum(csum(csum(tag, o[t][0]), o[t][1]), o[t][2]), o[t][3]) == oc[t])) break; \
        DG(3); asm volatile("s_waitcnt lgkmcnt(0)" ::: "memory"); OLOAD(t); OWAIT(t, 0); } } while (0)
    LAS char* od = sm + slot_addr(16) + lw * 4096 + l * 16;
    auto hint = [&](unsigned off) { const unsigned t = (unsigned)__builtin_amdgcn_s_memrealtime();
      __builtin_amdgcn_raw_buffer_store_b64(u32x2{tag, t}, rw, l < 16 && lw == 1 ? (unsigned)w * SLAB + S_H + off + l * 8 : 0x40000000u, 0, SC1); };
    auto until = [&](unsigned t) { _Pragma("unroll 1") for (int spin = 0; spin < 256; ++spin) if ((int)((unsigned)__builtin_amdgcn_s_memrealtime() - t) >= 0) break; };
    const char* hp[3];
    UNROLL for (int t = 0; t < 3; ++t) hp[t] = ws + oo[t] + S_H;
    u64 h0, h1, h2;
#define HLOAD() asm volatile("s_load_dwordx2 %0, %3, 0x0 glc\n\ts_load_dwordx2 %1, %4, 0x0 glc\n\ts_load_dwordx2 %2, %5, 0x0 glc" : "=&s"(h0), "=&s"(h1), "=&s"(h2) : "s"(hp[0]), "s"(hp[1]), "s"(hp[2]) : "memory")
#pragma unroll 1
    for (int i = 1; i < 4; ++i) unitW(i, rslot(i + 1));
    LBAR(8);
    if (lw == 0) {
      if (HL_AT == 0) HLOAD();
      unitW(4, rslot(5));
      if (HL_AT == 1) HLOAD();
      unitW(5, rslot(6));
      if (HL_AT == 2) HLOAD();
#pragma unroll 1
      for (unsigned spin = 0; spin < 4096; ++spin) {
        asm volatile("s_waitcnt lgkmcnt(0)" : "+s"(h0), "+s"(h1), "+s"(h2) :: "memory");
        DG(2);
        if ((unsigned)h0 == tag && (unsigned)h1 == tag && (unsigned)h2 == tag) break;
        HLOAD();
      }
      asm volatile("s_waitcnt lgkmcnt(0)" : "+s"(h0), "+s"(h1), "+s"(h2) :: "memory");
    } else { unitW(4, rslot(5)); unitW(5, rslot(6)); }
    LBAR(8);
    ST(4);
    OLOAD(0); OLOAD(1);
    LBAR(10);
    unitW(6, rslot(7));
    OPUT(0, 9);
    asm volatile("s_waitcnt lgkmcnt(0)\n\ts_barrier" ::: "memory"); LTS();
    unitW(7, L_P);
    OLOAD(2);
    if (H2_AT == 3) hint(128);
    OPUT(1, 13 + (H2_AT == 3));
    if (lw == 2) asm volatile("ds_write_b32 %0, %1" :: "v"(L_Q), "v"(0) : "memory");
    asm volatile("s_waitcnt vmcnt(%0) lgkmcnt(0)\n\ts_barrier" :: "i"(5 + (H2_AT == 3)) : "memory"); LTS();
    if (H2_AT == 4) hint(128);
    OPUT(2, 1);
    asm volatile("s_waitcnt lgkmcnt(0)\n\ts_barrier" ::: "memory"); LTS();
    ST(5);
    if (lw == 2) {
      const char* hb = ws + (size_t)(j * 8) * SLAB + S_H + 128;
      u64 h[8];
#pragma unroll 1
      for (unsigned spin = 0; spin < 4096; ++spin) {
        asm volatile("s_load_dwordx2 %0, %8, %9 glc\n\ts_load_dwordx2 %1, %8, %10 glc\n\ts_load_dwordx2 %2, %8, %11 glc\n\ts_load_dwordx2 %3, %8, %12 glc\n\t"
                     "s_load_dwordx2 %4, %8, %13 glc\n\ts_load_dwordx2 %5, %8, %14 glc\n\ts_load_dwordx2 %6, %8, %15 glc\n\ts_load_dwordx2 %7, %8, %16 glc\n\ts_waitcnt lgkmcnt(0)"
                     : "=&s"(h[0]), "=&s"(h[1]), "=&s"(h[2]), "=&s"(h[3]), "=&s"(h[4]), "=&s"(h[5]), "=&s"(h[6]), "=&s"(h[7])
                     : "s"(hb), "i"(0), "i"(SLAB), "i"(2 * SLAB), "i"(3 * SLAB), "i"(4 * SLAB), "i"(5 * SLAB), "i"(6 * SLAB), "i"(7 * SLAB) : "memory");
        unsigned d = 0; DG(0);
        UNROLL for (int i = 0; i < 8; ++i) d |= (unsigned)h[i] ^ tag;
        if (d == 0) break;
      }
      ST(7);
      const unsigned t0 = (unsigned)(h[0] >> 32); int m = 0;
      UNROLL for (int i = 1; i < 8; ++i) m = max(m, (int)((unsigned)(h[i] >> 32) - t0));
      until(t0 + m + C2);
      asm volatile("ds_write_b32 %0, %1" :: "v"(L_Q), "v"(1) : "memory");
    }
    if (lw == 0) {
      {
        const unsigned frag = ((hh >> 1) * 2 + (xcd >> 2)) * 2 + (hh & 1), pl = 32 * ((l >> 3) & 1) + 8 * (xcd & 3) + (l & 7);
        const unsigned pb = (unsigned)((b * 4 + (l >> 4)) * 8) * SLAB;
        u32x4 v[8];
        unsigned ad = pb + S_T + frag * 1024 + pl * 16, ac = pb + S_TC + frag * 256 + (xcd & 3) * 64 + (l & 15) * 4;
        asm volatile("s_sleep %2" : "+v"(ad), "+v"(ac) : "i"(T_DELAY) : "memory");
#pragma unroll 1
        for (unsigned spin = 0; spin < SPIN_CAP; ++spin) {
          asm volatile("" ::: "memory");
          unsigned c[8], bad = 0; DG(1);
          UNROLL for (int p = 0; p < 8; ++p) { v[p] = __builtin_amdgcn_raw_buffer_load_b128(rw, ad, p * SLAB, SC1);
                                               c[p] = __builtin_amdgcn_raw_buffer_load_b32(rw, ac, p * SLAB, SC1); }
          UNROLL for (int p = 0; p < 8; ++p) bad |= csum(tag, v[p]) ^ c[p];
          if (__all(bad == 0)) break;
          __builtin_amdgcn_s_sleep(BACKOFF);
        }
        if ((w & 31) == 0 && l == 0) st32(tag, rw, W_EPO + b * 256);
        float a[8] = {};
        UNROLL for (int p = 0; p < 8; ++p) UNROLL for (int i = 0; i < 4; ++i) { a[2 * i] += blo(v[p][i]); a[2 * i + 1] += bhi(v[p][i]); }
        float e[4];
        UNROLL for (int i = 0; i < 4; ++i) e[i] = sum16(a[i], a[4 + i]);
        const int n = 512 + 16 * hh + 8 * ((l >> 4) & 1) + 4 * ((l >> 3) & 1) + 2 * (l >> 5);
        *(unsigned*)(out + (size_t)(b * 64 + 8 * xcd + (l & 7)) * 1152 + n * 2) = pk(sum32(e[0], e[2]), sum32(e[1], e[3]));
      }
    }
    ST(6);
    if (threadIdx.x == 0) for (int i = 0; i < 7; ++i) ((u64*)(ws + STAMP_OFF))[w * STAMP_W + i] = st_[i] ? st_[i] : st_[0];
    if (threadIdx.x == 128) ((u64*)(ws + STAMP_OFF))[w * STAMP_W + 7] = st_[7];
    return;
  }
  const int cw = wv - 4, r = l & 31, hf = l >> 5, kt = cw >> 1, qt = cw & 1;
  const unsigned slab = (unsigned)w * SLAB;
  const unsigned hoff = l < 16 && cw == 0 ? slab + S_H + l * 8 : 0x40000000u;
  if (PREF && cw == 0) {
    const char* p = (const char*)(pc & ~127ull) + ((j & 7) * (CODE_LINES / 8) + (l < CODE_LINES / 8 ? l : 0)) * 128;
    if (PREF & 2) {
      const int e = l & 1;
      if ((l >> 1) == 16) p = kc + kvo + e * 130944;
      if ((l >> 1) == 17) p = vc + kvo + e * 130944;
      if ((l >> 1) == 18) p = wo + (size_t)xcd * 512 * 1152 + e * 511 * 1152;
      if ((l >> 1) == 19) p = ws + b * 32 * SLAB + e * (32 * SLAB - 128);
      if ((l >> 1) == 20) p = out + (size_t)(b * 64 + 8 * xcd + e * 7) * 1152;
      if ((l >> 1) == 21) p = x + b * 73728 + e * 73600;
      if (l >= 48 && l < 53) p = wq + h * 256 + (size_t)(l - 48) * 143 * 8192;
    }
    asm volatile("s_mov_b32 m0, %1\n\ts_nop 0\n\tglobal_load_lds_dword %0, off" :: "v"(p), "i"(73728) : "memory");
  }
  const unsigned tag = *(const CAS unsigned*)(ws + W_EPO + b * 256) + 0x9E3779B9u;
  const int qq = (l & 15) >> 2, pp = l & 3, blk = (l >> 4) & 1;
  const int trc = 16 * ((4 * cw + 2 * blk + (pp >> 1)) ^ (qq << 2)) + 8 * (pp & 1);
  asm volatile("" :: "s"(tag));
  if (PREW) { __builtin_amdgcn_raw_buffer_store_b64(u32x2{~tag, ~tag}, rw, hoff, 0, SC1); __builtin_amdgcn_raw_buffer_store_b64(u32x2{~tag, ~tag}, rw, hoff + 128, 0, SC1); }
  {
    f32x16 qa0 = {}, qa1 = {};
    LAS char* aw = opq(sm + 8192 + (8 * hf + qq) * 256 + trc);
    LAS char* ax[4];
    UNROLL for (int s = 0; s < 4; ++s) ax[s] = sm + ((r * 128 + 16 * (hf ^ ((r >> 1) & 7))) ^ (32 * s));
    bf16x8 fa[2][4], f0[2][4], f1[2][4];
    UNROLL for (int i = 0; i < 9; ++i) {
      CBAR();
      const int so = (i % 3) * 24576, cur = i & 1, prv = cur ^ 1;
      UNROLL for (int s = 0; s < 4; ++s) {
        if (i > 0) { if (s & 1) qa1 = MFMA(fa[prv][s >> 1], f1[prv][s >> 1], qa1); else qa0 = MFMA(fa[prv][s >> 1], f0[prv][s >> 1], qa0); }
        FENCE();
        fa[cur][s] = rdtr(aw + so + s * 4096, 1024); f0[cur][s] = rd128(ax[s] + so); f1[cur][s] = rd128(ax[s] + so + 4096);
        FENCE();
      }
      if (i > 0) { UNROLL for (int s = 2; s < 4; ++s) { qa0 = MFMA(fa[prv][s], f0[prv][s], qa0); qa1 = MFMA(fa[prv][s], f1[prv][s], qa1); } }
    }
    FENCE();
    UNROLL for (int s = 0; s < 4; ++s) qa0 = MFMA(fa[0][s], f0[0][s], qa0);
    FENCE();
    UNROLL for (int s = 0; s < 4; ++s) qa1 = MFMA(fa[0][s], f1[0][s], qa1);
    FENCE();
    LAS char* q = opq(sm + L_Q + cw * 4096 + r * 16 + 8 * hf);
    UNROLL for (int s = 0; s < 2; ++s) { const u32x4 p0 = pk8(qa0, s); *(LAS u32x2*)(q + s * 2048) = u32x2{p0.x, p0.y}; *(LAS u32x2*)(q + s * 2048 + 512) = u32x2{p0.z, p0.w}; }
    FENCE();
    UNROLL for (int s = 0; s < 2; ++s) { const u32x4 p1 = pk8(qa1, s); *(LAS u32x2*)(q + s * 2048 + 1024) = u32x2{p1.x, p1.y}; *(LAS u32x2*)(q + s * 2048 + 1536) = u32x2{p1.z, p1.w}; }
    asm volatile("s_waitcnt vmcnt(0)" ::: "memory");
  }
  ST(1);
  constexpr float CS = 0.08838834764831845f * 1.4426950408889634f;
  f32x16 S[8];
  float mu[8], su[8];
  LAS char* pr = opq(sm + L_P + l * 16);
  LAS char* pq = opq(sm + L_Q + qt * 1024 + l * 16);
#define SM_MAX(u) do { float a_ = max3(S[u][0], S[u][1], S[u][2]), b_ = max3(S[u][3], S[u][4], S[u][5]); \
    a_ = max3(a_, S[u][6], S[u][7]); b_ = max3(b_, S[u][8], S[u][9]); a_ = max3(a_, S[u][10], S[u][11]); b_ = max3(b_, S[u][12], S[u][13]); \
    a_ = max3(a_, S[u][14], S[u][15]); mu[u] = -CS * max2(a_, b_); PIN(mu[u]); } while (0)
#define SM_F(u) do { UNROLL for (int i = 0; i < 16; i += 2) { f32x2 t_ = {S[u][i], S[u][i + 1]}; t_ = __builtin_elementwise_fma(t_, f32x2{CS, CS}, f32x2{mu[u], mu[u]}); PIN(t_); S[u][i] = t_.x; S[u][i + 1] = t_.y; } } while (0)
#define SM_E(u, i) do { f32x2 t_ = {__builtin_amdgcn_exp2f(S[u][i]), __builtin_amdgcn_exp2f(S[u][i + 1])}; PIN(t_); S[u][i] = t_.x; S[u][i + 1] = t_.y; } while (0)
#define SM_A(u) do { f32x2 a_ = f32x2{S[u][0], S[u][1]} + f32x2{S[u][2], S[u][3]}, b_ = f32x2{S[u][4], S[u][5]} + f32x2{S[u][6], S[u][7]}; \
    a_ += f32x2{S[u][8], S[u][9]}; b_ += f32x2{S[u][10], S[u][11]}; a_ += f32x2{S[u][12], S[u][13]}; b_ += f32x2{S[u][14], S[u][15]}; a_ += b_; su[u] = a_.x + a_.y; PIN(su[u]); } while (0)
  {
    bf16x8 qf[8];
    LAS char* ak[8];
    UNROLL for (int s = 0; s < 8; ++s) ak[s] = opq(sm + 98304 + (((32 * kt + r) * 256 + 16 * (hf ^ (r & 15))) ^ (32 * s)));
    UNROLL for (int u = 0; u < 8; ++u) {
      CBAR();
      bf16x8 kf[8];
      f32x16 a = {};
      const int v = u > 0 ? u - 1 : 0, v2 = u > 1 ? u - 2 : 0;
      UNROLL for (int s = 0; s < 4; ++s) { if (u == 0) qf[s] = rd128(pq + s * 2048); kf[s] = rd128(ak[s] + (u & 3) * 16384); }
      FENCE();
      if (u > 1) SM_A(v2);
      FENCE();
      if (u > 0) { SM_MAX(v); SM_F(v); }
      UNROLL for (int s = 0; s < 8; ++s) {
        MF(a, kf[s], qf[s]);
        if (s < 4) { if (u == 0) qf[s + 4] = rd128(pq + (s + 4) * 2048); kf[s + 4] = rd128(ak[s + 4] + (u & 3) * 16384); }
        if (u > 0) SM_E(v, 2 * s);
        if (u == 3 && s >= 4) { ak[2 * s - 8] = opq(ak[2 * s - 8] - 98304); ak[2 * s - 7] = opq(ak[2 * s - 7] - 98304); }
      }
      S[u] = a;
    }
  }
  LAS f32x2* stm = (LAS f32x2*)opq(sm + L_Q + r * 8);
  {
    FENCE();
    float mw = min3(min3(mu[0], mu[1], mu[2]), min3(mu[3], mu[4], mu[5]), mu[6]);
    SM_A(6);
    FENCE();
    SM_MAX(7);
    mw = swap32min(min2(mw, mu[7]));
    FENCE();
    SM_F(7);
    UNROLL for (int i = 0; i < 16; i += 2) SM_E(7, i);
    UNROLL for (int u = 0; u < 8; ++u) mu[u] = __builtin_amdgcn_exp2f(mw - mu[u]);
    FENCE();
    SM_A(7);
    float lw_ = mu[0] * su[0], lx = mu[1] * su[1];
    UNROLL for (int u = 2; u < 8; u += 2) { lw_ = __builtin_fmaf(mu[u], su[u], lw_); lx = __builtin_fmaf(mu[u + 1], su[u + 1], lx); }
    lw_ = sum32(lw_ + lx, lw_ + lx);
    stm[cw * 32] = f32x2{mw, lw_};
    CBAR();
    ST(2);
    const f32x2 ot = stm[(cw ^ 2) * 32];
    const float mg = min2(mw, ot.x), ea = __builtin_amdgcn_exp2f(mg - mw), eb = __builtin_amdgcn_exp2f(mg - ot.x);
    const float g = ea * __builtin_amdgcn_rcpf(__builtin_fmaf(ea, lw_, eb * ot.y));
    UNROLL for (int u = 0; u < 8; ++u) mu[u] *= g;
  }
  LAS char* pw = opq(sm + L_P + kt * 4096 + qt * 1024 + l * 16);
  unsigned pd[8];
#define PV_C(u, k) do { float x_ = S[u][2 * (k)] * mu[u]; PIN(x_); float y_ = S[u][2 * (k) + 1] * mu[u]; PIN(y_); pd[k] = pk(x_, y_); PIN(pd[k]); } while (0)
#define PV_C2(u, k) do { float x_ = S[u][2 * (k)] * mu[u], y_ = S[u][2 * (k) + 1] * mu[u], z_ = S[u][2 * (k) + 2] * mu[u], w_ = S[u][2 * (k) + 3] * mu[u]; \
    asm volatile("" : "+v"(x_), "+v"(z_)); asm volatile("" : "+v"(y_), "+v"(w_)); pd[k] = pk(x_, y_); pd[k + 1] = pk(z_, w_); asm volatile("" : "+v"(pd[k]), "+v"(pd[k + 1])); } while (0)
#define PV_W(u, h) do { *(LAS u32x4*)(pw + ((u) & 1) * 8192 + (h) * 2048) = u32x4{pd[4 * (h)], pd[4 * (h) + 1], pd[4 * (h) + 2], pd[4 * (h) + 3]}; FENCE(); } while (0)
  S[0] *= mu[0];
  *(LAS u32x4*)pw = pk8(S[0], 0); *(LAS u32x4*)(pw + 2048) = pk8(S[0], 1);
  FENCE();
  S[1] *= mu[1];
  UNROLL for (int k = 0; k < 8; ++k) { pd[k] = pk(S[1][2 * k], S[1][2 * k + 1]); PIN(pd[k]); }
  f32x16 o0 = {}, o1 = {};
  LAS char* av1 = opq(sm + (4 * hf + qq) * 256 + trc);
  LAS char* av0 = opq(av1 + 98304);
  UNROLL for (int u = 0; u < 7; ++u) {
    CBAR();
    bf16x8 va[4], p0[4], p1[4];
    const int v = u < 6 ? u + 2 : 7;
#define PV_RD(ks) do { va[ks] = rdtr((u < 4 ? av0 : av1) + (u & 3) * 16384 + (ks) * 4096, 2048); \
      p0[ks] = rd128(pr + (u & 1) * 8192 + (ks) * 2048); p1[ks] = rd128(pr + (u & 1) * 8192 + (ks) * 2048 + 1024); FENCE(); } while (0)
    PV_RD(0); PV_RD(1);
    MF(o0, va[0], p0[0]); PV_RD(2); MF(o1, va[0], p1[0]); PV_RD(3);
    MF(o0, va[1], p0[1]); PV_W(u + 1, 0); MF(o1, va[1], p1[1]); PV_W(u + 1, 1);
    MF(o0, va[2], p0[2]); if (u < 6) PV_C2(v, 0);
    MF(o1, va[2], p1[2]); if (u < 6) PV_C2(v, 2);
    MF(o0, va[3], p0[3]); if (u < 6) PV_C2(v, 4);
    MF(o1, va[3], p1[3]); if (u < 6) PV_C2(v, 6);
  }
  {
    CBAR();
    bf16x8 va[4], p0[4], p1[4];
    UNROLL for (int ks = 0; ks < 6; ++ks) {
      if (ks >= 2) { o0 = MFMA(va[ks - 2], p0[ks - 2], o0); FENCE(); }
      if (ks < 4) { va[ks] = rdtr(av1 + 49152 + ks * 4096, 2048); p0[ks] = rd128(pr + 8192 + ks * 2048); }
      else { p1[2 * ks - 8] = rd128(pr + 8192 + (2 * ks - 8) * 2048 + 1024); p1[2 * ks - 7] = rd128(pr + 8192 + (2 * ks - 7) * 2048 + 1024); }
      FENCE();
    }
    const int off = cw * 4096 + l * 16;
    LAS char* d = opq(sm + L_Q + off);
    o1 = MFMA(va[0], p1[0], o1); o1 = MFMA(va[1], p1[1], o1);
    FENCE();
    const u32x4 a0 = pk8(o0, 0);
    st128(a0, rw, slab + S_O + off);
    FENCE();
    o1 = MFMA(va[2], p1[2], o1);
    FENCE();
    const u32x4 a1 = pk8(o0, 1);
    st128(a1, rw, slab + S_O + off + 2048);
    FENCE();
    o1 = MFMA(va[3], p1[3], o1);
    FENCE();
    unsigned ck = csum(csum(tag, a0), a1); PIN(ck);
    *(LAS u32x4*)d = a0;
    FENCE();
    const u32x4 b0 = pk8(o1, 0), b1 = pk8(o1, 1);
    st128(b0, rw, slab + S_O + off + 1024); st128(b1, rw, slab + S_O + off + 3072);
    st32(csum(csum(ck, b0), b1), rw, slab + S_OC + cw * 256 + l * 4);
    __builtin_amdgcn_raw_buffer_store_b64(u32x2{tag, tag}, rw, hoff, 0, SC1);
    FENCE();
    *(LAS u32x4*)(d + 2048) = a1; *(LAS u32x4*)(d + 1024) = b0; *(LAS u32x4*)(d + 3072) = b1;
  }
  ST(3);
  bf16x8 wf[32];
  LAS char* av2 = opq(av1 + L_P);
  LAS char* po = opq(pr + (slot_addr(16) - L_P));
  auto loadW1 = [&](int i, int ks) { wf[4 * i + ks] = rdtr((i < 3 ? av0 + (i + 1) * 16384 : i < 7 ? av1 + (i - 3) * 16384 : av2) + ks * 4096, 2048); };
  f32x16 c0 = {}, c1 = {};
  auto head = [&](int t, LAS char* p, int wnext) {
    bf16x8 g0[8], g1[8];
    UNROLL for (int ks = 0; ks < 10; ++ks) {
      if (ks >= 2) MF(c0, wf[8 * t + ks - 2], g0[ks - 2]);
      if (ks < 8) { g0[ks] = rd128(p + ks * 2048); g1[ks] = rd128(p + ks * 2048 + 1024); FENCE(); }
      if (ks >= 2) { MF(c1, wf[8 * t + ks - 2], g1[ks - 2]);
        if (wnext >= 0) { loadW1(wnext + ((ks - 2) >> 2), (ks - 2) & 3); FENCE(); } }
    }
  };
  CBAR();
  u32x4 t0, t1;
  {
    f32x16 t = {};
    LAS char* at = opq(sm + slot_addr(16) + (4 * hf + qq) * 128 + 16 * ((4 * kt + 2 * blk + (pp >> 1)) ^ ((qq >> 1) << 2)) + 8 * (pp & 1));
    bf16x8 ta[8], tb[8];
    UNROLL for (int ks = 0; ks < 11; ++ks) {
      if (ks >= 3) MF(t, ta[ks - 3], tb[ks - 3]);
      if (ks < 8) { ta[ks] = rdtr(at + ks * 2048, 1024); tb[ks] = rd128(pq + ks * 2048); }
      if (ks >= 7) { loadW1((ks - 7) >> 1, ((ks - 7) & 1) * 2); loadW1((ks - 7) >> 1, ((ks - 7) & 1) * 2 + 1); }
      FENCE();
    }
    head(0, pr + (L_Q - L_P), -1);
    t0 = pk8(t, 0); t1 = pk8(t, 1);
  }
  CBAR();
  UNROLL for (int i = 0; i < 8; ++i) loadW1(2 + (i >> 2), i & 3);
  unsigned tc0 = csum(tag, t0), tc1 = csum(tag, t1);
  const unsigned cl = ((r >> 3) * 16 + hf * 8 + (r & 7)) * 4;
  unsigned aT = slab + S_T + cw * 2048 + l * 16, aTC = slab + S_TC + cw * 512 + cl, aM = slab + S_M + cw * 4096 + l * 16, aMC = slab + S_MC + cw * 512 + cl;
  const unsigned tile = cw * 2 + (xcd >> 2), pb = (unsigned)(j * 8 + 2 * (l >> 4)) * SLAB;
  unsigned aR = pb + S_M + tile * 2048 + (32 * ((l >> 3) & 1) + 8 * (xcd & 3) + (l & 7)) * 16, aRC = pb + S_MC + tile * 256 + (xcd & 3) * 64 + (l & 15) * 4;
  unsigned aO = (unsigned)(b * 64 + 8 * xcd + (l & 7)) * 1152 + (128 * hh + 32 * cw + 16 * blk + 8 * hf + 4 * ((l >> 3) & 1)) * 2;
  asm volatile("" : "+v"(tc0), "+v"(tc1), "+v"(aT), "+v"(aTC), "+v"(aM), "+v"(aMC), "+v"(aR), "+v"(aRC), "+v"(aO));
  CBAR();
  UNROLL for (int i = 0; i < 8; ++i) loadW1(4 + (i >> 2), i & 3);
  CBAR();
  st128(t0, rw, aT); st128(t1, rw, aT + 1024); st32(tc0, rw, aTC); st32(tc1, rw, aTC + 256);
  FENCE();
  head(1, po, -1);
  CBAR(); head(2, po + 16384, 6);
  CBAR(); head(3, po + 32768, -1);
  ST(4);
  {
    const u32x4 a0 = pk8(c0, 0), a1 = pk8(c0, 1);
    st128(a0, rw, aM); st128(a1, rw, aM + 1024);
    FENCE();
    const u32x4 b0 = pk8(c1, 0), b1 = pk8(c1, 1);
    st128(b0, rw, aM + 2048); st128(b1, rw, aM + 3072);
    FENCE();
    st32(csum(csum(tag, a0), a1), rw, aMC); st32(csum(csum(tag, b0), b1), rw, aMC + 256);
  }
  ST(5);
  {
#pragma unroll 1
    for (unsigned spin = 0; spin < SPIN_CAP; ++spin) if (__builtin_amdgcn_readfirstlane(*(volatile LAS unsigned*)(sm + L_Q))) break;
    ST(7);
    u32x4 v[2][2];
#pragma unroll 1
    for (unsigned spin = 0; spin < SPIN_CAP; ++spin) {
      asm volatile("" ::: "memory");
      unsigned c[2]; DG(1);
      UNROLL for (int p = 0; p < 2; ++p) {
        v[p][0] = __builtin_amdgcn_raw_buffer_load_b128(rw, aR, p * SLAB, SC1);
        v[p][1] = __builtin_amdgcn_raw_buffer_load_b128(rw, aR + 1024, p * SLAB, SC1);
        c[p] = __builtin_amdgcn_raw_buffer_load_b32(rw, aRC, p * SLAB, SC1);
      }
      if (__all(((csum(csum(tag, v[0][0]), v[0][1]) ^ c[0]) | (csum(csum(tag, v[1][0]), v[1][1]) ^ c[1])) == 0)) break;
      __builtin_amdgcn_s_sleep(BACKOFF);
    }
    float e[8];
    UNROLL for (int i = 0; i < 4; ++i) {
      e[2 * i] = sum16(blo(v[0][0][i]) + blo(v[1][0][i]), blo(v[0][1][i]) + blo(v[1][1][i]));
      e[2 * i + 1] = sum16(bhi(v[0][0][i]) + bhi(v[1][0][i]), bhi(v[0][1][i]) + bhi(v[1][1][i]));
    }
    *(u32x2*)(out + aO) = u32x2{pk(sum32(e[0], e[4]), sum32(e[1], e[5])), pk(sum32(e[2], e[6]), sum32(e[3], e[7]))};
  }
  ST(6);
  if (threadIdx.x == 256) for (int i = 0; i < 8; ++i) ((u64*)(ws + STAMP_OFF))[w * STAMP_W + 8 + i] = st_[i] ? st_[i] : st_[0];
}

__global__ void k_probe(char* ws) {
  const u64* st = (const u64*)(ws + STAMP_OFF); int t = threadIdx.x;
  __shared__ u64 a[256], b[256];
  a[t] = st[t * STAMP_W]; b[t] = st[t * STAMP_W + 1];
  __syncthreads();
  if (t == 0) {
    u64 mn = ~0ull, mx = 0, e = 0, sum = 0;
    for (int i = 0; i < 256; ++i) { mn = a[i] < mn ? a[i] : mn; mx = a[i] > mx ? a[i] : mx; e = b[i] > e ? b[i] : e; sum += b[i] - a[i]; }
    u64 d = sum / 256;
    if (d > 10000) d = 10000;
    u64 t0 = __builtin_amdgcn_s_memrealtime();
    while (__builtin_amdgcn_s_memrealtime() - t0 < d * 100) __builtin_amdgcn_s_sleep(4);
  }
}

__global__ void k_probe(char* ws) {
  const unsigned* st = (const unsigned*)(ws + (40u << 20)); int t = threadIdx.x;
  __shared__ int a[256], b[256];
  unsigned z = st[192];
  a[t] = (int)(st[t * 256 + 192] - z); b[t] = (int)(st[t * 256 + 192] - z);
  __syncthreads();
  if (t == 0) {
    int mn = 1 << 30, mx = -(1 << 30), e = -(1 << 30), sum = 0;
    for (int i = 0; i < 256; ++i) { mn = a[i] < mn ? a[i] : mn; mx = a[i] > mx ? a[i] : mx; e = b[i] > e ? b[i] : e; sum += b[i] - a[i]; }
    int d = e - mn;
    if (d > 10000 || d < 0) d = 10000;
    unsigned long long t0 = __builtin_amdgcn_s_memrealtime();
    while (__builtin_amdgcn_s_memrealtime() - t0 < (unsigned long long)d * 100) __builtin_amdgcn_s_sleep(4);
  }
}
extern "C" void kernel_launch(void* const* d_in, const int* in_sizes, int n_in, void* d_out, int out_size, void* d_ws, size_t ws_size, hipStream_t stream) {
  gqa_fused<<<256, 512, 0, stream>>>((const char*)d_in[0], (const char*)d_in[1], (const char*)d_in[2], (const char*)d_in[3], (const char*)d_in[4], (char*)d_out, (char*)d_ws);
  k_probe<<<1, 256, 0, stream>>>((char*)d_ws);
  k_probe<<<1, 256, 0, stream>>>((char*)d_ws);
}
```

```cpp
#include <hip/hip_runtime.h>

__global__ __launch_bounds__(512, 2) void gqa_fused(const void* x, const void* kc, const void* vc, const void* wq, const void* wo, void* out, void* ws) {
  __shared__ __attribute__((aligned(256))) char smem[163840];
  asm volatile(
    "  s_load_dwordx8 s[4:11], %0, 0x0\n"
    "  s_getpc_b64 s[26:27]\n"
    "  s_lshr_b32 s19, %1, 3\n"
    "  v_lshrrev_b32 v2, 6, v0\n"
    "  v_lshl_add_u32 v2, v2, 5, s19\n"
    "  v_min_u32 v2, 140, v2\n"
    "  v_lshlrev_b32 v2, 7, v2\n"
    "  global_load_dword v19, v2, s[26:27]\n"
    "  s_load_dwordx4 s[12:15], %0, 0x20\n"
    "  v_readfirstlane_b32 s56, v0\n"
    "  s_load_dwordx2 s[16:17], %0, 0x30\n"
    "  s_and_b32 s18, %1, 7\n"
    "  s_lshr_b32 s20, %1, 5\n"
    "  s_and_b32 s21, s19, 3\n"
    "  s_bfe_u32 s23, s56, 0x20006\n"
    "  v_and_b32 v0, 63, v0\n"
    "  s_lshl2_add_u32 s22, s18, s21\n"
    "  v_lshrrev_b32 v1, 4, v0\n"
    "  v_and_b32 v4, 15, v0\n"
    "  s_lshl_b32 s63, s23, 12\n"
    "  s_bitcmp1_b32 s56, 8\n"
    "  s_cbranch_scc1 .Lgqa_g1\n"
    "  v_lshrrev_b32 v2, 3, v0\n"
    "  v_and_b32 v3, 7, v0\n"
    "  s_lshl_b32 s56, s23, 4\n"
    "  s_movk_i32 s57, 1152\n"
    "  v_xor_b32 v5, v3, v1\n"
    "  v_add_u32 v6, s56, v2\n"
    "  v_lshlrev_b32 v5, 4, v5\n"
    "  v_lshlrev_b32 v7, 6, v1\n"
    "  v_mad_u32_u24 v224, v6, s57, v5\n"
    "  v_lshlrev_b32 v8, 4, v4\n"
    "  s_movk_i32 s58, 8192\n"
    "  v_xor_b32 v8, v8, v7\n"
    "  v_add_u32 v9, s56, v1\n"
    "  v_xad_u32 v225, v224, 64, s58\n"
    "  v_lshl_add_u32 v226, v9, 13, v8\n"
    "  s_mul_i32 s59, s20, 73728\n"
    "  s_lshl_b32 s60, s22, 8\n"
    "  s_add_u32 s61, s21, s20\n"
    "  s_and_b32 s61, s61, 3\n"
    "  s_lshl_b32 s61, s61, 1\n"
    "  s_lshl_b32 s62, s23, 11\n"
    "  v_add_u32 v227, 31744, v226\n"
    "  v_add_u32 v228, 63488, v226\n"
    "  v_add_u32 v229, 95232, v226\n"
    "  s_mov_b32 s34, 73728\n"
    "  s_mov_b32 s35, 0x20000\n"
    "  s_sub_u32 s38, 4718592, s60\n"
    "  s_mov_b32 s39, 0x20000\n"
    "  s_add_u32 s64, s63, 8192\n"
    "  s_lshl_b32 s67, s61, 7\n"
    "  s_lshl_b32 s68, s61, 19\n"
    "  s_mov_b32 m0, s62\n"
    "  s_waitcnt lgkmcnt(0)\n"
    "  s_add_u32 s32, s4, s59\n"
    "  s_addc_u32 s33, s5, 0\n"
    "  s_and_b32 s33, s33, 0xffff\n"
    "  s_add_u32 s36, s10, s60\n"
    "  buffer_load_dwordx4 v224, s[32:35], s67 offen lds\n"
    "  s_addc_u32 s37, s11, 0\n"
    "  buffer_load_dwordx4 v225, s[32:35], s67 offen offset:1024 lds\n"
    "  s_mov_b32 m0, s64\n"
    "  s_and_b32 s37, s37, 0xffff\n"
    "  buffer_load_dwordx4 v226, s[36:39], s68 offen lds\n"
    "  buffer_load_dwordx4 v227, s[36:39], s68 offen offset:1024 lds\n"
    "  buffer_load_dwordx4 v228, s[36:39], s68 offen offset:2048 lds\n"
    "  buffer_load_dwordx4 v229, s[36:39], s68 offen offset:3072 lds\n"
    "  s_add_u32 m0, s62, 24576\n"
    "  s_addk_i32 s67, 128\n"
    "  buffer_load_dwordx4 v224, s[32:35], s67 offen lds\n"
    "  buffer_load_dwordx4 v225, s[32:35], s67 offen offset:1024 lds\n"
    "  s_lshl_b32 s67, s20, 7\n"
    "  s_add_u32 s67, s67, 65536\n"
    "  s_load_dword s24, s[16:17], s67\n"
    "  s_lshr_b32 s67, s22, 1\n"
    "  s_lshl4_add_u32 s67, s20, s67\n"
    "  s_lshl_b32 s67, s67, 17\n"
    "  s_add_u32 s40, s6, s67\n"
    "  s_addc_u32 s41, s7, 0\n"
    "  s_and_b32 s41, s41, 0xffff\n"
    "  s_mov_b32 s42, 131072\n"
    "  s_mov_b32 s43, 0x20000\n"
    "  s_add_u32 s44, s8, s67\n"
    "  s_addc_u32 s45, s9, 0\n"
    "  s_and_b32 s45, s45, 0xffff\n"
    "  s_mov_b32 s46, 131072\n"
    "  s_mov_b32 s47, 0x20000\n"
    "  v_xor_b32 v5, v4, v1\n"
    "  s_lshl_b32 s67, s23, 10\n"
    "  v_lshl_add_u32 v6, v1, 8, s67\n"
    "  v_lshl_add_u32 v230, v5, 4, v6\n"
    "  v_add_u32 v234, v6, v8\n"
    "  v_xor_b32 v231, 64, v230\n"
    "  v_xor_b32 v232, 0x80, v230\n"
    "  v_xor_b32 v233, 0xc0, v230\n"
    "  v_add_u32 v231, 3072, v231\n"
    "  v_add_u32 v235, 3072, v234\n"
    "  v_add_u32 v232, 6144, v232\n"
    "  v_add_u32 v236, 6144, v234\n"
    "  v_add_u32 v233, 9216, v233\n"
    "  v_add_u32 v237, 9216, v234\n"
    "  s_mul_i32 s67, s19, 144\n"
    "  s_mul_i32 s68, s23, 36\n"
    "  s_add_u32 s67, s67, s68\n"
    "  s_add_u32 s67, s67, 20\n"
    "  v_min_u32 v5, 15, v0\n"
    "  v_add_lshl_u32 v238, v5, s67, 7\n"
    "  s_mul_i32 s67, s18, 589824\n"
    "  s_add_u32 s48, s12, s67\n"
    "  s_addc_u32 s49, s13, 0\n"
    "  s_and_b32 s49, s49, 0xffff\n"
    "  s_mov_b32 s50, 589824\n"
    "  s_mov_b32 s51, 0x20000\n"
    "  s_mov_b32 s28, s16\n"
    "  s_and_b32 s29, s17, 0xffff\n"
    "  s_mov_b32 s30, 11993088\n"
    "  s_mov_b32 s31, 0x20000\n"
    "  s_mov_b32 s52, s14\n"
    "  s_and_b32 s53, s15, 0xffff\n"
    "  s_mov_b32 s54, 589824\n"
    "  s_mov_b32 s55, 0x20000\n"
    "  s_lshr_b32 s56, s23, 1\n"
    "  s_and_b32 s57, s23, 1\n"
    "  v_and_b32 v10, 31, v0\n"
    "  v_lshrrev_b32 v11, 5, v0\n"
    "  v_lshrrev_b32 v12, 2, v4\n"
    "  v_and_b32 v13, 1, v1\n"
    "  v_bfe_u32 v14, v0, 1, 1\n"
    "  v_and_b32 v15, 1, v0\n"
    "  s_lshl_b32 s67, s23, 2\n"
    "  v_lshl_add_u32 v16, v13, 1, v14\n"
    "  v_add_u32 v17, s67, v16\n"
    "  v_lshlrev_b32 v18, 2, v12\n"
    "  v_xor_b32 v17, v17, v18\n"
    "  v_lshlrev_b32 v17, 4, v17\n"
    "  v_lshl_add_u32 v17, v15, 3, v17\n"
    "  v_lshl_add_u32 v18, v11, 2, v12\n"
    "  v_lshl_add_u32 v210, v18, 8, v17\n"
    "  v_add_u32 v209, 98304, v210\n"
    "  v_add_u32 v211, 65536, v210\n"
    "  s_lshl_b32 s67, s56, 2\n"
    "  v_add_u32 v16, s67, v16\n"
    "  v_lshrrev_b32 v17, 1, v12\n"
    "  v_lshlrev_b32 v17, 2, v17\n"
    "  v_xor_b32 v16, v16, v17\n"
    "  v_lshlrev_b32 v16, 4, v16\n"
    "  v_lshl_add_u32 v16, v15, 3, v16\n"
    "  v_lshl_add_u32 v16, v18, 7, v16\n"
    "  v_add_u32 v213, 98304, v16\n"
    "  v_lshlrev_b32 v208, 4, v0\n"
    "  v_add_u32 v206, 65536, v208\n"
    "  v_add_u32 v212, 98304, v208\n"
    "  s_lshl_b32 s67, s57, 10\n"
    "  s_add_u32 s67, s67, 81920\n"
    "  v_add_u32 v207, s67, v208\n"
    "  v_add_u32 v215, s63, v208\n"
    "  v_add_u32 v216, 81920, v215\n"
    "  v_lshlrev_b32 v16, 3, v10\n"
    "  v_add_u32 v214, 65536, v16\n"
    "  v_mov_b32 v223, 81920\n"
    "  s_lshl_b32 s67, s23, 8\n"
    "  v_lshl_add_u32 v217, v0, 2, s67\n"
    "  v_bfe_u32 v16, v0, 3, 2\n"
    "  v_lshl_add_u32 v16, v16, 1, v11\n"
    "  v_lshl_add_u32 v16, v16, 3, v3\n"
    "  s_lshl_b32 s67, s23, 9\n"
    "  v_lshl_add_u32 v218, v16, 2, s67\n"
    "  v_bfe_u32 v16, v0, 3, 1\n"
    "  v_lshlrev_b32 v17, 9, v16\n"
    "  v_lshl_add_u32 v17, v3, 4, v17\n"
    "  s_mov_b32 s67, 92160\n"
    "  v_mad_u32_u24 v219, v1, s67, v17\n"
    "  v_lshlrev_b32 v17, 2, v4\n"
    "  v_mad_u32_u24 v220, v1, s67, v17\n"
    "  v_lshlrev_b32 v17, 5, v13\n"
    "  v_lshl_add_u32 v17, v11, 4, v17\n"
    "  v_lshl_add_u32 v17, v16, 3, v17\n"
    "  s_movk_i32 s67, 1152\n"
    "  v_mad_u32_u24 v221, v3, s67, v17\n"
    "  s_lshl_b32 s67, %1, 8\n"
    "  v_lshl_add_u32 v222, v0, 2, s67\n"
    "  s_cmp_eq_u32 s23, 0\n"
    "  s_cselect_b32 s67, 32, 0\n"
    "  v_cmp_gt_u32 vcc, s67, v0\n"
    "  s_nop 1\n"
    "  v_cndmask_b32 v222, 2.0, v222, vcc\n"
    "  s_mul_i32 s25, %1, 46080\n"
    "  s_add_u32 s25, s25, 196608\n"
    "  s_add_u32 s70, s25, 16384\n"
    "  s_add_u32 s73, s25, 27648\n"
    "  s_add_u32 s74, s25, 44032\n"
    "  v_mov_b32 v21, 0x3f80\n"
    "  v_mov_b32 v22, 0x3f800000\n"
    "  s_lshr_b32 s79, s18, 2\n"
    "  s_lshl1_add_u32 s79, s23, s79\n"
    "  s_and_b32 s80, s18, 3\n"
    "  s_mul_i32 s81, s19, 368640\n"
    "  s_lshl_b32 s75, s79, 11\n"
    "  s_add_u32 s75, s75, s81\n"
    "  s_lshl_b32 s82, s80, 7\n"
    "  s_add_u32 s75, s75, s82\n"
    "  s_add_u32 s75, s75, 224256\n"
    "  s_lshl_b32 s76, s79, 8\n"
    "  s_add_u32 s76, s76, s81\n"
    "  s_lshl_b32 s82, s80, 6\n"
    "  s_add_u32 s76, s76, s82\n"
    "  s_add_u32 s76, s76, 240640\n"
    "  s_lshl3_add_u32 s77, s20, s18\n"
    "  s_mul_i32 s77, s77, 9216\n"
    "  s_lshl2_add_u32 s79, s21, s23\n"
    "  s_lshl_b32 s79, s79, 6\n"
    "  s_add_u32 s77, s77, s79\n"
    "  s_add_u32 s61, s61, 1\n"
    "  s_movk_i32 s65, 24576\n"
    "  s_mov_b32 s66, 1\n"
    "  s_lshl_b32 s68, s61, 19\n"
    "  s_lshl2_add_u32 s85, s19, s23\n"
    "  s_mul_i32 s85, s85, 4608\n"
    "  s_add_u32 s86, s48, s85\n"
    "  s_addc_u32 s87, s49, 0\n"
    "  s_branch .Lgqa_qw\n"
    ".Lgqa_qloop:\n"
    "  s_waitcnt vmcnt(6)\n"
    "  s_barrier\n"
    ".Lgqa_qx:\n"
    "  s_add_u32 m0, s65, s62\n"
    "  s_lshl_b32 s67, s61, 7\n"
    "  buffer_load_dwordx4 v224, s[32:35], s67 offen lds\n"
    "  s_lshl_b32 s68, s61, 19\n"
    "  buffer_load_dwordx4 v225, s[32:35], s67 offen offset:1024 lds\n"
    ".Lgqa_qw:\n"
    "  s_add_u32 m0, s65, s64\n"
    "  s_add_u32 s61, s61, 1\n"
    "  buffer_load_dwordx4 v226, s[36:39], s68 offen lds\n"
    "  buffer_load_dwordx4 v227, s[36:39], s68 offen offset:1024 lds\n"
    "  buffer_load_dwordx4 v228, s[36:39], s68 offen offset:2048 lds\n"
    "  buffer_load_dwordx4 v229, s[36:39], s68 offen offset:3072 lds\n"
    "  s_load_dword s88, s[86:87], 0\n"
    "  s_load_dword s88, s[86:87], 64\n"
    "  s_load_dword s88, s[86:87], 128\n"
    "  s_load_dword s88, s[86:87], 192\n"
    "  s_load_dword s88, s[86:87], 256\n"
    "  s_add_u32 s86, s86, 320\n"
    "  s_addc_u32 s87, s87, 0\n"
    "  s_cmp_lg_u32 s61, 9\n"
    "  s_cselect_b32 s61, s61, 0\n"
    "  s_addk_i32 s65, 24576\n"
    "  s_cmp_lg_u32 s65, 73728\n"
    "  s_cselect_b32 s65, s65, 0\n"
    "  s_add_u32 s66, s66, 1\n"
    "  s_cmp_lg_u32 s66, 9\n"
    "  s_cbranch_scc1 .Lgqa_qloop\n"
    "  s_waitcnt vmcnt(6)\n"
    "  s_barrier\n"
    "  s_add_u32 m0, s63, 98304\n"
    "  s_mov_b32 s69, 0\n"
    "  buffer_load_dwordx4 v230, s[40:43], s69 offen offset:0 lds\n"
    "  buffer_load_dwordx4 v231, s[40:43], s69 offen offset:1024 lds\n"
    "  buffer_load_dwordx4 v232, s[40:43], s69 offen offset:2048 lds\n"
    "  buffer_load_dwordx4 v233, s[40:43], s69 offen offset:3072 lds\n"
    "  s_add_u32 m0, s63, 114688\n"
    "  s_mov_b32 s69, 16384\n"
    "  buffer_load_dwordx4 v230, s[40:43], s69 offen offset:0 lds\n"
    "  buffer_load_dwordx4 v231, s[40:43], s69 offen offset:1024 lds\n"
    "  buffer_load_dwordx4 v232, s[40:43], s69 offen offset:2048 lds\n"
    "  buffer_load_dwordx4 v233, s[40:43], s69 offen offset:3072 lds\n"
    "  s_waitcnt vmcnt(8)\n"
    "  s_barrier\n"
    "  s_add_u32 m0, s63, 131072\n"
    "  s_mov_b32 s69, 32768\n"
    "  buffer_load_dwordx4 v230, s[40:43], s69 offen offset:0 lds\n"
    "  buffer_load_dwordx4 v231, s[40:43], s69 offen offset:1024 lds\n"
    "  buffer_load_dwordx4 v232, s[40:43], s69 offen offset:2048 lds\n"
    "  buffer_load_dwordx4 v233, s[40:43], s69 offen offset:3072 lds\n"
    "  s_add_u32 m0, s63, 147456\n"
    "  s_mov_b32 s69, 49152\n"
    "  buffer_load_dwordx4 v230, s[40:43], s69 offen offset:0 lds\n"
    "  buffer_load_dwordx4 v231, s[40:43], s69 offen offset:1024 lds\n"
    "  buffer_load_dwordx4 v232, s[40:43], s69 offen offset:2048 lds\n"
    "  buffer_load_dwordx4 v233, s[40:43], s69 offen offset:3072 lds\n"
    "  s_waitcnt vmcnt(12)\n"
    "  s_barrier\n"
    "  s_add_u32 m0, s63, 0\n"
    "  s_mov_b32 s69, 65536\n"
    "  buffer_load_dwordx4 v230, s[40:43], s69 offen offset:0 nt lds\n"
    "  buffer_load_dwordx4 v231, s[40:43], s69 offen offset:1024 nt lds\n"
    "  buffer_load_dwordx4 v232, s[40:43], s69 offen offset:2048 nt lds\n"
    "  buffer_load_dwordx4 v233, s[40:43], s69 offen offset:3072 nt lds\n"
    "  s_waitcnt vmcnt(12)\n"
    "  s_barrier\n"
    "  s_add_u32 m0, s63, 16384\n"
    "  s_mov_b32 s69, 81920\n"
    "  buffer_load_dwordx4 v230, s[40:43], s69 offen offset:0 nt lds\n"
    "  buffer_load_dwordx4 v231, s[40:43], s69 offen offset:1024 nt lds\n"
    "  buffer_load_dwordx4 v232, s[40:43], s69 offen offset:2048 nt lds\n"
    "  buffer_load_dwordx4 v233, s[40:43], s69 offen offset:3072 nt lds\n"
    "  s_waitcnt vmcnt(12)\n"
    "  s_barrier\n"
    "  s_add_u32 m0, s63, 32768\n"
    "  s_mov_b32 s69, 98304\n"
    "  buffer_load_dwordx4 v230, s[40:43], s69 offen offset:0 nt lds\n"
    "  buffer_load_dwordx4 v231, s[40:43], s69 offen offset:1024 nt lds\n"
    "  buffer_load_dwordx4 v232, s[40:43], s69 offen offset:2048 nt lds\n"
    "  buffer_load_dwordx4 v233, s[40:43], s69 offen offset:3072 nt lds\n"
    "  s_waitcnt vmcnt(12)\n"
    "  s_barrier\n"
    "  s_add_u32 m0, s63, 49152\n"
    "  s_mov_b32 s69, 114688\n"
    "  buffer_load_dwordx4 v230, s[40:43], s69 offen offset:0 nt lds\n"
    "  buffer_load_dwordx4 v231, s[40:43], s69 offen offset:1024 nt lds\n"
    "  buffer_load_dwordx4 v232, s[40:43], s69 offen offset:2048 nt lds\n"
    "  buffer_load_dwordx4 v233, s[40:43], s69 offen offset:3072 nt lds\n"
    "  s_waitcnt vmcnt(12)\n"
    "  s_barrier\n"
    "  s_add_u32 m0, s63, 98304\n"
    "  s_mov_b32 s69, 0\n"
    "  buffer_load_dwordx4 v234, s[44:47], s69 offen offset:0 nt lds\n"
    "  buffer_load_dwordx4 v235, s[44:47], s69 offen offset:1024 nt lds\n"
    "  buffer_load_dwordx4 v236, s[44:47], s69 offen offset:2048 nt lds\n"
    "  buffer_load_dwordx4 v237, s[44:47], s69 offen offset:3072 nt lds\n"
    "  s_waitcnt vmcnt(12)\n"
    "  s_barrier\n"
    "  s_cmp_eq_u32 s23, 0\n"
    "  s_cselect_b32 s69, 0, 2.0\n"
    "  s_add_u32 m0, s63, 114688\n"
    "  s_nop 0\n"
    "  buffer_load_dword v238, s[48:51], s69 offen lds\n"
    "  s_mov_b32 s69, 16384\n"
    "  buffer_load_dwordx4 v234, s[44:47], s69 offen offset:0 nt lds\n"
    "  buffer_load_dwordx4 v235, s[44:47], s69 offen offset:1024 nt lds\n"
    "  buffer_load_dwordx4 v236, s[44:47], s69 offen offset:2048 nt lds\n"
    "  buffer_load_dwordx4 v237, s[44:47], s69 offen offset:3072 nt lds\n"
    "  s_waitcnt vmcnt(13)\n"
    "  s_barrier\n"
    "  s_add_u32 m0, s63, 131072\n"
    "  s_mov_b32 s69, 32768\n"
    "  buffer_load_dwordx4 v234, s[44:47], s69 offen offset:0 nt lds\n"
    "  buffer_load_dwordx4 v235, s[44:47], s69 offen offset:1024 nt lds\n"
    "  buffer_load_dwordx4 v236, s[44:47], s69 offen offset:2048 nt lds\n"
    "  buffer_load_dwordx4 v237, s[44:47], s69 offen offset:3072 nt lds\n"
    "  s_waitcnt vmcnt(13)\n"
    "  s_barrier\n"
    "  s_cmp_eq_u32 s23, 1\n"
    "  s_cselect_b32 s69, 0, 2.0\n"
    "  s_add_u32 m0, s63, 147456\n"
    "  s_nop 0\n"
    "  buffer_load_dword v238, s[48:51], s69 offen lds\n"
    "  s_mov_b32 s69, 49152\n"
    "  buffer_load_dwordx4 v234, s[44:47], s69 offen offset:0 nt lds\n"
    "  buffer_load_dwordx4 v235, s[44:47], s69 offen offset:1024 nt lds\n"
    "  buffer_load_dwordx4 v236, s[44:47], s69 offen offset:2048 nt lds\n"
    "  buffer_load_dwordx4 v237, s[44:47], s69 offen offset:3072 nt lds\n"
    "  s_waitcnt lgkmcnt(0)\n"
    "  s_add_u32 s24, s24, 2654435769\n"
    "  v_mov_b32 v20, s24\n"
    "  s_waitcnt vmcnt(14) lgkmcnt(0)\n"
    "  s_barrier\n"
    "  ds_read_b64_tr_b16 v[48:49], v209 offset:0\n"
    "  ds_read_b64_tr_b16 v[50:51], v209 offset:2048\n"
    "  ds_read_b128 v[52:55], v206 offset:0\n"
    "  ds_read_b128 v[56:59], v206 offset:1024\n"
    "  ds_read_b64_tr_b16 v[60:61], v209 offset:4096\n"
    "  ds_read_b64_tr_b16 v[62:63], v209 offset:6144\n"
    "  ds_read_b128 v[64:67], v206 offset:2048\n"
    "  ds_read_b128 v[68:71], v206 offset:3072\n"
    "  s_waitcnt lgkmcnt(5)\n"
    "  v_mfma_f32_32x32x16_bf16 v[96:111], v[48:51], v[52:55], 0\n"
    "  ds_read_b64_tr_b16 v[72:73], v209 offset:8192\n"
    "  ds_read_b64_tr_b16 v[74:75], v209 offset:10240\n"
    "  ds_read_b128 v[76:79], v206 offset:4096\n"
    "  ds_read_b128 v[80:83], v206 offset:5120\n"
    "  s_waitcnt lgkmcnt(8)\n"
    "  v_mfma_f32_32x32x16_bf16 v[112:127], v[48:51], v[56:59], 0\n"
    "  ds_read_b64_tr_b16 v[84:85], v209 offset:12288\n"
    "  ds_read_b64_tr_b16 v[86:87], v209 offset:14336\n"
    "  ds_read_b128 v[88:91], v206 offset:6144\n"
    "  ds_read_b128 v[92:95], v206 offset:7168\n"
    "  s_waitcnt lgkmcnt(9)\n"
    "  v_mfma_f32_32x32x16_bf16 v[96:111], v[60:63], v[64:67], v[96:111]\n"
    "  s_add_u32 m0, s63, 0\n"
    "  s_mov_b32 s69, 65536\n"
    "  buffer_load_dwordx4 v234, s[44:47], s69 offen offset:0 nt lds\n"
    "  s_waitcnt lgkmcnt(8)\n"
    "  v_mfma_f32_32x32x16_bf16 v[112:127], v[60:63], v[68:71], v[112:127]\n"
    "  s_waitcnt lgkmcnt(5)\n"
    "  v_mfma_f32_32x32x16_bf16 v[96:111], v[72:75], v[76:79], v[96:111]\n"
    "  buffer_load_dwordx4 v235, s[44:47], s69 offen offset:1024 nt lds\n"
    "  s_waitcnt lgkmcnt(4)\n"
    "  v_mfma_f32_32x32x16_bf16 v[112:127], v[72:75], v[80:83], v[112:127]\n"
    "  s_waitcnt lgkmcnt(1)\n"
    "  v_mfma_f32_32x32x16_bf16 v[96:111], v[84:87], v[88:91], v[96:111]\n"
    "  buffer_load_dwordx4 v236, s[44:47], s69 offen offset:2048 nt lds\n"
    "  s_waitcnt lgkmcnt(0)\n"
    "  v_mfma_f32_32x32x16_bf16 v[112:127], v[84:87], v[92:95], v[112:127]\n"
    "  buffer_load_dwordx4 v237, s[44:47], s69 offen offset:3072 nt lds\n"
    "  s_waitcnt vmcnt(13) lgkmcnt(0)\n"
    "  s_barrier\n"
    "  ds_read_b32 v10, v214 offset:17408\n"
    "  ds_read_b32 v11, v214 offset:17664\n"
    "  ds_read_b32 v12, v214 offset:17920\n"
    "  ds_read_b32 v13, v214 offset:18176\n"
    ".Lgqa_fixed0:\n"
    "  ds_read_b64_tr_b16 v[48:49], v209 offset:16384\n"
    "  ds_read_b64_tr_b16 v[50:51], v209 offset:18432\n"
    "  ds_read_b128 v[52:55], v206 offset:8192\n"
    "  ds_read_b128 v[56:59], v206 offset:9216\n"
    "  s_waitcnt lgkmcnt(5)\n"
    "  v_or3_b32 v10, v10, v11, v12\n"
    "  s_waitcnt lgkmcnt(4)\n"
    "  v_or_b32 v10, v10, v13\n"
    "  v_cmp_ne_u32 vcc, 0, v10\n"
    "  ds_read_b64_tr_b16 v[60:61], v209 offset:20480\n"
    "  ds_read_b64_tr_b16 v[62:63], v209 offset:22528\n"
    "  ds_read_b128 v[64:67], v206 offset:10240\n"
    "  ds_read_b128 v[68:71], v206 offset:11264\n"
    "  s_cbranch_vccnz .Lgqa_fix0\n"
    "  s_waitcnt lgkmcnt(5)\n"
    "  v_mfma_f32_32x32x16_bf16 v[96:111], v[48:51], v[52:55], v[96:111]\n"
    "  ds_read_b64_tr_b16 v[72:73], v209 offset:24576\n"
    "  ds_read_b64_tr_b16 v[74:75], v209 offset:26624\n"
    "  ds_read_b128 v[76:79], v206 offset:12288\n"
    "  ds_read_b128 v[80:83], v206 offset:13312\n"
    "  s_waitcnt lgkmcnt(8)\n"
    "  v_mfma_f32_32x32x16_bf16 v[112:127], v[48:51], v[56:59], v[112:127]\n"
    "  ds_read_b64_tr_b16 v[84:85], v209 offset:28672\n"
    "  ds_read_b64_tr_b16 v[86:87], v209 offset:30720\n"
    "  ds_read_b128 v[88:91], v206 offset:14336\n"
    "  ds_read_b128 v[92:95], v206 offset:15360\n"
    "  s_waitcnt lgkmcnt(9)\n"
    "  v_mfma_f32_32x32x16_bf16 v[96:111], v[60:63], v[64:67], v[96:111]\n"
    "  s_cmp_eq_u32 s23, 2\n"
    "  s_cselect_b32 s69, 0, 2.0\n"
    "  s_add_u32 m0, s63, 16384\n"
    "  s_nop 0\n"
    "  buffer_load_dword v238, s[48:51], s69 offen lds\n"
    "  s_mov_b32 s69, 81920\n"
    "  buffer_load_dwordx4 v234, s[44:47], s69 offen offset:0 nt lds\n"
    "  s_waitcnt lgkmcnt(8)\n"
    "  v_mfma_f32_32x32x16_bf16 v[112:127], v[60:63], v[68:71], v[112:127]\n"
    "  s_waitcnt lgkmcnt(5)\n"
    "  v_mfma_f32_32x32x16_bf16 v[96:111], v[72:75], v[76:79], v[96:111]\n"
    "  buffer_load_dwordx4 v235, s[44:47], s69 offen offset:1024 nt lds\n"
    "  s_waitcnt lgkmcnt(4)\n"
    "  v_mfma_f32_32x32x16_bf16 v[112:127], v[72:75], v[80:83], v[112:127]\n"
    "  s_waitcnt lgkmcnt(1)\n"
    "  v_mfma_f32_32x32x16_bf16 v[96:111], v[84:87], v[88:91], v[96:111]\n"
    "  buffer_load_dwordx4 v236, s[44:47], s69 offen offset:2048 nt lds\n"
    "  s_waitcnt lgkmcnt(0)\n"
    "  v_mfma_f32_32x32x16_bf16 v[112:127], v[84:87], v[92:95], v[112:127]\n"
    "  buffer_load_dwordx4 v237, s[44:47], s69 offen offset:3072 nt lds\n"
    "  s_waitcnt vmcnt(14) lgkmcnt(0)\n"
    "  s_barrier\n"
    "  ds_read_b64_tr_b16 v[48:49], v209 offset:32768\n"
    "  ds_read_b64_tr_b16 v[50:51], v209 offset:34816\n"
    "  ds_read_b128 v[52:55], v206 offset:0\n"
    "  ds_read_b128 v[56:59], v206 offset:1024\n"
    "  ds_read_b64_tr_b16 v[60:61], v209 offset:36864\n"
    "  ds_read_b64_tr_b16 v[62:63], v209 offset:38912\n"
    "  ds_read_b128 v[64:67], v206 offset:2048\n"
    "  ds_read_b128 v[68:71], v206 offset:3072\n"
    "  s_waitcnt lgkmcnt(5)\n"
    "  v_mfma_f32_32x32x16_bf16 v[96:111], v[48:51], v[52:55], v[96:111]\n"
    "  ds_read_b64_tr_b16 v[72:73], v209 offset:40960\n"
    "  ds_read_b64_tr_b16 v[74:75], v209 offset:43008\n"
    "  ds_read_b128 v[76:79], v206 offset:4096\n"
    "  ds_read_b128 v[80:83], v206 offset:5120\n"
    "  s_waitcnt lgkmcnt(8)\n"
    "  v_mfma_f32_32x32x16_bf16 v[112:127], v[48:51], v[56:59], v[112:127]\n"
    "  ds_read_b64_tr_b16 v[84:85], v209 offset:45056\n"
    "  ds_read_b64_tr_b16 v[86:87], v209 offset:47104\n"
    "  ds_read_b128 v[88:91], v206 offset:6144\n"
    "  ds_read_b128 v[92:95], v206 offset:7168\n"
    "  s_waitcnt lgkmcnt(9)\n"
    "  v_mfma_f32_32x32x16_bf16 v[96:111], v[60:63], v[64:67], v[96:111]\n"
    "  s_add_u32 m0, s63, 32768\n"
    "  s_mov_b32 s69, 98304\n"
    "  buffer_load_dwordx4 v234, s[44:47], s69 offen offset:0 nt lds\n"
    "  s_waitcnt lgkmcnt(8)\n"
    "  v_mfma_f32_32x32x16_bf16 v[112:127], v[60:63], v[68:71], v[112:127]\n"
    "  s_waitcnt lgkmcnt(5)\n"
    "  v_mfma_f32_32x32x16_bf16 v[96:111], v[72:75], v[76:79], v[96:111]\n"
    "  buffer_load_dwordx4 v235, s[44:47], s69 offen offset:1024 nt lds\n"
    "  s_waitcnt lgkmcnt(4)\n"
    "  v_mfma_f32_32x32x16_bf16 v[112:127], v[72:75], v[80:83], v[112:127]\n"
    "  s_waitcnt lgkmcnt(1)\n"
    "  v_mfma_f32_32x32x16_bf16 v[96:111], v[84:87], v[88:91], v[96:111]\n"
    "  buffer_load_dwordx4 v236, s[44:47], s69 offen offset:2048 nt lds\n"
    "  s_waitcnt lgkmcnt(0)\n"
    "  v_mfma_f32_32x32x16_bf16 v[112:127], v[84:87], v[92:95], v[112:127]\n"
    "  buffer_load_dwordx4 v237, s[44:47], s69 offen offset:3072 nt lds\n"
    "  s_waitcnt vmcnt(13) lgkmcnt(0)\n"
    "  s_barrier\n"
    "  ds_read_b64_tr_b16 v[48:49], v209 offset:49152\n"
    "  ds_read_b64_tr_b16 v[50:51], v209 offset:51200\n"
    "  ds_read_b128 v[52:55], v206 offset:8192\n"
    "  ds_read_b128 v[56:59], v206 offset:9216\n"
    "  ds_read_b64_tr_b16 v[60:61], v209 offset:53248\n"
    "  ds_read_b64_tr_b16 v[62:63], v209 offset:55296\n"
    "  ds_read_b128 v[64:67], v206 offset:10240\n"
    "  ds_read_b128 v[68:71], v206 offset:11264\n"
    "  s_waitcnt lgkmcnt(5)\n"
    "  v_mfma_f32_32x32x16_bf16 v[96:111], v[48:51], v[52:55], v[96:111]\n"
    "  ds_read_b64_tr_b16 v[72:73], v209 offset:57344\n"
    "  ds_read_b64_tr_b16 v[74:75], v209 offset:59392\n"
    "  ds_read_b128 v[76:79], v206 offset:12288\n"
    "  ds_read_b128 v[80:83], v206 offset:13312\n"
    "  s_waitcnt lgkmcnt(8)\n"
    "  v_mfma_f32_32x32x16_bf16 v[112:127], v[48:51], v[56:59], v[112:127]\n"
    "  ds_read_b64_tr_b16 v[84:85], v209 offset:61440\n"
    "  ds_read_b64_tr_b16 v[86:87], v209 offset:63488\n"
    "  ds_read_b128 v[88:91], v206 offset:14336\n"
    "  ds_read_b128 v[92:95], v206 offset:15360\n"
    "  s_waitcnt lgkmcnt(9)\n"
    "  v_mfma_f32_32x32x16_bf16 v[96:111], v[60:63], v[64:67], v[96:111]\n"
    "  s_cmp_eq_u32 s23, 3\n"
    "  s_cselect_b32 s69, 0, 2.0\n"
    "  s_add_u32 m0, s63, 49152\n"
    "  s_nop 0\n"
    "  buffer_load_dword v238, s[48:51], s69 offen lds\n"
    "  s_mov_b32 s69, 114688\n"
    "  buffer_load_dwordx4 v234, s[44:47], s69 offen offset:0 nt lds\n"
    "  s_waitcnt lgkmcnt(8)\n"
    "  v_mfma_f32_32x32x16_bf16 v[112:127], v[60:63], v[68:71], v[112:127]\n"
    "  s_waitcnt lgkmcnt(5)\n"
    "  v_mfma_f32_32x32x16_bf16 v[96:111], v[72:75], v[76:79], v[96:111]\n"
    "  buffer_load_dwordx4 v235, s[44:47], s69 offen offset:1024 nt lds\n"
    "  s_waitcnt lgkmcnt(4)\n"
    "  v_mfma_f32_32x32x16_bf16 v[112:127], v[72:75], v[80:83], v[112:127]\n"
    "  s_waitcnt lgkmcnt(1)\n"
    "  v_mfma_f32_32x32x16_bf16 v[96:111], v[84:87], v[88:91], v[96:111]\n"
    "  buffer_load_dwordx4 v236, s[44:47], s69 offen offset:2048 nt lds\n"
    "  s_waitcnt lgkmcnt(0)\n"
    "  v_mfma_f32_32x32x16_bf16 v[112:127], v[84:87], v[92:95], v[112:127]\n"
    "  buffer_load_dwordx4 v237, s[44:47], s69 offen offset:3072 nt lds\n"
    "  s_waitcnt vmcnt(14) lgkmcnt(0)\n"
    "  s_barrier\n"
    "  ds_read_b64_tr_b16 v[48:49], v210 offset:0\n"
    "  ds_read_b64_tr_b16 v[50:51], v210 offset:2048\n"
    "  ds_read_b128 v[52:55], v206 offset:0\n"
    "  ds_read_b128 v[56:59], v206 offset:1024\n"
    "  ds_read_b64_tr_b16 v[60:61], v210 offset:4096\n"
    "  ds_read_b64_tr_b16 v[62:63], v210 offset:6144\n"
    "  ds_read_b128 v[64:67], v206 offset:2048\n"
    "  ds_read_b128 v[68:71], v206 offset:3072\n"
    "  s_waitcnt lgkmcnt(5)\n"
    "  v_mfma_f32_32x32x16_bf16 v[96:111], v[48:51], v[52:55], v[96:111]\n"
    "  ds_read_b64_tr_b16 v[72:73], v210 offset:8192\n"
    "  ds_read_b64_tr_b16 v[74:75], v210 offset:10240\n"
    "  ds_read_b128 v[76:79], v206 offset:4096\n"
    "  ds_read_b128 v[80:83], v206 offset:5120\n"
    "  s_waitcnt lgkmcnt(8)\n"
    "  v_mfma_f32_32x32x16_bf16 v[112:127], v[48:51], v[56:59], v[112:127]\n"
    "  ds_read_b64_tr_b16 v[84:85], v210 offset:12288\n"
    "  ds_read_b64_tr_b16 v[86:87], v210 offset:14336\n"
    "  ds_read_b128 v[88:91], v206 offset:6144\n"
    "  ds_read_b128 v[92:95], v206 offset:7168\n"
    "  s_waitcnt lgkmcnt(9)\n"
    "  v_mfma_f32_32x32x16_bf16 v[96:111], v[60:63], v[64:67], v[96:111]\n"
    "  s_waitcnt lgkmcnt(8)\n"
    "  v_mfma_f32_32x32x16_bf16 v[112:127], v[60:63], v[68:71], v[112:127]\n"
    "  s_waitcnt lgkmcnt(5)\n"
    "  v_mfma_f32_32x32x16_bf16 v[96:111], v[72:75], v[76:79], v[96:111]\n"
    "  s_waitcnt lgkmcnt(4)\n"
    "  v_mfma_f32_32x32x16_bf16 v[112:127], v[72:75], v[80:83], v[112:127]\n"
    "  s_waitcnt lgkmcnt(1)\n"
    "  v_mfma_f32_32x32x16_bf16 v[96:111], v[84:87], v[88:91], v[96:111]\n"
    "  s_waitcnt lgkmcnt(0)\n"
    "  v_mfma_f32_32x32x16_bf16 v[112:127], v[84:87], v[92:95], v[112:127]\n"
    "  s_waitcnt vmcnt(9) lgkmcnt(0)\n"
    "  s_barrier\n"
    "  ds_read_b64_tr_b16 v[48:49], v210 offset:16384\n"
    "  ds_read_b64_tr_b16 v[50:51], v210 offset:18432\n"
    "  ds_read_b128 v[52:55], v206 offset:8192\n"
    "  ds_read_b128 v[56:59], v206 offset:9216\n"
    "  ds_read_b64_tr_b16 v[60:61], v210 offset:20480\n"
    "  ds_read_b64_tr_b16 v[62:63], v210 offset:22528\n"
    "  ds_read_b128 v[64:67], v206 offset:10240\n"
    "  ds_read_b128 v[68:71], v206 offset:11264\n"
    "  s_waitcnt lgkmcnt(5)\n"
    "  v_mfma_f32_32x32x16_bf16 v[96:111], v[48:51], v[52:55], v[96:111]\n"
    "  ds_read_b64_tr_b16 v[72:73], v210 offset:24576\n"
    "  ds_read_b64_tr_b16 v[74:75], v210 offset:26624\n"
    "  ds_read_b128 v[76:79], v206 offset:12288\n"
    "  ds_read_b128 v[80:83], v206 offset:13312\n"
    "  s_waitcnt lgkmcnt(8)\n"
    "  v_mfma_f32_32x32x16_bf16 v[112:127], v[48:51], v[56:59], v[112:127]\n"
    "  ds_read_b64_tr_b16 v[84:85], v210 offset:28672\n"
    "  ds_read_b64_tr_b16 v[86:87], v210 offset:30720\n"
    "  ds_read_b128 v[88:91], v206 offset:14336\n"
    "  ds_read_b128 v[92:95], v206 offset:15360\n"
    "  s_waitcnt lgkmcnt(9)\n"
    "  v_mfma_f32_32x32x16_bf16 v[96:111], v[60:63], v[64:67], v[96:111]\n"
    "  s_waitcnt lgkmcnt(8)\n"
    "  v_mfma_f32_32x32x16_bf16 v[112:127], v[60:63], v[68:71], v[112:127]\n"
    "  s_waitcnt lgkmcnt(5)\n"
    "  v_mfma_f32_32x32x16_bf16 v[96:111], v[72:75], v[76:79], v[96:111]\n"
    "  s_waitcnt lgkmcnt(4)\n"
    "  v_mfma_f32_32x32x16_bf16 v[112:127], v[72:75], v[80:83], v[112:127]\n"
    "  s_waitcnt lgkmcnt(1)\n"
    "  v_mfma_f32_32x32x16_bf16 v[96:111], v[84:87], v[88:91], v[96:111]\n"
    "  s_waitcnt lgkmcnt(0)\n"
    "  v_mfma_f32_32x32x16_bf16 v[112:127], v[84:87], v[92:95], v[112:127]\n"
    "  s_waitcnt vmcnt(5) lgkmcnt(0)\n"
    "  s_barrier\n"
    "  ds_read_b64_tr_b16 v[48:49], v210 offset:32768\n"
    "  ds_read_b64_tr_b16 v[50:51], v210 offset:34816\n"
    "  ds_read_b128 v[52:55], v206 offset:0\n"
    "  ds_read_b128 v[56:59], v206 offset:1024\n"
    "  ds_read_b64_tr_b16 v[60:61], v210 offset:36864\n"
    "  ds_read_b64_tr_b16 v[62:63], v210 offset:38912\n"
    "  ds_read_b128 v[64:67], v206 offset:2048\n"
    "  ds_read_b128 v[68:71], v206 offset:3072\n"
    "  s_waitcnt lgkmcnt(5)\n"
    "  v_mfma_f32_32x32x16_bf16 v[96:111], v[48:51], v[52:55], v[96:111]\n"
    "  ds_read_b64_tr_b16 v[72:73], v210 offset:40960\n"
    "  ds_read_b64_tr_b16 v[74:75], v210 offset:43008\n"
    "  ds_read_b128 v[76:79], v206 offset:4096\n"
    "  ds_read_b128 v[80:83], v206 offset:5120\n"
    "  s_waitcnt lgkmcnt(8)\n"
    "  v_mfma_f32_32x32x16_bf16 v[112:127], v[48:51], v[56:59], v[112:127]\n"
    "  ds_read_b64_tr_b16 v[84:85], v210 offset:45056\n"
    "  ds_read_b64_tr_b16 v[86:87], v210 offset:47104\n"
    "  ds_read_b128 v[88:91], v206 offset:6144\n"
    "  ds_read_b128 v[92:95], v206 offset:7168\n"
    "  s_waitcnt lgkmcnt(9)\n"
    "  v_mfma_f32_32x32x16_bf16 v[96:111], v[60:63], v[64:67], v[96:111]\n"
    "  s_waitcnt lgkmcnt(8)\n"
    "  v_mfma_f32_32x32x16_bf16 v[112:127], v[60:63], v[68:71], v[112:127]\n"
    "  s_waitcnt lgkmcnt(5)\n"
    "  v_mfma_f32_32x32x16_bf16 v[96:111], v[72:75], v[76:79], v[96:111]\n"
    "  s_waitcnt lgkmcnt(4)\n"
    "  v_mfma_f32_32x32x16_bf16 v[112:127], v[72:75], v[80:83], v[112:127]\n"
    "  s_waitcnt lgkmcnt(1)\n"
    "  v_mfma_f32_32x32x16_bf16 v[96:111], v[84:87], v[88:91], v[96:111]\n"
    "  s_waitcnt lgkmcnt(0)\n"
    "  v_mfma_f32_32x32x16_bf16 v[112:127], v[84:87], v[92:95], v[112:127]\n"
    "  v_add_u32 v14, s63, v214\n"
    "  s_waitcnt vmcnt(0) lgkmcnt(0)\n"
    "  s_barrier\n"
    "  ds_read_b64_tr_b16 v[48:49], v210 offset:49152\n"
    "  ds_read_b64_tr_b16 v[50:51], v210 offset:51200\n"
    "  ds_read_b128 v[52:55], v206 offset:8192\n"
    "  ds_read_b64_tr_b16 v[60:61], v210 offset:53248\n"
    "  ds_read_b64_tr_b16 v[62:63], v210 offset:55296\n"
    "  ds_read_b128 v[64:67], v206 offset:10240\n"
    "  s_waitcnt lgkmcnt(3)\n"
    "  v_mfma_f32_32x32x16_bf16 v[96:111], v[48:51], v[52:55], v[96:111]\n"
    "  ds_read_b64_tr_b16 v[72:73], v210 offset:57344\n"
    "  ds_read_b64_tr_b16 v[74:75], v210 offset:59392\n"
    "  ds_read_b128 v[76:79], v206 offset:12288\n"
    "  ds_read_b32 v10, v14 offset:16384\n"
    "  ds_read_b32 v11, v14 offset:16640\n"
    "  ds_read_b32 v12, v14 offset:16896\n"
    "  ds_read_b32 v13, v14 offset:17152\n"
    "  s_waitcnt lgkmcnt(7)\n"
    "  v_mfma_f32_32x32x16_bf16 v[96:111], v[60:63], v[64:67], v[96:111]\n"
    "  ds_read_b64_tr_b16 v[84:85], v210 offset:61440\n"
    "  ds_read_b64_tr_b16 v[86:87], v210 offset:63488\n"
    "  ds_read_b128 v[88:91], v206 offset:14336\n"
    "  s_waitcnt lgkmcnt(7)\n"
    "  v_mfma_f32_32x32x16_bf16 v[96:111], v[72:75], v[76:79], v[96:111]\n"
    "  ds_read_b128 v[56:59], v206 offset:9216\n"
    "  ds_read_b128 v[68:71], v206 offset:11264\n"
    "  s_waitcnt lgkmcnt(2)\n"
    "  v_mfma_f32_32x32x16_bf16 v[96:111], v[84:87], v[88:91], v[96:111]\n"
    "  ds_read_b128 v[80:83], v206 offset:13312\n"
    "  ds_read_b128 v[92:95], v206 offset:15360\n"
    "  v_add_f32 v10, v10, v12\n"
    "  v_add_f32 v11, v11, v13\n"
    "  v_rcp_f32 v10, v10\n"
    "  v_rcp_f32 v11, v11\n"
    "  s_waitcnt lgkmcnt(3)\n"
    "  v_mfma_f32_32x32x16_bf16 v[112:127], v[48:51], v[56:59], v[112:127]\n"
    "  s_waitcnt lgkmcnt(2)\n"
    "  v_mfma_f32_32x32x16_bf16 v[112:127], v[60:63], v[68:71], v[112:127]\n"
    "  s_nop 1\n"
    "  v_mul_f32 v96, v10, v96\n"
    "  v_mul_f32 v97, v10, v97\n"
    "  v_mul_f32 v98, v10, v98\n"
    "  v_mul_f32 v99, v10, v99\n"
    "  v_mul_f32 v100, v10, v100\n"
    "  v_mul_f32 v101, v10, v101\n"
    "  v_mul_f32 v102, v10, v102\n"
    "  v_mul_f32 v103, v10, v103\n"
    "  s_waitcnt lgkmcnt(1)\n"
    "  v_mfma_f32_32x32x16_bf16 v[112:127], v[72:75], v[80:83], v[112:127]\n"
    "  v_cvt_pk_bf16_f32 v24, v96, v97\n"
    "  v_cvt_pk_bf16_f32 v25, v98, v99\n"
    "  v_cvt_pk_bf16_f32 v26, v100, v101\n"
    "  v_cvt_pk_bf16_f32 v27, v102, v103\n"
    "  buffer_store_dwordx4 v[24:27], v215, s[28:31], s25 offen offset:0 sc1\n"
    "  v_mul_f32 v104, v10, v104\n"
    "  v_mul_f32 v105, v10, v105\n"
    "  v_mul_f32 v106, v10, v106\n"
    "  v_mul_f32 v107, v10, v107\n"
    "  v_mul_f32 v108, v10, v108\n"
    "  v_mul_f32 v109, v10, v109\n"
    "  v_mul_f32 v110, v10, v110\n"
    "  v_mul_f32 v111, v10, v111\n"
    "  s_waitcnt lgkmcnt(0)\n"
    "  v_mfma_f32_32x32x16_bf16 v[112:127], v[84:87], v[92:95], v[112:127]\n"
    "  v_cvt_pk_bf16_f32 v28, v104, v105\n"
    "  v_cvt_pk_bf16_f32 v29, v106, v107\n"
    "  v_cvt_pk_bf16_f32 v30, v108, v109\n"
    "  v_cvt_pk_bf16_f32 v31, v110, v111\n"
    "  buffer_store_dwordx4 v[28:31], v215, s[28:31], s25 offen offset:2048 sc1\n"
    "  v_add3_u32 v40, v24, v25, s24\n"
    "  v_add3_u32 v40, v26, v27, v40\n"
    "  v_add3_u32 v40, v28, v29, v40\n"
    "  v_add3_u32 v40, v30, v31, v40\n"
    "  s_nop 2\n"
    "  v_pk_mul_f32 v[112:113], v[112:113], v[10:11] op_sel:[0,1] op_sel_hi:[1,1]\n"
    "  v_pk_mul_f32 v[114:115], v[114:115], v[10:11] op_sel:[0,1] op_sel_hi:[1,1]\n"
    "  v_pk_mul_f32 v[116:117], v[116:117], v[10:11] op_sel:[0,1] op_sel_hi:[1,1]\n"
    "  v_pk_mul_f32 v[118:119], v[118:119], v[10:11] op_sel:[0,1] op_sel_hi:[1,1]\n"
    "  v_pk_mul_f32 v[120:121], v[120:121], v[10:11] op_sel:[0,1] op_sel_hi:[1,1]\n"
    "  v_pk_mul_f32 v[122:123], v[122:123], v[10:11] op_sel:[0,1] op_sel_hi:[1,1]\n"
    "  v_pk_mul_f32 v[124:125], v[124:125], v[10:11] op_sel:[0,1] op_sel_hi:[1,1]\n"
    "  v_pk_mul_f32 v[126:127], v[126:127], v[10:11] op_sel:[0,1] op_sel_hi:[1,1]\n"
    "  v_cvt_pk_bf16_f32 v32, v112, v113\n"
    "  v_cvt_pk_bf16_f32 v33, v114, v115\n"
    "  v_cvt_pk_bf16_f32 v34, v116, v117\n"
    "  v_cvt_pk_bf16_f32 v35, v118, v119\n"
    "  buffer_store_dwordx4 v[32:35], v215, s[28:31], s25 offen offset:1024 sc1\n"
    "  v_cvt_pk_bf16_f32 v36, v120, v121\n"
    "  v_cvt_pk_bf16_f32 v37, v122, v123\n"
    "  v_cvt_pk_bf16_f32 v38, v124, v125\n"
    "  v_cvt_pk_bf16_f32 v39, v126, v127\n"
    "  buffer_store_dwordx4 v[36:39], v215, s[28:31], s25 offen offset:3072 sc1\n"
    "  v_add3_u32 v40, v32, v33, v40\n"
    "  v_add3_u32 v40, v34, v35, v40\n"
    "  v_add3_u32 v40, v36, v37, v40\n"
    "  v_add3_u32 v40, v38, v39, v40\n"
    "  buffer_store_dword v40, v217, s[28:31], s70 offen offset:0 sc1\n"
    "  buffer_store_dword v20, v222, s[28:31], 0 offen offset:0 sc1\n"
    "  ds_write_b128 v216, v[24:27] offset:0\n"
    "  ds_write_b128 v216, v[28:31] offset:2048\n"
    "  ds_write_b128 v216, v[32:35] offset:1024\n"
    "  ds_write_b128 v216, v[36:39] offset:3072\n"
    "  s_waitcnt lgkmcnt(0)\n"
    "  s_barrier\n"
    "  ds_read_b64_tr_b16 v[48:49], v213 offset:0\n"
    "  ds_read_b64_tr_b16 v[50:51], v213 offset:1024\n"
    "  ds_read_b128 v[52:55], v207 offset:0\n"
    "  ds_read_b64_tr_b16 v[56:57], v213 offset:2048\n"
    "  ds_read_b64_tr_b16 v[58:59], v213 offset:3072\n"
    "  ds_read_b128 v[60:63], v207 offset:2048\n"
    "  ds_read_b64_tr_b16 v[64:65], v213 offset:4096\n"
    "  ds_read_b64_tr_b16 v[66:67], v213 offset:5120\n"
    "  ds_read_b128 v[68:71], v207 offset:4096\n"
    "  s_waitcnt lgkmcnt(6)\n"
    "  v_mfma_f32_32x32x16_bf16 v[80:95], v[48:51], v[52:55], 0\n"
    "  ds_read_b64_tr_b16 v[72:73], v213 offset:6144\n"
    "  ds_read_b64_tr_b16 v[74:75], v213 offset:7168\n"
    "  ds_read_b128 v[76:79], v207 offset:6144\n"
    "  s_waitcnt lgkmcnt(6)\n"
    "  v_mfma_f32_32x32x16_bf16 v[80:95], v[56:59], v[60:63], v[80:95]\n"
    "  ds_read_b64_tr_b16 v[48:49], v213 offset:8192\n"
    "  ds_read_b64_tr_b16 v[50:51], v213 offset:9216\n"
    "  ds_read_b128 v[52:55], v207 offset:8192\n"
    "  s_waitcnt lgkmcnt(6)\n"
    "  v_mfma_f32_32x32x16_bf16 v[80:95], v[64:67], v[68:71], v[80:95]\n"
    "  ds_read_b64_tr_b16 v[56:57], v213 offset:10240\n"
    "  ds_read_b64_tr_b16 v[58:59], v213 offset:11264\n"
    "  ds_read_b128 v[60:63], v207 offset:10240\n"
    "  s_waitcnt lgkmcnt(6)\n"
    "  v_mfma_f32_32x32x16_bf16 v[80:95], v[72:75], v[76:79], v[80:95]\n"
    "  ds_read_b64_tr_b16 v[64:65], v213 offset:12288\n"
    "  ds_read_b64_tr_b16 v[66:67], v213 offset:13312\n"
    "  ds_read_b128 v[68:71], v207 offset:12288\n"
    "  s_waitcnt lgkmcnt(6)\n"
    "  v_mfma_f32_32x32x16_bf16 v[80:95], v[48:51], v[52:55], v[80:95]\n"
    "  ds_read_b64_tr_b16 v[72:73], v213 offset:14336\n"
    "  ds_read_b64_tr_b16 v[74:75], v213 offset:15360\n"
    "  ds_read_b128 v[76:79], v207 offset:14336\n"
    "  s_waitcnt lgkmcnt(6)\n"
    "  v_mfma_f32_32x32x16_bf16 v[80:95], v[56:59], v[60:63], v[80:95]\n"
    "  s_waitcnt lgkmcnt(3)\n"
    "  v_mfma_f32_32x32x16_bf16 v[80:95], v[64:67], v[68:71], v[80:95]\n"
    "  s_waitcnt lgkmcnt(0)\n"
    "  v_mfma_f32_32x32x16_bf16 v[80:95], v[72:75], v[76:79], v[80:95]\n"
    "  ds_read_b64_tr_b16 v[160:161], v209 offset:16384\n"
    "  ds_read_b64_tr_b16 v[162:163], v209 offset:18432\n"
    "  ds_read_b128 v[24:27], v206 offset:16384\n"
    "  ds_read_b128 v[28:31], v206 offset:17408\n"
    "  ds_read_b64_tr_b16 v[164:165], v209 offset:20480\n"
    "  ds_read_b64_tr_b16 v[166:167], v209 offset:22528\n"
    "  ds_read_b128 v[32:35], v206 offset:18432\n"
    "  ds_read_b128 v[36:39], v206 offset:19456\n"
    "  s_waitcnt lgkmcnt(5)\n"
    "  v_mfma_f32_32x32x16_bf16 v[96:111], v[160:163], v[24:27], 0\n"
    "  ds_read_b64_tr_b16 v[168:169], v209 offset:24576\n"
    "  ds_read_b64_tr_b16 v[170:171], v209 offset:26624\n"
    "  ds_read_b128 v[40:43], v206 offset:20480\n"
    "  s_waitcnt lgkmcnt(7)\n"
    "  v_mfma_f32_32x32x16_bf16 v[112:127], v[160:163], v[28:31], 0\n"
    "  ds_read_b128 v[44:47], v206 offset:21504\n"
    "  s_waitcnt lgkmcnt(5)\n"
    "  v_mfma_f32_32x32x16_bf16 v[96:111], v[164:167], v[32:35], v[96:111]\n"
    "  ds_read_b64_tr_b16 v[172:173], v209 offset:28672\n"
    "  ds_read_b64_tr_b16 v[174:175], v209 offset:30720\n"
    "  ds_read_b128 v[24:27], v206 offset:22528\n"
    "  s_waitcnt lgkmcnt(7)\n"
    "  v_mfma_f32_32x32x16_bf16 v[112:127], v[164:167], v[36:39], v[112:127]\n"
    "  ds_read_b128 v[28:31], v206 offset:23552\n"
    "  s_waitcnt lgkmcnt(5)\n"
    "  v_mfma_f32_32x32x16_bf16 v[96:111], v[168:171], v[40:43], v[96:111]\n"
    "  ds_read_b64_tr_b16 v[176:177], v209 offset:32768\n"
    "  ds_read_b64_tr_b16 v[178:179], v209 offset:34816\n"
    "  ds_read_b128 v[32:35], v206 offset:24576\n"
    "  v_cvt_pk_bf16_f32 v72, v80, v81\n"
    "  v_cvt_pk_bf16_f32 v73, v82, v83\n"
    "  s_waitcnt lgkmcnt(7)\n"
    "  v_mfma_f32_32x32x16_bf16 v[112:127], v[168:171], v[44:47], v[112:127]\n"
    "  ds_read_b128 v[36:39], v206 offset:25600\n"
    "  v_cvt_pk_bf16_f32 v74, v84, v85\n"
    "  v_cvt_pk_bf16_f32 v75, v86, v87\n"
    "  s_waitcnt lgkmcnt(5)\n"
    "  v_mfma_f32_32x32x16_bf16 v[96:111], v[172:175], v[24:27], v[96:111]\n"
    "  ds_read_b64_tr_b16 v[180:181], v209 offset:36864\n"
    "  ds_read_b64_tr_b16 v[182:183], v209 offset:38912\n"
    "  ds_read_b128 v[40:43], v206 offset:26624\n"
    "  v_cvt_pk_bf16_f32 v76, v88, v89\n"
    "  v_cvt_pk_bf16_f32 v77, v90, v91\n"
    "  s_waitcnt lgkmcnt(7)\n"
    "  v_mfma_f32_32x32x16_bf16 v[112:127], v[172:175], v[28:31], v[112:127]\n"
    "  ds_read_b128 v[44:47], v206 offset:27648\n"
    "  v_cvt_pk_bf16_f32 v78, v92, v93\n"
    "  v_cvt_pk_bf16_f32 v79, v94, v95\n"
    "  s_waitcnt lgkmcnt(5)\n"
    "  v_mfma_f32_32x32x16_bf16 v[96:111], v[176:179], v[32:35], v[96:111]\n"
    "  ds_read_b64_tr_b16 v[184:185], v209 offset:40960\n"
    "  ds_read_b64_tr_b16 v[186:187], v209 offset:43008\n"
    "  ds_read_b128 v[24:27], v206 offset:28672\n"
    "  s_waitcnt lgkmcnt(7)\n"
    "  v_mfma_f32_32x32x16_bf16 v[112:127], v[176:179], v[36:39], v[112:127]\n"
    "  ds_read_b128 v[28:31], v206 offset:29696\n"
    "  s_waitcnt lgkmcnt(5)\n"
    "  v_mfma_f32_32x32x16_bf16 v[96:111], v[180:183], v[40:43], v[96:111]\n"
    "  ds_read_b64_tr_b16 v[188:189], v209 offset:45056\n"
    "  ds_read_b64_tr_b16 v[190:191], v209 offset:47104\n"
    "  ds_read_b128 v[32:35], v206 offset:30720\n"
    "  s_waitcnt lgkmcnt(7)\n"
    "  v_mfma_f32_32x32x16_bf16 v[112:127], v[180:183], v[44:47], v[112:127]\n"
    "  ds_read_b128 v[36:39], v206 offset:31744\n"
    "  s_waitcnt lgkmcnt(5)\n"
    "  v_mfma_f32_32x32x16_bf16 v[96:111], v[184:187], v[24:27], v[96:111]\n"
    "  s_waitcnt lgkmcnt(4)\n"
    "  v_mfma_f32_32x32x16_bf16 v[112:127], v[184:187], v[28:31], v[112:127]\n"
    "  s_waitcnt lgkmcnt(0)\n"
    "  s_barrier\n"
    "  v_mfma_f32_32x32x16_bf16 v[96:111], v[188:191], v[32:35], v[96:111]\n"
    "  v_mfma_f32_32x32x16_bf16 v[112:127], v[188:191], v[36:39], v[112:127]\n"
    "  ds_write_b128 v216, v[72:75] offset:2048\n"
    "  ds_write_b128 v216, v[76:79] offset:3072\n"
    "  s_nop 8\n"
    "  v_mov_b64 v[128:129], v[96:97]\n"
    "  v_mov_b64 v[130:131], v[98:99]\n"
    "  v_mov_b64 v[132:133], v[100:101]\n"
    "  v_mov_b64 v[134:135], v[102:103]\n"
    "  v_mov_b64 v[136:137], v[104:105]\n"
    "  v_mov_b64 v[138:139], v[106:107]\n"
    "  v_mov_b64 v[140:141], v[108:109]\n"
    "  v_mov_b64 v[142:143], v[110:111]\n"
    "  v_mov_b64 v[144:145], v[112:113]\n"
    "  v_mov_b64 v[146:147], v[114:115]\n"
    "  v_mov_b64 v[148:149], v[116:117]\n"
    "  v_mov_b64 v[150:151], v[118:119]\n"
    "  v_mov_b64 v[152:153], v[120:121]\n"
    "  v_mov_b64 v[154:155], v[122:123]\n"
    "  v_mov_b64 v[156:157], v[124:125]\n"
    "  v_mov_b64 v[158:159], v[126:127]\n"
    "  ds_read_b64_tr_b16 v[160:161], v209 offset:49152\n"
    "  ds_read_b64_tr_b16 v[162:163], v209 offset:51200\n"
    "  ds_read_b64_tr_b16 v[164:165], v209 offset:53248\n"
    "  ds_read_b64_tr_b16 v[166:167], v209 offset:55296\n"
    "  ds_read_b64_tr_b16 v[168:169], v209 offset:57344\n"
    "  ds_read_b64_tr_b16 v[170:171], v209 offset:59392\n"
    "  ds_read_b64_tr_b16 v[172:173], v209 offset:61440\n"
    "  ds_read_b64_tr_b16 v[174:175], v209 offset:63488\n"
    "  ds_read_b64_tr_b16 v[176:177], v210 offset:0\n"
    "  ds_read_b64_tr_b16 v[178:179], v210 offset:2048\n"
    "  ds_read_b64_tr_b16 v[180:181], v210 offset:4096\n"
    "  ds_read_b64_tr_b16 v[182:183], v210 offset:6144\n"
    "  ds_read_b64_tr_b16 v[184:185], v210 offset:8192\n"
    "  ds_read_b64_tr_b16 v[186:187], v210 offset:10240\n"
    "  ds_read_b64_tr_b16 v[188:189], v210 offset:12288\n"
    "  ds_read_b64_tr_b16 v[190:191], v210 offset:14336\n"
    "  s_barrier\n"
    "  ds_read_b64_tr_b16 v[64:65], v210 offset:16384\n"
    "  ds_read_b64_tr_b16 v[66:67], v210 offset:18432\n"
    "  ds_read_b64_tr_b16 v[68:69], v210 offset:20480\n"
    "  ds_read_b64_tr_b16 v[70:71], v210 offset:22528\n"
    "  ds_read_b64_tr_b16 v[72:73], v210 offset:24576\n"
    "  ds_read_b64_tr_b16 v[74:75], v210 offset:26624\n"
    "  ds_read_b64_tr_b16 v[76:77], v210 offset:28672\n"
    "  ds_read_b64_tr_b16 v[78:79], v210 offset:30720\n"
    "  ds_read_b64_tr_b16 v[80:81], v210 offset:32768\n"
    "  ds_read_b64_tr_b16 v[82:83], v210 offset:34816\n"
    "  ds_read_b64_tr_b16 v[84:85], v210 offset:36864\n"
    "  ds_read_b64_tr_b16 v[86:87], v210 offset:38912\n"
    "  ds_read_b64_tr_b16 v[88:89], v210 offset:40960\n"
    "  ds_read_b64_tr_b16 v[90:91], v210 offset:43008\n"
    "  ds_read_b64_tr_b16 v[92:93], v210 offset:45056\n"
    "  ds_read_b64_tr_b16 v[94:95], v210 offset:47104\n"
    "  s_waitcnt lgkmcnt(0)\n"
    ".Lgqa_again:\n"
    "  s_barrier\n"
    "  ds_read_b128 v[24:27], v208 offset:49152\n"
    "  ds_read_b128 v[28:31], v208 offset:50176\n"
    "  ds_read_b128 v[32:35], v208 offset:51200\n"
    "  ds_read_b128 v[36:39], v208 offset:52224\n"
    "  s_waitcnt lgkmcnt(3)\n"
    "  v_mfma_f32_32x32x16_bf16 v[96:111], v[160:163], v[24:27], v[96:111]\n"
    "  ds_read_b128 v[40:43], v208 offset:53248\n"
    "  s_waitcnt lgkmcnt(3)\n"
    "  v_mfma_f32_32x32x16_bf16 v[112:127], v[160:163], v[28:31], v[112:127]\n"
    "  ds_read_b128 v[44:47], v208 offset:54272\n"
    "  s_waitcnt lgkmcnt(3)\n"
    "  v_mfma_f32_32x32x16_bf16 v[96:111], v[164:167], v[32:35], v[96:111]\n"
    "  ds_read_b128 v[24:27], v208 offset:55296\n"
    "  s_waitcnt lgkmcnt(3)\n"
    "  v_mfma_f32_32x32x16_bf16 v[112:127], v[164:167], v[36:39], v[112:127]\n"
    "  ds_read_b128 v[28:31], v208 offset:56320\n"
    "  s_waitcnt lgkmcnt(3)\n"
    "  v_mfma_f32_32x32x16_bf16 v[96:111], v[168:171], v[40:43], v[96:111]\n"
    "  ds_read_b128 v[32:35], v208 offset:57344\n"
    "  s_waitcnt lgkmcnt(3)\n"
    "  v_mfma_f32_32x32x16_bf16 v[112:127], v[168:171], v[44:47], v[112:127]\n"
    "  ds_read_b128 v[36:39], v208 offset:58368\n"
    "  s_waitcnt lgkmcnt(3)\n"
    "  v_mfma_f32_32x32x16_bf16 v[96:111], v[172:175], v[24:27], v[96:111]\n"
    "  ds_read_b128 v[40:43], v208 offset:59392\n"
    "  s_waitcnt lgkmcnt(3)\n"
    "  v_mfma_f32_32x32x16_bf16 v[112:127], v[172:175], v[28:31], v[112:127]\n"
    "  ds_read_b128 v[44:47], v208 offset:60416\n"
    "  s_waitcnt lgkmcnt(3)\n"
    "  v_mfma_f32_32x32x16_bf16 v[96:111], v[176:179], v[32:35], v[96:111]\n"
    "  ds_read_b128 v[24:27], v208 offset:61440\n"
    "  s_waitcnt lgkmcnt(3)\n"
    "  v_mfma_f32_32x32x16_bf16 v[112:127], v[176:179], v[36:39], v[112:127]\n"
    "  ds_read_b128 v[28:31], v208 offset:62464\n"
    "  s_waitcnt lgkmcnt(3)\n"
    "  v_mfma_f32_32x32x16_bf16 v[96:111], v[180:183], v[40:43], v[96:111]\n"
    "  ds_read_b128 v[32:35], v208 offset:63488\n"
    "  s_waitcnt lgkmcnt(3)\n"
    "  v_mfma_f32_32x32x16_bf16 v[112:127], v[180:183], v[44:47], v[112:127]\n"
    "  ds_read_b128 v[36:39], v208 offset:64512\n"
    "  s_waitcnt lgkmcnt(3)\n"
    "  v_mfma_f32_32x32x16_bf16 v[96:111], v[184:187], v[24:27], v[96:111]\n"
    "  s_barrier\n"
    "  ds_read_b128 v[40:43], v206 offset:0\n"
    "  s_waitcnt lgkmcnt(3)\n"
    "  v_mfma_f32_32x32x16_bf16 v[112:127], v[184:187], v[28:31], v[112:127]\n"
    "  ds_read_b128 v[44:47], v206 offset:1024\n"
    "  s_waitcnt lgkmcnt(3)\n"
    "  v_mfma_f32_32x32x16_bf16 v[96:111], v[188:191], v[32:35], v[96:111]\n"
    "  ds_read_b128 v[24:27], v206 offset:2048\n"
    "  s_waitcnt lgkmcnt(3)\n"
    "  v_mfma_f32_32x32x16_bf16 v[112:127], v[188:191], v[36:39], v[112:127]\n"
    "  ds_read_b128 v[28:31], v206 offset:3072\n"
    "  s_waitcnt lgkmcnt(3)\n"
    "  v_mfma_f32_32x32x16_bf16 v[96:111], v[64:67], v[40:43], v[96:111]\n"
    "  ds_read_b128 v[32:35], v206 offset:4096\n"
    "  s_waitcnt lgkmcnt(3)\n"
    "  v_mfma_f32_32x32x16_bf16 v[112:127], v[64:67], v[44:47], v[112:127]\n"
    "  ds_read_b128 v[36:39], v206 offset:5120\n"
    "  s_waitcnt lgkmcnt(3)\n"
    "  v_mfma_f32_32x32x16_bf16 v[96:111], v[68:71], v[24:27], v[96:111]\n"
    "  ds_read_b128 v[40:43], v206 offset:6144\n"
    "  s_waitcnt lgkmcnt(3)\n"
    "  v_mfma_f32_32x32x16_bf16 v[112:127], v[68:71], v[28:31], v[112:127]\n"
    "  ds_read_b128 v[44:47], v206 offset:7168\n"
    "  s_waitcnt lgkmcnt(3)\n"
    "  v_mfma_f32_32x32x16_bf16 v[96:111], v[72:75], v[32:35], v[96:111]\n"
    "  ds_read_b128 v[24:27], v206 offset:8192\n"
    "  s_waitcnt lgkmcnt(3)\n"
    "  v_mfma_f32_32x32x16_bf16 v[112:127], v[72:75], v[36:39], v[112:127]\n"
    "  ds_read_b128 v[28:31], v206 offset:9216\n"
    "  s_waitcnt lgkmcnt(3)\n"
    "  v_mfma_f32_32x32x16_bf16 v[96:111], v[76:79], v[40:43], v[96:111]\n"
    "  ds_read_b128 v[32:35], v206 offset:10240\n"
    "  s_waitcnt lgkmcnt(3)\n"
    "  v_mfma_f32_32x32x16_bf16 v[112:127], v[76:79], v[44:47], v[112:127]\n"
    "  ds_read_b128 v[36:39], v206 offset:11264\n"
    "  s_waitcnt lgkmcnt(3)\n"
    "  v_mfma_f32_32x32x16_bf16 v[96:111], v[80:83], v[24:27], v[96:111]\n"
    "  ds_read_b128 v[40:43], v206 offset:12288\n"
    "  s_waitcnt lgkmcnt(3)\n"
    "  v_mfma_f32_32x32x16_bf16 v[112:127], v[80:83], v[28:31], v[112:127]\n"
    "  ds_read_b128 v[44:47], v206 offset:13312\n"
    "  s_waitcnt lgkmcnt(3)\n"
    "  v_mfma_f32_32x32x16_bf16 v[96:111], v[84:87], v[32:35], v[96:111]\n"
    "  ds_read_b128 v[24:27], v206 offset:14336\n"
    "  s_waitcnt lgkmcnt(3)\n"
    "  v_mfma_f32_32x32x16_bf16 v[112:127], v[84:87], v[36:39], v[112:127]\n"
    "  ds_read_b128 v[28:31], v206 offset:15360\n"
    "  s_waitcnt lgkmcnt(3)\n"
    "  v_mfma_f32_32x32x16_bf16 v[96:111], v[88:91], v[40:43], v[96:111]\n"
    "  s_barrier\n"
    "  ds_read_b64_tr_b16 v[160:161], v209 offset:16384\n"
    "  ds_read_b64_tr_b16 v[162:163], v209 offset:18432\n"
    "  ds_read_b128 v[32:35], v212 offset:0\n"
    "  s_waitcnt lgkmcnt(5)\n"
    "  v_mfma_f32_32x32x16_bf16 v[112:127], v[88:91], v[44:47], v[112:127]\n"
    "  ds_read_b64_tr_b16 v[164:165], v209 offset:20480\n"
    "  ds_read_b64_tr_b16 v[166:167], v209 offset:22528\n"
    "  ds_read_b128 v[36:39], v212 offset:2048\n"
    "  s_waitcnt lgkmcnt(7)\n"
    "  v_mfma_f32_32x32x16_bf16 v[96:111], v[92:95], v[24:27], v[96:111]\n"
    "  ds_read_b64_tr_b16 v[168:169], v209 offset:24576\n"
    "  ds_read_b64_tr_b16 v[170:171], v209 offset:26624\n"
    "  ds_read_b128 v[40:43], v212 offset:4096\n"
    "  s_waitcnt lgkmcnt(9)\n"
    "  v_mfma_f32_32x32x16_bf16 v[112:127], v[92:95], v[28:31], v[112:127]\n"
    "  ds_read_b64_tr_b16 v[172:173], v209 offset:28672\n"
    "  ds_read_b64_tr_b16 v[174:175], v209 offset:30720\n"
    "  ds_read_b128 v[44:47], v212 offset:6144\n"
    "  s_waitcnt lgkmcnt(9)\n"
    "  v_mfma_f32_32x32x16_bf16 v[96:111], v[160:163], v[32:35], v[96:111]\n"
    "  s_barrier\n"
    "  ds_read_b64_tr_b16 v[176:177], v209 offset:32768\n"
    "  ds_read_b64_tr_b16 v[178:179], v209 offset:34816\n"
    "  ds_read_b128 v[24:27], v212 offset:8192\n"
    "  s_waitcnt lgkmcnt(9)\n"
    "  v_mfma_f32_32x32x16_bf16 v[96:111], v[164:167], v[36:39], v[96:111]\n"
    "  ds_read_b64_tr_b16 v[180:181], v209 offset:36864\n"
    "  ds_read_b64_tr_b16 v[182:183], v209 offset:38912\n"
    "  ds_read_b128 v[28:31], v212 offset:10240\n"
    "  s_waitcnt lgkmcnt(9)\n"
    "  v_mfma_f32_32x32x16_bf16 v[96:111], v[168:171], v[40:43], v[96:111]\n"
    "  ds_read_b64_tr_b16 v[184:185], v209 offset:40960\n"
    "  ds_read_b64_tr_b16 v[186:187], v209 offset:43008\n"
    "  ds_read_b128 v[32:35], v212 offset:12288\n"
    "  s_waitcnt lgkmcnt(9)\n"
    "  v_mfma_f32_32x32x16_bf16 v[96:111], v[172:175], v[44:47], v[96:111]\n"
    "  ds_read_b64_tr_b16 v[188:189], v209 offset:45056\n"
    "  ds_read_b64_tr_b16 v[190:191], v209 offset:47104\n"
    "  ds_read_b128 v[36:39], v212 offset:14336\n"
    "  s_waitcnt lgkmcnt(9)\n"
    "  v_mfma_f32_32x32x16_bf16 v[96:111], v[176:179], v[24:27], v[96:111]\n"
    "  ds_read_b128 v[40:43], v212 offset:1024\n"
    "  s_waitcnt lgkmcnt(7)\n"
    "  v_mfma_f32_32x32x16_bf16 v[96:111], v[180:183], v[28:31], v[96:111]\n"
    "  ds_read_b128 v[44:47], v212 offset:3072\n"
    "  s_waitcnt lgkmcnt(5)\n"
    "  v_mfma_f32_32x32x16_bf16 v[96:111], v[184:187], v[32:35], v[96:111]\n"
    "  ds_read_b128 v[24:27], v212 offset:5120\n"
    "  s_waitcnt lgkmcnt(3)\n"
    "  v_mfma_f32_32x32x16_bf16 v[96:111], v[188:191], v[36:39], v[96:111]\n"
    "  ds_read_b128 v[28:31], v212 offset:7168\n"
    "  ds_read_b128 v[192:195], v223\n"
    "  s_waitcnt lgkmcnt(4)\n"
    "  v_mfma_f32_32x32x16_bf16 v[112:127], v[160:163], v[40:43], v[112:127]\n"
    "  ds_read_b128 v[32:35], v212 offset:9216\n"
    "  s_waitcnt lgkmcnt(4)\n"
    "  v_mfma_f32_32x32x16_bf16 v[112:127], v[164:167], v[44:47], v[112:127]\n"
    "  ds_read_b128 v[36:39], v212 offset:11264\n"
    "  s_waitcnt lgkmcnt(2)\n"
    "  v_add3_u32 v196, v192, v193, v194\n"
    "  v_add_u32 v196, v196, v195\n"
    "  v_cmp_ne_u32 vcc, 4, v196\n"
    "  v_mfma_f32_32x32x16_bf16 v[112:127], v[168:171], v[24:27], v[112:127]\n"
    "  ds_read_b128 v[40:43], v212 offset:13312\n"
    "  v_cvt_pk_bf16_f32 v48, v96, v97\n"
    "  v_cvt_pk_bf16_f32 v49, v98, v99\n"
    "  v_cvt_pk_bf16_f32 v50, v100, v101\n"
    "  v_cvt_pk_bf16_f32 v51, v102, v103\n"
    "  v_mfma_f32_32x32x16_bf16 v[112:127], v[172:175], v[28:31], v[112:127]\n"
    "  ds_read_b128 v[44:47], v212 offset:15360\n"
    "  s_cbranch_vccnz .Lgqa_verd\n"
    ".Lgqa_good:\n"
    "  buffer_store_dwordx4 v[48:51], v215, s[28:31], s73 offen offset:0 sc1\n"
    "  v_cvt_pk_bf16_f32 v52, v104, v105\n"
    "  v_cvt_pk_bf16_f32 v53, v106, v107\n"
    "  s_waitcnt lgkmcnt(3)\n"
    "  v_mfma_f32_32x32x16_bf16 v[112:127], v[176:179], v[32:35], v[112:127]\n"
    "  v_cvt_pk_bf16_f32 v54, v108, v109\n"
    "  v_cvt_pk_bf16_f32 v55, v110, v111\n"
    "  v_add3_u32 v12, v48, v49, s24\n"
    "  v_add3_u32 v12, v50, v51, v12\n"
    "  s_waitcnt lgkmcnt(2)\n"
    "  v_mfma_f32_32x32x16_bf16 v[112:127], v[180:183], v[36:39], v[112:127]\n"
    "  buffer_store_dwordx4 v[52:55], v215, s[28:31], s73 offen offset:1024 sc1\n"
    "  v_add3_u32 v12, v52, v53, v12\n"
    "  v_add3_u32 v12, v54, v55, v12\n"
    "  s_waitcnt lgkmcnt(1)\n"
    "  v_mfma_f32_32x32x16_bf16 v[112:127], v[184:187], v[40:43], v[112:127]\n"
    "  buffer_store_dword v12, v218, s[28:31], s74 offen offset:0 sc1\n"
    "  s_waitcnt lgkmcnt(0)\n"
    "  v_mfma_f32_32x32x16_bf16 v[112:127], v[188:191], v[44:47], v[112:127]\n"
    "  s_nop 11\n"
    "  v_cvt_pk_bf16_f32 v56, v112, v113\n"
    "  v_cvt_pk_bf16_f32 v57, v114, v115\n"
    "  v_cvt_pk_bf16_f32 v58, v116, v117\n"
    "  v_cvt_pk_bf16_f32 v59, v118, v119\n"
    "  buffer_store_dwordx4 v[56:59], v215, s[28:31], s73 offen offset:2048 sc1\n"
    "  v_cvt_pk_bf16_f32 v60, v120, v121\n"
    "  v_cvt_pk_bf16_f32 v61, v122, v123\n"
    "  v_cvt_pk_bf16_f32 v62, v124, v125\n"
    "  v_cvt_pk_bf16_f32 v63, v126, v127\n"
    "  buffer_store_dwordx4 v[60:63], v215, s[28:31], s73 offen offset:3072 sc1\n"
    "  v_add3_u32 v13, v56, v57, s24\n"
    "  v_add3_u32 v13, v58, v59, v13\n"
    "  v_add3_u32 v13, v60, v61, v13\n"
    "  v_add3_u32 v13, v62, v63, v13\n"
    "  buffer_store_dword v13, v218, s[28:31], s74 offen offset:256 sc1\n"
    "  s_memrealtime s[60:61]\n"
    "  s_lshr_b32 s58, s18, 2\n"
    "  s_mul_i32 s58, s58, 20\n"
    "  s_add_u32 s58, s58, 59\n"
    "  s_mov_b32 s59, 1048576\n"
    ".Lgqa_go:\n"
    "  ds_read_b32 v1, v223 offset:16\n"
    "  s_sub_u32 s59, s59, 1\n"
    "  s_cbranch_scc1 .Lgqa_rl0\n"
    "  s_waitcnt lgkmcnt(0)\n"
    "  v_readfirstlane_b32 s62, v1\n"
    "  s_cmp_eq_u32 s62, 0\n"
    "  s_cbranch_scc1 .Lgqa_go\n"
    "  s_sub_u32 s62, s62, 25\n"
    "  s_max_i32 s62, s62, 0\n"
    "  s_add_u32 s58, s58, s60\n"
    "  s_add_u32 s58, s58, s62\n"
    "  s_movk_i32 s59, 256\n"
    ".Lgqa_gw:\n"
    "  s_memrealtime s[60:61]\n"
    "  s_sub_u32 s59, s59, 1\n"
    "  s_cbranch_scc1 .Lgqa_rl0\n"
    "  s_waitcnt lgkmcnt(0)\n"
    "  s_sub_u32 s60, s60, s58\n"
    "  s_cmp_lt_i32 s60, 0\n"
    "  s_cbranch_scc1 .Lgqa_gw\n"
    ".Lgqa_rl0:\n"
    "  s_mov_b32 s59, 1048576\n"
    "  s_waitcnt lgkmcnt(0)\n"
    "  s_add_u32 s60, s75, 46080\n"
    "  s_add_u32 s61, s76, 46080\n"
    ".Lgqa_rl:\n"
    "  buffer_load_dwordx4 v[24:27], v219, s[28:31], s75 offen offset:0 sc1\n"
    "  buffer_load_dwordx4 v[28:31], v219, s[28:31], s75 offen offset:1024 sc1\n"
    "  buffer_load_dwordx4 v[32:35], v219, s[28:31], s60 offen offset:0 sc1\n"
    "  buffer_load_dwordx4 v[36:39], v219, s[28:31], s60 offen offset:1024 sc1\n"
    "  buffer_load_dword v40, v220, s[28:31], s76 offen sc1\n"
    "  buffer_load_dword v41, v220, s[28:31], s61 offen sc1\n"
    "  s_waitcnt vmcnt(5)\n"
    "  v_lshlrev_b32 v64, 16, v24\n"
    "  v_and_b32 v65, 0xffff0000, v24\n"
    "  v_lshlrev_b32 v66, 16, v25\n"
    "  v_and_b32 v67, 0xffff0000, v25\n"
    "  v_lshlrev_b32 v68, 16, v26\n"
    "  v_and_b32 v69, 0xffff0000, v26\n"
    "  v_lshlrev_b32 v70, 16, v27\n"
    "  v_and_b32 v71, 0xffff0000, v27\n"
    "  s_waitcnt vmcnt(4)\n"
    "  v_lshlrev_b32 v72, 16, v28\n"
    "  v_and_b32 v73, 0xffff0000, v28\n"
    "  v_lshlrev_b32 v74, 16, v29\n"
    "  v_and_b32 v75, 0xffff0000, v29\n"
    "  v_lshlrev_b32 v76, 16, v30\n"
    "  v_and_b32 v77, 0xffff0000, v30\n"
    "  v_lshlrev_b32 v78, 16, v31\n"
    "  v_and_b32 v79, 0xffff0000, v31\n"
    "  s_waitcnt vmcnt(3)\n"
    "  v_dot2c_f32_bf16 v64, v21, v32\n"
    "  v_dot2c_f32_bf16 v65, v22, v32\n"
    "  v_dot2c_f32_bf16 v66, v21, v33\n"
    "  v_dot2c_f32_bf16 v67, v22, v33\n"
    "  v_dot2c_f32_bf16 v68, v21, v34\n"
    "  v_dot2c_f32_bf16 v69, v22, v34\n"
    "  v_dot2c_f32_bf16 v70, v21, v35\n"
    "  v_dot2c_f32_bf16 v71, v22, v35\n"
    "  s_waitcnt vmcnt(2)\n"
    "  v_dot2c_f32_bf16 v72, v21, v36\n"
    "  v_dot2c_f32_bf16 v73, v22, v36\n"
    "  v_dot2c_f32_bf16 v74, v21, v37\n"
    "  v_dot2c_f32_bf16 v75, v22, v37\n"
    "  v_dot2c_f32_bf16 v76, v21, v38\n"
    "  v_dot2c_f32_bf16 v77, v22, v38\n"
    "  v_dot2c_f32_bf16 v78, v21, v39\n"
    "  v_dot2c_f32_bf16 v79, v22, v39\n"
    "  v_permlane16_swap_b32 v64, v72\n"
    "  v_permlane16_swap_b32 v65, v73\n"
    "  v_permlane16_swap_b32 v66, v74\n"
    "  v_permlane16_swap_b32 v67, v75\n"
    "  v_permlane16_swap_b32 v68, v76\n"
    "  v_permlane16_swap_b32 v69, v77\n"
    "  v_permlane16_swap_b32 v70, v78\n"
    "  v_permlane16_swap_b32 v71, v79\n"
    "  v_pk_add_f32 v[64:65], v[64:65], v[72:73]\n"
    "  v_pk_add_f32 v[66:67], v[66:67], v[74:75]\n"
    "  v_pk_add_f32 v[68:69], v[68:69], v[76:77]\n"
    "  v_pk_add_f32 v[70:71], v[70:71], v[78:79]\n"
    "  s_nop 0\n"
    "  v_permlane32_swap_b32 v64, v68\n"
    "  v_permlane32_swap_b32 v65, v69\n"
    "  v_permlane32_swap_b32 v66, v70\n"
    "  v_permlane32_swap_b32 v67, v71\n"
    "  v_pk_add_f32 v[64:65], v[64:65], v[68:69]\n"
    "  v_pk_add_f32 v[66:67], v[66:67], v[70:71]\n"
    "  v_cvt_pk_bf16_f32 v64, v64, v65\n"
    "  v_cvt_pk_bf16_f32 v65, v66, v67\n"
    "  buffer_store_dwordx2 v[64:65], v221, s[52:55], s77 offen\n"
    "  v_add3_u32 v42, v24, v25, s24\n"
    "  v_add3_u32 v42, v26, v27, v42\n"
    "  v_add3_u32 v42, v28, v29, v42\n"
    "  v_add3_u32 v42, v30, v31, v42\n"
    "  v_add3_u32 v43, v32, v33, s24\n"
    "  v_add3_u32 v43, v34, v35, v43\n"
    "  v_add3_u32 v43, v36, v37, v43\n"
    "  v_add3_u32 v43, v38, v39, v43\n"
    "  s_waitcnt vmcnt(1)\n"
    "  v_xor_b32 v42, v42, v40\n"
    "  v_xor_b32 v43, v43, v41\n"
    "  v_or_b32 v42, v42, v43\n"
    "  v_cmp_ne_u32 vcc, 0, v42\n"
    "  s_cbranch_vccz .Lgqa_rg\n"
    "  s_sub_u32 s59, s59, 1\n"
    "  s_waitcnt vmcnt(0)\n"
    "  s_sleep 4\n"
    "  s_cbranch_scc0 .Lgqa_rl\n"
    ".Lgqa_rg:\n"
    "  s_endpgm\n"
    ".Lgqa_g1:\n"
    "  s_lshr_b32 s56, s23, 1\n"
    "  s_and_b32 s57, s23, 1\n"
    "  s_lshl_b32 s62, s20, 7\n"
    "  s_add_u32 s62, s62, 65536\n"
    "  v_and_b32 v24, 31, v0\n"
    "  v_lshrrev_b32 v25, 5, v0\n"
    "  v_lshrrev_b32 v26, 2, v4\n"
    "  v_and_b32 v27, 1, v1\n"
    "  v_bfe_u32 v28, v0, 1, 1\n"
    "  v_and_b32 v29, 1, v0\n"
    "  s_waitcnt lgkmcnt(0)\n"
    "  s_load_dword s24, s[16:17], s62\n"
    "  s_cmp_lg_u32 s23, 0\n"
    "  s_cbranch_scc1 .Lgqa_nowarm\n"
    ".Lgqa_nowarm:\n"
    "  s_mov_b32 s28, s16\n"
    "  s_and_b32 s29, s17, 0xffff\n"
    "  s_mov_b32 s30, 11993088\n"
    "  s_mov_b32 s31, 0x20000\n"
    "  s_mov_b32 s52, s14\n"
    "  s_and_b32 s53, s15, 0xffff\n"
    "  s_mov_b32 s54, 589824\n"
    "  s_mov_b32 s55, 0x20000\n"
    "  s_mul_i32 s62, s18, 589824\n"
    "  s_add_u32 s48, s12, s62\n"
    "  s_addc_u32 s49, s13, 0\n"
    "  s_and_b32 s49, s49, 0xffff\n"
    "  s_mov_b32 s50, 589824\n"
    "  s_mov_b32 s51, 0x20000\n"
    "  s_mov_b32 s60, 0x3e0293ee\n"
    "  s_mov_b32 s61, 0x3e0293ee\n"
    "  s_lshl_b32 s62, s23, 2\n"
    "  v_lshl_add_u32 v30, v27, 1, v28\n"
    "  v_add_u32 v31, s62, v30\n"
    "  v_lshlrev_b32 v32, 2, v26\n"
    "  v_xor_b32 v31, v31, v32\n"
    "  v_lshlrev_b32 v31, 4, v31\n"
    "  v_lshl_add_u32 v30, v29, 3, v31\n"
    "  v_lshl_add_u32 v31, v25, 3, v26\n"
    "  v_lshl_add_u32 v2, v31, 8, v30\n"
    "  v_add_u32 v2, 8192, v2\n"
    "  v_xor_b32 v33, v4, v25\n"
    "  v_bfe_u32 v32, v0, 1, 3\n"
    "  v_xor_b32 v32, v32, v25\n"
    "  v_lshlrev_b32 v32, 4, v32\n"
    "  v_lshl_add_u32 v3, v24, 7, v32\n"
    "  v_xor_b32 v4, 32, v3\n"
    "  v_xor_b32 v5, 64, v3\n"
    "  v_xor_b32 v6, 96, v3\n"
    "  v_lshlrev_b32 v32, 4, v33\n"
    "  v_lshl_add_u32 v32, v24, 8, v32\n"
    "  s_lshl_b32 s62, s56, 13\n"
    "  s_add_u32 s62, s62, 98304\n"
    "  v_add_u32 v7, s62, v32\n"
    "  v_xor_b32 v8, 32, v7\n"
    "  v_xor_b32 v9, 64, v7\n"
    "  v_xor_b32 v10, 96, v7\n"
    "  v_xor_b32 v11, 128, v7\n"
    "  v_xor_b32 v12, 160, v7\n"
    "  v_xor_b32 v13, 192, v7\n"
    "  v_xor_b32 v14, 224, v7\n"
    "  s_lshl_b32 s62, s57, 10\n"
    "  s_add_u32 s58, s62, 81920\n"
    "  v_lshl_add_u32 v15, v0, 4, s58\n"
    "  s_lshl_b32 s58, s56, 12\n"
    "  s_add_u32 s62, s62, s58\n"
    "  s_add_u32 s62, s62, 65536\n"
    "  v_lshl_add_u32 v16, v0, 4, s62\n"
    "  s_add_u32 s62, s63, 81920\n"
    "  v_lshlrev_b32 v32, 4, v24\n"
    "  v_lshl_add_u32 v32, v25, 3, v32\n"
    "  v_add_u32 v18, s62, v32\n"
    "  v_lshlrev_b32 v32, 3, v24\n"
    "  v_add_u32 v17, 65536, v32\n"
    "  s_add_u32 s62, s21, 1\n"
    "  s_and_b32 s62, s62, 3\n"
    "  s_lshl2_add_u32 s62, s20, s62\n"
    "  s_lshl3_add_u32 s62, s62, s18\n"
    "  s_mul_i32 s70, s62, 46080\n"
    "  s_add_u32 s70, s70, 196608\n"
    "  s_lshl_b32 s62, s62, 8\n"
    "  s_add_u32 s74, s16, s62\n"
    "  s_addc_u32 s75, s17, 0\n"
    "  s_add_u32 s62, s21, 2\n"
    "  s_and_b32 s62, s62, 3\n"
    "  s_lshl2_add_u32 s62, s20, s62\n"
    "  s_lshl3_add_u32 s62, s62, s18\n"
    "  s_mul_i32 s71, s62, 46080\n"
    "  s_add_u32 s71, s71, 196608\n"
    "  s_lshl_b32 s62, s62, 8\n"
    "  s_add_u32 s76, s16, s62\n"
    "  s_addc_u32 s77, s17, 0\n"
    "  s_add_u32 s62, s21, 3\n"
    "  s_and_b32 s62, s62, 3\n"
    "  s_lshl2_add_u32 s62, s20, s62\n"
    "  s_lshl3_add_u32 s62, s62, s18\n"
    "  s_mul_i32 s72, s62, 46080\n"
    "  s_add_u32 s72, s72, 196608\n"
    "  s_lshl_b32 s62, s62, 8\n"
    "  s_add_u32 s78, s16, s62\n"
    "  s_addc_u32 s79, s17, 0\n"
    "  s_lshl_b32 s62, %1, 8\n"
    "  v_lshl_add_u32 v30, v0, 3, s62\n"
    "  s_cmp_eq_u32 s23, 0\n"
    "  s_cselect_b32 s62, 32, 0\n"
    "  v_cmp_gt_u32 vcc, s62, v0\n"
    "  s_nop 1\n"
    "  v_cndmask_b32 v30, 2.0, v30, vcc\n"
    "  s_mov_b32 s59, 0\n"
    "  s_waitcnt lgkmcnt(0)\n"
    "  v_mov_b32 v32, s24\n"
    "  v_mov_b32 v33, s24\n"
    "  s_add_u32 s24, s24, 2654435769\n"
    "  buffer_store_dwordx2 v[32:33], v30, s[28:31], 0 offen sc1\n"
    "  s_waitcnt lgkmcnt(0)\n"
    "  s_barrier\n"
    "  ds_read_b64_tr_b16 v[160:161], v2 offset:0\n"
    "  ds_read_b64_tr_b16 v[162:163], v2 offset:1024\n"
    "  ds_read_b128 v[164:167], v3 offset:0\n"
    "  ds_read_b128 v[168:171], v3 offset:4096\n"
    "  ds_read_b64_tr_b16 v[172:173], v2 offset:4096\n"
    "  ds_read_b64_tr_b16 v[174:175], v2 offset:5120\n"
    "  ds_read_b128 v[176:179], v4 offset:0\n"
    "  ds_read_b128 v[180:183], v4 offset:4096\n"
    "  ds_read_b64_tr_b16 v[184:185], v2 offset:8192\n"
    "  ds_read_b64_tr_b16 v[186:187], v2 offset:9216\n"
    "  ds_read_b128 v[188:191], v5 offset:0\n"
    "  ds_read_b128 v[192:195], v5 offset:4096\n"
    "  ds_read_b64_tr_b16 v[196:197], v2 offset:12288\n"
    "  ds_read_b64_tr_b16 v[198:199], v2 offset:13312\n"
    "  ds_read_b128 v[200:203], v6 offset:0\n"
    "  ds_read_b128 v[204:207], v6 offset:4096\n"
    "  v_and_b32 v64, 15, v0\n"
    "  v_lshrrev_b32 v65, 3, v0\n"
    "  v_and_b32 v66, 7, v0\n"
    "  v_lshlrev_b32 v67, 6, v1\n"
    "  v_lshlrev_b32 v68, 4, v64\n"
    "  v_xor_b32 v68, v68, v67\n"
    "  s_lshl_b32 s58, s23, 4\n"
    "  s_movk_i32 s64, 1152\n"
    "  v_add_u32 v69, s58, v1\n"
    "  s_lshl_b32 s62, s21, 8\n"
    "  v_add_u32 v68, s62, v68\n"
    "  v_mad_u32_u24 v20, v69, s64, v68\n"
    "  s_lshl_b32 s62, s21, 7\n"
    "  s_lshl1_add_u32 s62, s58, s62\n"
    "  v_add_u32 v69, s62, v65\n"
    "  v_and_b32 v67, 1, v1\n"
    "  v_lshlrev_b32 v67, 2, v67\n"
    "  v_xor_b32 v67, v67, v66\n"
    "  v_lshlrev_b32 v67, 4, v67\n"
    "  v_add_u32 v67, 1024, v67\n"
    "  v_mad_u32_u24 v28, v69, s64, v67\n"
    "  v_add_u32 v21, 3584, v20\n"
    "  v_add_u32 v29, 8192, v28\n"
    "  v_add_u32 v22, 7168, v20\n"
    "  v_add_u32 v30, 16384, v28\n"
    "  v_add_u32 v23, 10752, v20\n"
    "  v_add_u32 v31, 24576, v28\n"
    "  s_waitcnt lgkmcnt(0)\n"
    "  s_barrier\n"
    "  v_mfma_f32_32x32x16_bf16 v[128:143], v[160:163], v[164:167], 0\n"
    "  ds_read_b64_tr_b16 v[208:209], v2 offset:24576\n"
    "  ds_read_b64_tr_b16 v[210:211], v2 offset:25600\n"
    "  ds_read_b128 v[212:215], v3 offset:24576\n"
    "  ds_read_b128 v[216:219], v3 offset:28672\n"
    "  v_mfma_f32_32x32x16_bf16 v[144:159], v[160:163], v[168:171], 0\n"
    "  ds_read_b64_tr_b16 v[220:221], v2 offset:28672\n"
    "  ds_read_b64_tr_b16 v[222:223], v2 offset:29696\n"
    "  ds_read_b128 v[224:227], v4 offset:24576\n"
    "  ds_read_b128 v[228:231], v4 offset:28672\n"
    "  v_mfma_f32_32x32x16_bf16 v[128:143], v[172:175], v[176:179], v[128:143]\n"
    "  ds_read_b64_tr_b16 v[232:233], v2 offset:32768\n"
    "  ds_read_b64_tr_b16 v[234:235], v2 offset:33792\n"
    "  ds_read_b128 v[236:239], v5 offset:24576\n"
    "  ds_read_b128 v[240:243], v5 offset:28672\n"
    "  v_mfma_f32_32x32x16_bf16 v[144:159], v[172:175], v[180:183], v[144:159]\n"
    "  ds_read_b64_tr_b16 v[244:245], v2 offset:36864\n"
    "  ds_read_b64_tr_b16 v[246:247], v2 offset:37888\n"
    "  ds_read_b128 v[248:251], v6 offset:24576\n"
    "  ds_read_b128 v[252:255], v6 offset:28672\n"
    "  v_mfma_f32_32x32x16_bf16 v[128:143], v[184:187], v[188:191], v[128:143]\n"
    "  v_mfma_f32_32x32x16_bf16 v[144:159], v[184:187], v[192:195], v[144:159]\n"
    "  v_mfma_f32_32x32x16_bf16 v[128:143], v[196:199], v[200:203], v[128:143]\n"
    "  v_mfma_f32_32x32x16_bf16 v[144:159], v[196:199], v[204:207], v[144:159]\n"
    "  s_waitcnt lgkmcnt(0)\n"
    "  s_barrier\n"
    "  v_mfma_f32_32x32x16_bf16 v[128:143], v[208:211], v[212:215], v[128:143]\n"
    "  ds_read_b64_tr_b16 v[160:161], v2 offset:49152\n"
    "  ds_read_b64_tr_b16 v[162:163], v2 offset:50176\n"
    "  ds_read_b128 v[164:167], v3 offset:49152\n"
    "  ds_read_b128 v[168:171], v3 offset:53248\n"
    "  v_mfma_f32_32x32x16_bf16 v[144:159], v[208:211], v[216:219], v[144:159]\n"
    "  ds_read_b64_tr_b16 v[172:173], v2 offset:53248\n"
    "  ds_read_b64_tr_b16 v[174:175], v2 offset:54272\n"
    "  ds_read_b128 v[176:179], v4 offset:49152\n"
    "  ds_read_b128 v[180:183], v4 offset:53248\n"
    "  v_mfma_f32_32x32x16_bf16 v[128:143], v[220:223], v[224:227], v[128:143]\n"
    "  ds_read_b64_tr_b16 v[184:185], v2 offset:57344\n"
    "  ds_read_b64_tr_b16 v[186:187], v2 offset:58368\n"
    "  ds_read_b128 v[188:191], v5 offset:49152\n"
    "  ds_read_b128 v[192:195], v5 offset:53248\n"
    "  v_mfma_f32_32x32x16_bf16 v[144:159], v[220:223], v[228:231], v[144:159]\n"
    "  ds_read_b64_tr_b16 v[196:197], v2 offset:61440\n"
    "  ds_read_b64_tr_b16 v[198:199], v2 offset:62464\n"
    "  ds_read_b128 v[200:203], v6 offset:49152\n"
    "  ds_read_b128 v[204:207], v6 offset:53248\n"
    "  v_mfma_f32_32x32x16_bf16 v[128:143], v[232:235], v[236:239], v[128:143]\n"
    "  v_mfma_f32_32x32x16_bf16 v[144:159], v[232:235], v[240:243], v[144:159]\n"
    "  v_mfma_f32_32x32x16_bf16 v[128:143], v[244:247], v[248:251], v[128:143]\n"
    "  v_mfma_f32_32x32x16_bf16 v[144:159], v[244:247], v[252:255], v[144:159]\n"
    "  s_waitcnt lgkmcnt(0)\n"
    "  s_barrier\n"
    "  v_mfma_f32_32x32x16_bf16 v[128:143], v[160:163], v[164:167], v[128:143]\n"
    "  ds_read_b64_tr_b16 v[208:209], v2 offset:0\n"
    "  ds_read_b64_tr_b16 v[210:211], v2 offset:1024\n"
    "  ds_read_b128 v[212:215], v3 offset:0\n"
    "  ds_read_b128 v[216:219], v3 offset:4096\n"
    "  v_mfma_f32_32x32x16_bf16 v[144:159], v[160:163], v[168:171], v[144:159]\n"
    "  ds_read_b64_tr_b16 v[220:221], v2 offset:4096\n"
    "  ds_read_b64_tr_b16 v[222:223], v2 offset:5120\n"
    "  ds_read_b128 v[224:227], v4 offset:0\n"
    "  ds_read_b128 v[228:231], v4 offset:4096\n"
    "  v_mfma_f32_32x32x16_bf16 v[128:143], v[172:175], v[176:179], v[128:143]\n"
    "  ds_read_b64_tr_b16 v[232:233], v2 offset:8192\n"
    "  ds_read_b64_tr_b16 v[234:235], v2 offset:9216\n"
    "  ds_read_b128 v[236:239], v5 offset:0\n"
    "  ds_read_b128 v[240:243], v5 offset:4096\n"
    "  v_mfma_f32_32x32x16_bf16 v[144:159], v[172:175], v[180:183], v[144:159]\n"
    "  ds_read_b64_tr_b16 v[244:245], v2 offset:12288\n"
    "  ds_read_b64_tr_b16 v[246:247], v2 offset:13312\n"
    "  ds_read_b128 v[248:251], v6 offset:0\n"
    "  ds_read_b128 v[252:255], v6 offset:4096\n"
    "  v_mfma_f32_32x32x16_bf16 v[128:143], v[184:187], v[188:191], v[128:143]\n"
    "  v_mfma_f32_32x32x16_bf16 v[144:159], v[184:187], v[192:195], v[144:159]\n"
    "  v_mfma_f32_32x32x16_bf16 v[128:143], v[196:199], v[200:203], v[128:143]\n"
    "  v_mfma_f32_32x32x16_bf16 v[144:159], v[196:199], v[204:207], v[144:159]\n"
    "  s_waitcnt lgkmcnt(0)\n"
    "  s_barrier\n"
    "  v_mfma_f32_32x32x16_bf16 v[128:143], v[208:211], v[212:215], v[128:143]\n"
    "  ds_read_b64_tr_b16 v[160:161], v2 offset:24576\n"
    "  ds_read_b64_tr_b16 v[162:163], v2 offset:25600\n"
    "  ds_read_b128 v[164:167], v3 offset:24576\n"
    "  ds_read_b128 v[168:171], v3 offset:28672\n"
    "  v_mfma_f32_32x32x16_bf16 v[144:159], v[208:211], v[216:219], v[144:159]\n"
    "  ds_read_b64_tr_b16 v[172:173], v2 offset:28672\n"
    "  ds_read_b64_tr_b16 v[174:175], v2 offset:29696\n"
    "  ds_read_b128 v[176:179], v4 offset:24576\n"
    "  ds_read_b128 v[180:183], v4 offset:28672\n"
    "  v_mfma_f32_32x32x16_bf16 v[128:143], v[220:223], v[224:227], v[128:143]\n"
    "  ds_read_b64_tr_b16 v[184:185], v2 offset:32768\n"
    "  ds_read_b64_tr_b16 v[186:187], v2 offset:33792\n"
    "  ds_read_b128 v[188:191], v5 offset:24576\n"
    "  ds_read_b128 v[192:195], v5 offset:28672\n"
    "  v_mfma_f32_32x32x16_bf16 v[144:159], v[220:223], v[228:231], v[144:159]\n"
    "  ds_read_b64_tr_b16 v[196:197], v2 offset:36864\n"
    "  ds_read_b64_tr_b16 v[198:199], v2 offset:37888\n"
    "  ds_read_b128 v[200:203], v6 offset:24576\n"
    "  ds_read_b128 v[204:207], v6 offset:28672\n"
    "  v_mfma_f32_32x32x16_bf16 v[128:143], v[232:235], v[236:239], v[128:143]\n"
    "  v_mfma_f32_32x32x16_bf16 v[144:159], v[232:235], v[240:243], v[144:159]\n"
    "  v_mfma_f32_32x32x16_bf16 v[128:143], v[244:247], v[248:251], v[128:143]\n"
    "  v_mfma_f32_32x32x16_bf16 v[144:159], v[244:247], v[252:255], v[144:159]\n"
    "  s_waitcnt lgkmcnt(0)\n"
    "  s_barrier\n"
    "  v_mfma_f32_32x32x16_bf16 v[128:143], v[160:163], v[164:167], v[128:143]\n"
    "  ds_read_b64_tr_b16 v[208:209], v2 offset:49152\n"
    "  ds_read_b64_tr_b16 v[210:211], v2 offset:50176\n"
    "  ds_read_b128 v[212:215], v3 offset:49152\n"
    "  ds_read_b128 v[216:219], v3 offset:53248\n"
    "  v_mfma_f32_32x32x16_bf16 v[144:159], v[160:163], v[168:171], v[144:159]\n"
    "  ds_read_b64_tr_b16 v[220:221], v2 offset:53248\n"
    "  ds_read_b64_tr_b16 v[222:223], v2 offset:54272\n"
    "  ds_read_b128 v[224:227], v4 offset:49152\n"
    "  ds_read_b128 v[228:231], v4 offset:53248\n"
    "  v_mfma_f32_32x32x16_bf16 v[128:143], v[172:175], v[176:179], v[128:143]\n"
    "  ds_read_b64_tr_b16 v[232:233], v2 offset:57344\n"
    "  ds_read_b64_tr_b16 v[234:235], v2 offset:58368\n"
    "  ds_read_b128 v[236:239], v5 offset:49152\n"
    "  ds_read_b128 v[240:243], v5 offset:53248\n"
    "  v_mfma_f32_32x32x16_bf16 v[144:159], v[172:175], v[180:183], v[144:159]\n"
    "  ds_read_b64_tr_b16 v[244:245], v2 offset:61440\n"
    "  ds_read_b64_tr_b16 v[246:247], v2 offset:62464\n"
    "  ds_read_b128 v[248:251], v6 offset:49152\n"
    "  ds_read_b128 v[252:255], v6 offset:53248\n"
    "  v_mfma_f32_32x32x16_bf16 v[128:143], v[184:187], v[188:191], v[128:143]\n"
    "  v_mfma_f32_32x32x16_bf16 v[144:159], v[184:187], v[192:195], v[144:159]\n"
    "  v_mfma_f32_32x32x16_bf16 v[128:143], v[196:199], v[200:203], v[128:143]\n"
    "  v_mfma_f32_32x32x16_bf16 v[144:159], v[196:199], v[204:207], v[144:159]\n"
    "  s_waitcnt lgkmcnt(0)\n"
    "  s_barrier\n"
    "  v_mfma_f32_32x32x16_bf16 v[128:143], v[208:211], v[212:215], v[128:143]\n"
    "  ds_read_b64_tr_b16 v[160:161], v2 offset:0\n"
    "  ds_read_b64_tr_b16 v[162:163], v2 offset:1024\n"
    "  ds_read_b128 v[164:167], v3 offset:0\n"
    "  ds_read_b128 v[168:171], v3 offset:4096\n"
    "  v_mfma_f32_32x32x16_bf16 v[144:159], v[208:211], v[216:219], v[144:159]\n"
    "  ds_read_b64_tr_b16 v[172:173], v2 offset:4096\n"
    "  ds_read_b64_tr_b16 v[174:175], v2 offset:5120\n"
    "  ds_read_b128 v[176:179], v4 offset:0\n"
    "  ds_read_b128 v[180:183], v4 offset:4096\n"
    "  v_mfma_f32_32x32x16_bf16 v[128:143], v[220:223], v[224:227], v[128:143]\n"
    "  ds_read_b64_tr_b16 v[184:185], v2 offset:8192\n"
    "  ds_read_b64_tr_b16 v[186:187], v2 offset:9216\n"
    "  ds_read_b128 v[188:191], v5 offset:0\n"
    "  ds_read_b128 v[192:195], v5 offset:4096\n"
    "  v_mfma_f32_32x32x16_bf16 v[144:159], v[220:223], v[228:231], v[144:159]\n"
    "  ds_read_b64_tr_b16 v[196:197], v2 offset:12288\n"
    "  ds_read_b64_tr_b16 v[198:199], v2 offset:13312\n"
    "  ds_read_b128 v[200:203], v6 offset:0\n"
    "  ds_read_b128 v[204:207], v6 offset:4096\n"
    "  v_mfma_f32_32x32x16_bf16 v[128:143], v[232:235], v[236:239], v[128:143]\n"
    "  v_mfma_f32_32x32x16_bf16 v[144:159], v[232:235], v[240:243], v[144:159]\n"
    "  v_mfma_f32_32x32x16_bf16 v[128:143], v[244:247], v[248:251], v[128:143]\n"
    "  v_mfma_f32_32x32x16_bf16 v[144:159], v[244:247], v[252:255], v[144:159]\n"
    "  s_waitcnt lgkmcnt(0)\n"
    "  s_barrier\n"
    "  v_mfma_f32_32x32x16_bf16 v[128:143], v[160:163], v[164:167], v[128:143]\n"
    "  ds_read_b64_tr_b16 v[208:209], v2 offset:24576\n"
    "  ds_read_b64_tr_b16 v[210:211], v2 offset:25600\n"
    "  ds_read_b128 v[212:215], v3 offset:24576\n"
    "  ds_read_b128 v[216:219], v3 offset:28672\n"
    "  v_mfma_f32_32x32x16_bf16 v[144:159], v[160:163], v[168:171], v[144:159]\n"
    "  ds_read_b64_tr_b16 v[220:221], v2 offset:28672\n"
    "  ds_read_b64_tr_b16 v[222:223], v2 offset:29696\n"
    "  ds_read_b128 v[224:227], v4 offset:24576\n"
    "  ds_read_b128 v[228:231], v4 offset:28672\n"
    "  v_mfma_f32_32x32x16_bf16 v[128:143], v[172:175], v[176:179], v[128:143]\n"
    "  ds_read_b64_tr_b16 v[232:233], v2 offset:32768\n"
    "  ds_read_b64_tr_b16 v[234:235], v2 offset:33792\n"
    "  ds_read_b128 v[236:239], v5 offset:24576\n"
    "  ds_read_b128 v[240:243], v5 offset:28672\n"
    "  v_mfma_f32_32x32x16_bf16 v[144:159], v[172:175], v[180:183], v[144:159]\n"
    "  ds_read_b64_tr_b16 v[244:245], v2 offset:36864\n"
    "  ds_read_b64_tr_b16 v[246:247], v2 offset:37888\n"
    "  ds_read_b128 v[248:251], v6 offset:24576\n"
    "  ds_read_b128 v[252:255], v6 offset:28672\n"
    "  v_mfma_f32_32x32x16_bf16 v[128:143], v[184:187], v[188:191], v[128:143]\n"
    "  v_mfma_f32_32x32x16_bf16 v[144:159], v[184:187], v[192:195], v[144:159]\n"
    "  v_mfma_f32_32x32x16_bf16 v[128:143], v[196:199], v[200:203], v[128:143]\n"
    "  v_mfma_f32_32x32x16_bf16 v[144:159], v[196:199], v[204:207], v[144:159]\n"
    "  s_waitcnt lgkmcnt(0)\n"
    "  s_barrier\n"
    "  v_mfma_f32_32x32x16_bf16 v[128:143], v[208:211], v[212:215], v[128:143]\n"
    "  ds_read_b64_tr_b16 v[160:161], v2 offset:49152\n"
    "  ds_read_b64_tr_b16 v[162:163], v2 offset:50176\n"
    "  ds_read_b128 v[164:167], v3 offset:49152\n"
    "  ds_read_b128 v[168:171], v3 offset:53248\n"
    "  v_mfma_f32_32x32x16_bf16 v[144:159], v[208:211], v[216:219], v[144:159]\n"
    "  ds_read_b64_tr_b16 v[172:173], v2 offset:53248\n"
    "  ds_read_b64_tr_b16 v[174:175], v2 offset:54272\n"
    "  ds_read_b128 v[176:179], v4 offset:49152\n"
    "  ds_read_b128 v[180:183], v4 offset:53248\n"
    "  v_mfma_f32_32x32x16_bf16 v[128:143], v[220:223], v[224:227], v[128:143]\n"
    "  ds_read_b64_tr_b16 v[184:185], v2 offset:57344\n"
    "  ds_read_b64_tr_b16 v[186:187], v2 offset:58368\n"
    "  ds_read_b128 v[188:191], v5 offset:49152\n"
    "  ds_read_b128 v[192:195], v5 offset:53248\n"
    "  v_mfma_f32_32x32x16_bf16 v[144:159], v[220:223], v[228:231], v[144:159]\n"
    "  ds_read_b64_tr_b16 v[196:197], v2 offset:61440\n"
    "  ds_read_b64_tr_b16 v[198:199], v2 offset:62464\n"
    "  ds_read_b128 v[200:203], v6 offset:49152\n"
    "  ds_read_b128 v[204:207], v6 offset:53248\n"
    "  v_mfma_f32_32x32x16_bf16 v[128:143], v[232:235], v[236:239], v[128:143]\n"
    "  v_mfma_f32_32x32x16_bf16 v[144:159], v[232:235], v[240:243], v[144:159]\n"
    "  v_mfma_f32_32x32x16_bf16 v[128:143], v[244:247], v[248:251], v[128:143]\n"
    "  v_mfma_f32_32x32x16_bf16 v[144:159], v[244:247], v[252:255], v[144:159]\n"
    "  s_waitcnt lgkmcnt(13)\n"
    "  v_mfma_f32_32x32x16_bf16 v[128:143], v[160:163], v[164:167], v[128:143]\n"
    "  s_waitcnt lgkmcnt(9)\n"
    "  v_mfma_f32_32x32x16_bf16 v[128:143], v[172:175], v[176:179], v[128:143]\n"
    "  s_waitcnt lgkmcnt(5)\n"
    "  v_mfma_f32_32x32x16_bf16 v[128:143], v[184:187], v[188:191], v[128:143]\n"
    "  s_waitcnt lgkmcnt(1)\n"
    "  v_mfma_f32_32x32x16_bf16 v[128:143], v[196:199], v[200:203], v[128:143]\n"
    "  v_mfma_f32_32x32x16_bf16 v[144:159], v[160:163], v[168:171], v[144:159]\n"
    "  v_mfma_f32_32x32x16_bf16 v[144:159], v[172:175], v[180:183], v[144:159]\n"
    "  s_waitcnt vmcnt(0)\n"
    "  s_nop 8\n"
    "  v_mul_f32 v128, s60, v128\n"
    "  v_mul_f32 v129, s60, v129\n"
    "  v_mul_f32 v130, s60, v130\n"
    "  v_mul_f32 v131, s60, v131\n"
    "  v_mul_f32 v132, s60, v132\n"
    "  v_mul_f32 v133, s60, v133\n"
    "  v_mul_f32 v134, s60, v134\n"
    "  v_mul_f32 v135, s60, v135\n"
    "  v_mfma_f32_32x32x16_bf16 v[144:159], v[184:187], v[192:195], v[144:159]\n"
    "  v_mul_f32 v136, s60, v136\n"
    "  v_mul_f32 v137, s60, v137\n"
    "  v_mul_f32 v138, s60, v138\n"
    "  v_mul_f32 v139, s60, v139\n"
    "  v_mul_f32 v140, s60, v140\n"
    "  v_mul_f32 v141, s60, v141\n"
    "  v_mul_f32 v142, s60, v142\n"
    "  v_mul_f32 v143, s60, v143\n"
    "  s_waitcnt lgkmcnt(0)\n"
    "  v_mfma_f32_32x32x16_bf16 v[144:159], v[196:199], v[204:207], v[144:159]\n"
    "  v_cvt_pk_bf16_f32 v48, v128, v129\n"
    "  v_cvt_pk_bf16_f32 v49, v130, v131\n"
    "  v_cvt_pk_bf16_f32 v50, v132, v133\n"
    "  v_cvt_pk_bf16_f32 v51, v134, v135\n"
    "  ds_write2st64_b64 v18, v[48:49], v[50:51] offset0:0 offset1:1\n"
    "  v_cvt_pk_bf16_f32 v52, v136, v137\n"
    "  v_cvt_pk_bf16_f32 v53, v138, v139\n"
    "  v_cvt_pk_bf16_f32 v54, v140, v141\n"
    "  v_cvt_pk_bf16_f32 v55, v142, v143\n"
    "  ds_write2st64_b64 v18, v[52:53], v[54:55] offset0:4 offset1:5\n"
    "  s_nop 1\n"
    "  v_pk_mul_f32 v[144:145], v[144:145], s[60:61]\n"
    "  v_pk_mul_f32 v[146:147], v[146:147], s[60:61]\n"
    "  v_pk_mul_f32 v[148:149], v[148:149], s[60:61]\n"
    "  v_pk_mul_f32 v[150:151], v[150:151], s[60:61]\n"
    "  v_pk_mul_f32 v[152:153], v[152:153], s[60:61]\n"
    "  v_pk_mul_f32 v[154:155], v[154:155], s[60:61]\n"
    "  v_pk_mul_f32 v[156:157], v[156:157], s[60:61]\n"
    "  v_pk_mul_f32 v[158:159], v[158:159], s[60:61]\n"
    "  v_cvt_pk_bf16_f32 v56, v144, v145\n"
    "  v_cvt_pk_bf16_f32 v57, v146, v147\n"
    "  v_cvt_pk_bf16_f32 v58, v148, v149\n"
    "  v_cvt_pk_bf16_f32 v59, v150, v151\n"
    "  ds_write2st64_b64 v18, v[56:57], v[58:59] offset0:2 offset1:3\n"
    "  v_cvt_pk_bf16_f32 v60, v152, v153\n"
    "  v_cvt_pk_bf16_f32 v61, v154, v155\n"
    "  v_cvt_pk_bf16_f32 v62, v156, v157\n"
    "  v_cvt_pk_bf16_f32 v63, v158, v159\n"
    "  ds_write2st64_b64 v18, v[60:61], v[62:63] offset0:6 offset1:7\n"
    "  s_lshl_b32 s62, s23, 8\n"
    "  v_add_u32 v2, s62, v17\n"
    "  s_xor_b32 s62, s62, 512\n"
    "  v_add_u32 v18, s62, v17\n"
    "  s_waitcnt lgkmcnt(0)\n"
    "  s_barrier\n"
    "  ds_read_b128 v[96:99], v15 offset:0\n"
    "  ds_read_b128 v[64:67], v7 offset:0\n"
    "  ds_read_b128 v[100:103], v15 offset:2048\n"
    "  ds_read_b128 v[68:71], v8 offset:0\n"
    "  ds_read_b128 v[104:107], v15 offset:4096\n"
    "  ds_read_b128 v[72:75], v9 offset:0\n"
    "  s_waitcnt lgkmcnt(4)\n"
    "  v_mfma_f32_32x32x16_bf16 v[128:143], v[64:67], v[96:99], 0\n"
    "  ds_read_b128 v[108:111], v15 offset:6144\n"
    "  ds_read_b128 v[76:79], v10 offset:0\n"
    "  s_waitcnt lgkmcnt(4)\n"
    "  v_mfma_f32_32x32x16_bf16 v[128:143], v[68:71], v[100:103], v[128:143]\n"
    "  ds_read_b128 v[112:115], v15 offset:8192\n"
    "  ds_read_b128 v[80:83], v11 offset:0\n"
    "  v_mov_b32 v24, 0\n"
    "  v_mov_b32 v26, 0\n"
    "  s_waitcnt lgkmcnt(4)\n"
    "  v_mfma_f32_32x32x16_bf16 v[128:143], v[72:75], v[104:107], v[128:143]\n"
    "  ds_read_b128 v[116:119], v15 offset:10240\n"
    "  ds_read_b128 v[84:87], v12 offset:0\n"
    "  s_waitcnt lgkmcnt(4)\n"
    "  v_mfma_f32_32x32x16_bf16 v[128:143], v[76:79], v[108:111], v[128:143]\n"
    "  ds_read_b128 v[120:123], v15 offset:12288\n"
    "  ds_read_b128 v[88:91], v13 offset:0\n"
    "  s_waitcnt lgkmcnt(4)\n"
    "  v_mfma_f32_32x32x16_bf16 v[128:143], v[80:83], v[112:115], v[128:143]\n"
    "  ds_read_b128 v[124:127], v15 offset:14336\n"
    "  ds_read_b128 v[92:95], v14 offset:0\n"
    "  s_waitcnt lgkmcnt(4)\n"
    "  v_mfma_f32_32x32x16_bf16 v[128:143], v[84:87], v[116:119], v[128:143]\n"
    "  s_waitcnt lgkmcnt(2)\n"
    "  v_mfma_f32_32x32x16_bf16 v[128:143], v[88:91], v[120:123], v[128:143]\n"
    "  s_waitcnt lgkmcnt(0)\n"
    "  v_mfma_f32_32x32x16_bf16 v[128:143], v[92:95], v[124:127], v[128:143]\n"
    "  s_waitcnt lgkmcnt(0)\n"
    "  s_barrier\n"
    "  ds_read_b128 v[64:67], v7 offset:16384\n"
    "  ds_read_b128 v[68:71], v8 offset:16384\n"
    "  ds_read_b128 v[72:75], v9 offset:16384\n"
    "  s_waitcnt lgkmcnt(2)\n"
    "  v_mfma_f32_32x32x16_bf16 v[144:159], v[64:67], v[96:99], 0\n"
    "  ds_read_b128 v[76:79], v10 offset:16384\n"
    "  s_waitcnt lgkmcnt(2)\n"
    "  v_mfma_f32_32x32x16_bf16 v[144:159], v[68:71], v[100:103], v[144:159]\n"
    "  ds_read_b128 v[80:83], v11 offset:16384\n"
    "  s_waitcnt lgkmcnt(2)\n"
    "  v_mfma_f32_32x32x16_bf16 v[144:159], v[72:75], v[104:107], v[144:159]\n"
    "  ds_read_b128 v[84:87], v12 offset:16384\n"
    "  v_maximum3_f32 v36, v128, v129, v128\n"
    "  v_maximum3_f32 v37, v130, v131, v130\n"
    "  v_maximum3_f32 v36, v132, v133, v36\n"
    "  v_maximum3_f32 v37, v134, v135, v37\n"
    "  s_waitcnt lgkmcnt(2)\n"
    "  v_mfma_f32_32x32x16_bf16 v[144:159], v[76:79], v[108:111], v[144:159]\n"
    "  ds_read_b128 v[88:91], v13 offset:16384\n"
    "  v_maximum3_f32 v36, v136, v137, v36\n"
    "  v_maximum3_f32 v37, v138, v139, v37\n"
    "  v_maximum3_f32 v36, v140, v141, v36\n"
    "  v_maximum3_f32 v37, v142, v143, v37\n"
    "  s_waitcnt lgkmcnt(2)\n"
    "  v_mfma_f32_32x32x16_bf16 v[144:159], v[80:83], v[112:115], v[144:159]\n"
    "  ds_read_b128 v[92:95], v14 offset:16384\n"
    "  v_maximum3_f32 v36, v36, v37, v37\n"
    "  v_mov_b32 v37, v36\n"
    "  s_nop 1\n"
    "  v_permlane32_swap_b32 v36, v37\n"
    "  v_maximum3_f32 v36, v36, v37, v37\n"
    "  s_waitcnt lgkmcnt(2)\n"
    "  v_mfma_f32_32x32x16_bf16 v[144:159], v[84:87], v[116:119], v[144:159]\n"
    "  ds_write_b32 v2, v36 offset:0\n"
    "  s_waitcnt lgkmcnt(2)\n"
    "  v_mfma_f32_32x32x16_bf16 v[144:159], v[88:91], v[120:123], v[144:159]\n"
    "  s_waitcnt lgkmcnt(1)\n"
    "  v_mfma_f32_32x32x16_bf16 v[144:159], v[92:95], v[124:127], v[144:159]\n"
    "  s_waitcnt lgkmcnt(0)\n"
    "  s_barrier\n"
    "  ds_read_b32 v37, v18 offset:0\n"
    "  ds_read_b128 v[64:67], v7 offset:32768\n"
    "  ds_read_b128 v[68:71], v8 offset:32768\n"
    "  ds_read_b128 v[72:75], v9 offset:32768\n"
    "  s_waitcnt lgkmcnt(2)\n"
    "  v_mfma_f32_32x32x16_bf16 v[160:175], v[64:67], v[96:99], 0\n"
    "  ds_read_b128 v[76:79], v10 offset:32768\n"
    "  s_waitcnt lgkmcnt(2)\n"
    "  v_mfma_f32_32x32x16_bf16 v[160:175], v[68:71], v[100:103], v[160:175]\n"
    "  ds_read_b128 v[80:83], v11 offset:32768\n"
    "  v_maximum3_f32 v36, v36, v37, v37\n"
    "  v_sub_f32 v48, 0, v36\n"
    "  v_sub_f32 v49, 0, v36\n"
    "  v_sub_f32 v50, 0, v36\n"
    "  v_sub_f32 v51, 0, v36\n"
    "  s_waitcnt lgkmcnt(2)\n"
    "  v_mfma_f32_32x32x16_bf16 v[160:175], v[72:75], v[104:107], v[160:175]\n"
    "  ds_read_b128 v[84:87], v12 offset:32768\n"
    "  v_sub_f32 v52, 0, v36\n"
    "  v_sub_f32 v53, 0, v36\n"
    "  v_sub_f32 v54, 0, v36\n"
    "  v_sub_f32 v55, 0, v36\n"
    "  v_sub_f32 v56, 0, v36\n"
    "  s_waitcnt lgkmcnt(2)\n"
    "  v_mfma_f32_32x32x16_bf16 v[160:175], v[76:79], v[108:111], v[160:175]\n"
    "  ds_read_b128 v[88:91], v13 offset:32768\n"
    "  v_sub_f32 v57, 0, v36\n"
    "  v_sub_f32 v58, 0, v36\n"
    "  v_sub_f32 v59, 0, v36\n"
    "  v_sub_f32 v60, 0, v36\n"
    "  v_sub_f32 v61, 0, v36\n"
    "  s_waitcnt lgkmcnt(2)\n"
    "  v_mfma_f32_32x32x16_bf16 v[160:175], v[80:83], v[112:115], v[160:175]\n"
    "  ds_read_b128 v[92:95], v14 offset:32768\n"
    "  v_sub_f32 v62, 0, v36\n"
    "  v_sub_f32 v63, 0, v36\n"
    "  v_add_f32 v128, v48, v128\n"
    "  v_add_f32 v129, v48, v129\n"
    "  v_add_f32 v130, v48, v130\n"
    "  s_waitcnt lgkmcnt(2)\n"
    "  v_mfma_f32_32x32x16_bf16 v[160:175], v[84:87], v[116:119], v[160:175]\n"
    "  v_add_f32 v131, v48, v131\n"
    "  v_add_f32 v132, v48, v132\n"
    "  v_add_f32 v133, v48, v133\n"
    "  v_add_f32 v134, v48, v134\n"
    "  v_add_f32 v135, v48, v135\n"
    "  v_add_f32 v136, v48, v136\n"
    "  s_waitcnt lgkmcnt(1)\n"
    "  v_mfma_f32_32x32x16_bf16 v[160:175], v[88:91], v[120:123], v[160:175]\n"
    "  v_add_f32 v137, v48, v137\n"
    "  v_add_f32 v138, v48, v138\n"
    "  v_add_f32 v139, v48, v139\n"
    "  v_add_f32 v140, v48, v140\n"
    "  v_add_f32 v141, v48, v141\n"
    "  v_add_f32 v142, v48, v142\n"
    "  s_waitcnt lgkmcnt(0)\n"
    "  v_mfma_f32_32x32x16_bf16 v[160:175], v[92:95], v[124:127], v[160:175]\n"
    "  v_add_f32 v143, v48, v143\n"
    "  s_waitcnt lgkmcnt(0)\n"
    "  s_barrier\n"
    "  ds_read_b128 v[64:67], v7 offset:49152\n"
    "  ds_read_b128 v[68:71], v8 offset:49152\n"
    "  ds_read_b128 v[72:75], v9 offset:49152\n"
    "  s_waitcnt lgkmcnt(2)\n"
    "  v_mfma_f32_32x32x16_bf16 v[176:191], v[64:67], v[96:99], v[48:63]\n"
    "  ds_read_b128 v[76:79], v10 offset:49152\n"
    "  s_waitcnt lgkmcnt(2)\n"
    "  v_mfma_f32_32x32x16_bf16 v[176:191], v[68:71], v[100:103], v[176:191]\n"
    "  ds_read_b128 v[80:83], v11 offset:49152\n"
    "  v_add_f32 v144, v48, v144\n"
    "  v_add_f32 v145, v48, v145\n"
    "  v_add_f32 v146, v48, v146\n"
    "  v_add_f32 v147, v48, v147\n"
    "  v_add_f32 v148, v48, v148\n"
    "  v_add_f32 v149, v48, v149\n"
    "  v_add_f32 v150, v48, v150\n"
    "  s_waitcnt lgkmcnt(2)\n"
    "  v_mfma_f32_32x32x16_bf16 v[176:191], v[72:75], v[104:107], v[176:191]\n"
    "  ds_read_b128 v[84:87], v12 offset:49152\n"
    "  v_add_f32 v151, v48, v151\n"
    "  v_add_f32 v152, v48, v152\n"
    "  v_add_f32 v153, v48, v153\n"
    "  v_add_f32 v154, v48, v154\n"
    "  v_add_f32 v155, v48, v155\n"
    "  v_add_f32 v156, v48, v156\n"
    "  v_add_f32 v157, v48, v157\n"
    "  s_waitcnt lgkmcnt(2)\n"
    "  v_mfma_f32_32x32x16_bf16 v[176:191], v[76:79], v[108:111], v[176:191]\n"
    "  ds_read_b128 v[88:91], v13 offset:49152\n"
    "  v_add_f32 v158, v48, v158\n"
    "  v_add_f32 v159, v48, v159\n"
    "  v_exp_f32 v128, v128\n"
    "  v_exp_f32 v129, v129\n"
    "  v_exp_f32 v130, v130\n"
    "  s_waitcnt lgkmcnt(2)\n"
    "  v_mfma_f32_32x32x16_bf16 v[176:191], v[80:83], v[112:115], v[176:191]\n"
    "  ds_read_b128 v[92:95], v14 offset:49152\n"
    "  v_exp_f32 v131, v131\n"
    "  v_exp_f32 v132, v132\n"
    "  v_exp_f32 v133, v133\n"
    "  v_exp_f32 v134, v134\n"
    "  s_waitcnt lgkmcnt(2)\n"
    "  v_mfma_f32_32x32x16_bf16 v[176:191], v[84:87], v[116:119], v[176:191]\n"
    "  v_exp_f32 v135, v135\n"
    "  v_maximum3_f32 v36, v144, v145, v144\n"
    "  v_maximum3_f32 v37, v146, v147, v146\n"
    "  v_maximum3_f32 v36, v148, v149, v36\n"
    "  v_maximum3_f32 v37, v150, v151, v37\n"
    "  v_maximum3_f32 v36, v152, v153, v36\n"
    "  v_maximum3_f32 v37, v154, v155, v37\n"
    "  s_waitcnt lgkmcnt(1)\n"
    "  v_mfma_f32_32x32x16_bf16 v[176:191], v[88:91], v[120:123], v[176:191]\n"
    "  v_maximum3_f32 v36, v156, v157, v36\n"
    "  v_maximum3_f32 v37, v158, v159, v37\n"
    "  v_subrev_u32 v7, 98304, v7\n"
    "  v_subrev_u32 v8, 98304, v8\n"
    "  v_subrev_u32 v9, 98304, v9\n"
    "  v_subrev_u32 v10, 98304, v10\n"
    "  v_subrev_u32 v11, 98304, v11\n"
    "  v_subrev_u32 v12, 98304, v12\n"
    "  s_waitcnt lgkmcnt(0)\n"
    "  v_mfma_f32_32x32x16_bf16 v[176:191], v[92:95], v[124:127], v[176:191]\n"
    "  v_subrev_u32 v13, 98304, v13\n"
    "  v_subrev_u32 v14, 98304, v14\n"
    "  s_waitcnt lgkmcnt(0)\n"
    "  s_barrier\n"
    "  ds_read_b128 v[64:67], v7 offset:0\n"
    "  ds_read_b128 v[68:71], v8 offset:0\n"
    "  ds_read_b128 v[72:75], v9 offset:0\n"
    "  s_waitcnt lgkmcnt(2)\n"
    "  v_mfma_f32_32x32x16_bf16 v[192:207], v[64:67], v[96:99], v[48:63]\n"
    "  ds_read_b128 v[76:79], v10 offset:0\n"
    "  s_waitcnt lgkmcnt(2)\n"
    "  v_mfma_f32_32x32x16_bf16 v[192:207], v[68:71], v[100:103], v[192:207]\n"
    "  ds_read_b128 v[80:83], v11 offset:0\n"
    "  v_add_f32 v160, v48, v160\n"
    "  v_add_f32 v161, v48, v161\n"
    "  v_add_f32 v162, v48, v162\n"
    "  v_add_f32 v163, v48, v163\n"
    "  v_add_f32 v164, v48, v164\n"
    "  v_add_f32 v165, v48, v165\n"
    "  v_add_f32 v166, v48, v166\n"
    "  s_waitcnt lgkmcnt(2)\n"
    "  v_mfma_f32_32x32x16_bf16 v[192:207], v[72:75], v[104:107], v[192:207]\n"
    "  ds_read_b128 v[84:87], v12 offset:0\n"
    "  v_add_f32 v167, v48, v167\n"
    "  v_add_f32 v168, v48, v168\n"
    "  v_add_f32 v169, v48, v169\n"
    "  v_add_f32 v170, v48, v170\n"
    "  v_add_f32 v171, v48, v171\n"
    "  v_add_f32 v172, v48, v172\n"
    "  v_add_f32 v173, v48, v173\n"
    "  s_waitcnt lgkmcnt(2)\n"
    "  v_mfma_f32_32x32x16_bf16 v[192:207], v[76:79], v[108:111], v[192:207]\n"
    "  ds_read_b128 v[88:91], v13 offset:0\n"
    "  v_add_f32 v174, v48, v174\n"
    "  v_add_f32 v175, v48, v175\n"
    "  v_maximum3_f32 v36, v176, v177, v36\n"
    "  v_maximum3_f32 v37, v178, v179, v37\n"
    "  v_maximum3_f32 v36, v180, v181, v36\n"
    "  v_maximum3_f32 v37, v182, v183, v37\n"
    "  v_maximum3_f32 v36, v184, v185, v36\n"
    "  s_waitcnt lgkmcnt(2)\n"
    "  v_mfma_f32_32x32x16_bf16 v[192:207], v[80:83], v[112:115], v[192:207]\n"
    "  ds_read_b128 v[92:95], v14 offset:0\n"
    "  v_maximum3_f32 v37, v186, v187, v37\n"
    "  v_maximum3_f32 v36, v188, v189, v36\n"
    "  v_maximum3_f32 v37, v190, v191, v37\n"
    "  v_maximum3_f32 v36, v160, v161, v36\n"
    "  v_maximum3_f32 v37, v162, v163, v37\n"
    "  v_maximum3_f32 v36, v164, v165, v36\n"
    "  v_maximum3_f32 v37, v166, v167, v37\n"
    "  s_waitcnt lgkmcnt(2)\n"
    "  v_mfma_f32_32x32x16_bf16 v[192:207], v[84:87], v[116:119], v[192:207]\n"
    "  v_maximum3_f32 v36, v168, v169, v36\n"
    "  v_maximum3_f32 v37, v170, v171, v37\n"
    "  v_maximum3_f32 v36, v172, v173, v36\n"
    "  v_maximum3_f32 v37, v174, v175, v37\n"
    "  v_maximum3_f32 v36, v36, v37, v37\n"
    "  v_cmp_lt_f32 vcc, 0x42000000, v36\n"
    "  v_exp_f32 v136, v136\n"
    "  v_exp_f32 v137, v137\n"
    "  s_waitcnt lgkmcnt(1)\n"
    "  v_mfma_f32_32x32x16_bf16 v[192:207], v[88:91], v[120:123], v[192:207]\n"
    "  v_exp_f32 v138, v138\n"
    "  v_exp_f32 v139, v139\n"
    "  v_exp_f32 v140, v140\n"
    "  v_exp_f32 v141, v141\n"
    "  v_exp_f32 v142, v142\n"
    "  s_waitcnt lgkmcnt(0)\n"
    "  v_mfma_f32_32x32x16_bf16 v[192:207], v[92:95], v[124:127], v[192:207]\n"
    "  v_exp_f32 v143, v143\n"
    "  s_waitcnt lgkmcnt(0)\n"
    "  s_barrier\n"
    "  ds_read_b128 v[64:67], v7 offset:16384\n"
    "  ds_read_b128 v[68:71], v8 offset:16384\n"
    "  ds_read_b128 v[72:75], v9 offset:16384\n"
    "  s_waitcnt lgkmcnt(2)\n"
    "  v_mfma_f32_32x32x16_bf16 v[208:223], v[64:67], v[96:99], v[48:63]\n"
    "  ds_read_b128 v[76:79], v10 offset:16384\n"
    "  s_waitcnt lgkmcnt(2)\n"
    "  v_mfma_f32_32x32x16_bf16 v[208:223], v[68:71], v[100:103], v[208:223]\n"
    "  ds_read_b128 v[80:83], v11 offset:16384\n"
    "  s_cbranch_vccnz .Lgqa_biga\n"
    ".Lgqa_oka:\n"
    "  v_exp_f32 v144, v144\n"
    "  v_exp_f32 v145, v145\n"
    "  v_exp_f32 v146, v146\n"
    "  v_exp_f32 v147, v147\n"
    "  s_waitcnt lgkmcnt(2)\n"
    "  v_mfma_f32_32x32x16_bf16 v[208:223], v[72:75], v[104:107], v[208:223]\n"
    "  ds_read_b128 v[84:87], v12 offset:16384\n"
    "  v_exp_f32 v148, v148\n"
    "  v_exp_f32 v149, v149\n"
    "  v_exp_f32 v150, v150\n"
    "  v_exp_f32 v151, v151\n"
    "  s_waitcnt lgkmcnt(2)\n"
    "  v_mfma_f32_32x32x16_bf16 v[208:223], v[76:79], v[108:111], v[208:223]\n"
    "  ds_read_b128 v[88:91], v13 offset:16384\n"
    "  v_exp_f32 v152, v152\n"
    "  v_exp_f32 v153, v153\n"
    "  v_exp_f32 v154, v154\n"
    "  v_exp_f32 v155, v155\n"
    "  s_waitcnt lgkmcnt(2)\n"
    "  v_mfma_f32_32x32x16_bf16 v[208:223], v[80:83], v[112:115], v[208:223]\n"
    "  ds_read_b128 v[92:95], v14 offset:16384\n"
    "  v_exp_f32 v156, v156\n"
    "  v_exp_f32 v157, v157\n"
    "  v_exp_f32 v158, v158\n"
    "  v_exp_f32 v159, v159\n"
    "  s_waitcnt lgkmcnt(2)\n"
    "  v_mfma_f32_32x32x16_bf16 v[208:223], v[84:87], v[116:119], v[208:223]\n"
    "  v_cvt_pk_bf16_f32 v40, v128, v129\n"
    "  v_cvt_pk_bf16_f32 v41, v130, v131\n"
    "  v_cvt_pk_bf16_f32 v42, v132, v133\n"
    "  v_cvt_pk_bf16_f32 v43, v134, v135\n"
    "  v_cvt_pk_bf16_f32 v44, v136, v137\n"
    "  v_cvt_pk_bf16_f32 v45, v138, v139\n"
    "  v_cvt_pk_bf16_f32 v46, v140, v141\n"
    "  v_cvt_pk_bf16_f32 v47, v142, v143\n"
    "  v_add_f32 v32, v128, v132\n"
    "  v_add_f32 v33, v129, v133\n"
    "  s_waitcnt lgkmcnt(1)\n"
    "  v_mfma_f32_32x32x16_bf16 v[208:223], v[88:91], v[120:123], v[208:223]\n"
    "  v_add_f32 v34, v130, v134\n"
    "  v_add_f32 v35, v131, v135\n"
    "  v_add_f32 v32, v32, v136\n"
    "  v_add_f32 v33, v33, v137\n"
    "  v_add_f32 v34, v34, v138\n"
    "  v_add_f32 v35, v35, v139\n"
    "  v_add_f32 v32, v32, v140\n"
    "  v_add_f32 v33, v33, v141\n"
    "  v_add_f32 v34, v34, v142\n"
    "  v_add_f32 v35, v35, v143\n"
    "  s_waitcnt lgkmcnt(0)\n"
    "  v_mfma_f32_32x32x16_bf16 v[208:223], v[92:95], v[124:127], v[208:223]\n"
    "  v_maximum3_f32 v36, v192, v193, v192\n"
    "  v_maximum3_f32 v37, v194, v195, v194\n"
    "  v_maximum3_f32 v36, v196, v197, v36\n"
    "  v_maximum3_f32 v37, v198, v199, v37\n"
    "  v_maximum3_f32 v36, v200, v201, v36\n"
    "  v_maximum3_f32 v37, v202, v203, v37\n"
    "  v_maximum3_f32 v36, v204, v205, v36\n"
    "  v_maximum3_f32 v37, v206, v207, v37\n"
    "  s_waitcnt lgkmcnt(0)\n"
    "  s_barrier\n"
    "  ds_read_b128 v[64:67], v7 offset:32768\n"
    "  ds_read_b128 v[68:71], v8 offset:32768\n"
    "  ds_read_b128 v[72:75], v9 offset:32768\n"
    "  s_waitcnt lgkmcnt(2)\n"
    "  v_mfma_f32_32x32x16_bf16 v[224:239], v[64:67], v[96:99], v[48:63]\n"
    "  ds_read_b128 v[76:79], v10 offset:32768\n"
    "  s_waitcnt lgkmcnt(2)\n"
    "  v_mfma_f32_32x32x16_bf16 v[224:239], v[68:71], v[100:103], v[224:239]\n"
    "  ds_read_b128 v[80:83], v11 offset:32768\n"
    "  v_exp_f32 v160, v160\n"
    "  v_exp_f32 v161, v161\n"
    "  v_exp_f32 v162, v162\n"
    "  v_exp_f32 v163, v163\n"
    "  s_waitcnt lgkmcnt(2)\n"
    "  v_mfma_f32_32x32x16_bf16 v[224:239], v[72:75], v[104:107], v[224:239]\n"
    "  ds_read_b128 v[84:87], v12 offset:32768\n"
    "  ds_write_b128 v16, v[40:43] offset:0\n"
    "  v_exp_f32 v164, v164\n"
    "  s_waitcnt lgkmcnt(3)\n"
    "  v_mfma_f32_32x32x16_bf16 v[224:239], v[76:79], v[108:111], v[224:239]\n"
    "  ds_read_b128 v[88:91], v13 offset:32768\n"
    "  v_exp_f32 v165, v165\n"
    "  v_exp_f32 v166, v166\n"
    "  v_exp_f32 v167, v167\n"
    "  ds_write_b128 v16, v[44:47] offset:2048\n"
    "  s_waitcnt lgkmcnt(4)\n"
    "  v_mfma_f32_32x32x16_bf16 v[224:239], v[80:83], v[112:115], v[224:239]\n"
    "  ds_read_b128 v[92:95], v14 offset:32768\n"
    "  v_exp_f32 v168, v168\n"
    "  v_exp_f32 v169, v169\n"
    "  v_exp_f32 v170, v170\n"
    "  v_exp_f32 v171, v171\n"
    "  s_waitcnt lgkmcnt(4)\n"
    "  v_mfma_f32_32x32x16_bf16 v[224:239], v[84:87], v[116:119], v[224:239]\n"
    "  v_exp_f32 v172, v172\n"
    "  v_exp_f32 v173, v173\n"
    "  v_exp_f32 v174, v174\n"
    "  v_exp_f32 v175, v175\n"
    "  v_cvt_pk_bf16_f32 v40, v144, v145\n"
    "  v_cvt_pk_bf16_f32 v41, v146, v147\n"
    "  s_waitcnt lgkmcnt(2)\n"
    "  v_mfma_f32_32x32x16_bf16 v[224:239], v[88:91], v[120:123], v[224:239]\n"
    "  v_cvt_pk_bf16_f32 v42, v148, v149\n"
    "  v_cvt_pk_bf16_f32 v43, v150, v151\n"
    "  v_cvt_pk_bf16_f32 v44, v152, v153\n"
    "  v_cvt_pk_bf16_f32 v45, v154, v155\n"
    "  v_cvt_pk_bf16_f32 v46, v156, v157\n"
    "  v_cvt_pk_bf16_f32 v47, v158, v159\n"
    "  v_maximum3_f32 v36, v208, v209, v36\n"
    "  v_maximum3_f32 v37, v210, v211, v37\n"
    "  v_maximum3_f32 v36, v212, v213, v36\n"
    "  v_maximum3_f32 v37, v214, v215, v37\n"
    "  s_waitcnt lgkmcnt(0)\n"
    "  v_mfma_f32_32x32x16_bf16 v[224:239], v[92:95], v[124:127], v[224:239]\n"
    "  v_maximum3_f32 v36, v216, v217, v36\n"
    "  v_maximum3_f32 v37, v218, v219, v37\n"
    "  v_maximum3_f32 v36, v220, v221, v36\n"
    "  v_maximum3_f32 v37, v222, v223, v37\n"
    "  s_waitcnt lgkmcnt(0)\n"
    "  s_barrier\n"
    "  ds_read_b128 v[64:67], v7 offset:49152\n"
    "  ds_read_b128 v[68:71], v8 offset:49152\n"
    "  ds_read_b128 v[72:75], v9 offset:49152\n"
    "  s_waitcnt lgkmcnt(2)\n"
    "  v_mfma_f32_32x32x16_bf16 v[240:255], v[64:67], v[96:99], v[48:63]\n"
    "  ds_read_b128 v[76:79], v10 offset:49152\n"
    "  s_waitcnt lgkmcnt(2)\n"
    "  v_mfma_f32_32x32x16_bf16 v[240:255], v[68:71], v[100:103], v[240:255]\n"
    "  ds_read_b128 v[80:83], v11 offset:49152\n"
    "  v_exp_f32 v176, v176\n"
    "  v_exp_f32 v177, v177\n"
    "  v_exp_f32 v178, v178\n"
    "  v_exp_f32 v179, v179\n"
    "  s_waitcnt lgkmcnt(2)\n"
    "  v_mfma_f32_32x32x16_bf16 v[240:255], v[72:75], v[104:107], v[240:255]\n"
    "  ds_read_b128 v[84:87], v12 offset:49152\n"
    "  ds_write_b128 v16, v[40:43] offset:8192\n"
    "  v_exp_f32 v180, v180\n"
    "  s_waitcnt lgkmcnt(3)\n"
    "  v_mfma_f32_32x32x16_bf16 v[240:255], v[76:79], v[108:111], v[240:255]\n"
    "  ds_read_b128 v[88:91], v13 offset:49152\n"
    "  v_exp_f32 v181, v181\n"
    "  v_exp_f32 v182, v182\n"
    "  v_exp_f32 v183, v183\n"
    "  ds_write_b128 v16, v[44:47] offset:10240\n"
    "  s_waitcnt lgkmcnt(4)\n"
    "  v_mfma_f32_32x32x16_bf16 v[240:255], v[80:83], v[112:115], v[240:255]\n"
    "  ds_read_b128 v[92:95], v14 offset:49152\n"
    "  v_exp_f32 v184, v184\n"
    "  v_exp_f32 v185, v185\n"
    "  v_exp_f32 v186, v186\n"
    "  v_exp_f32 v187, v187\n"
    "  s_waitcnt lgkmcnt(4)\n"
    "  v_mfma_f32_32x32x16_bf16 v[240:255], v[84:87], v[116:119], v[240:255]\n"
    "  v_exp_f32 v188, v188\n"
    "  v_exp_f32 v189, v189\n"
    "  v_exp_f32 v190, v190\n"
    "  v_exp_f32 v191, v191\n"
    "  v_add_f32 v32, v32, v144\n"
    "  v_add_f32 v33, v33, v145\n"
    "  s_waitcnt lgkmcnt(2)\n"
    "  v_mfma_f32_32x32x16_bf16 v[240:255], v[88:91], v[120:123], v[240:255]\n"
    "  v_add_f32 v34, v34, v146\n"
    "  v_add_f32 v35, v35, v147\n"
    "  v_add_f32 v32, v32, v148\n"
    "  v_add_f32 v33, v33, v149\n"
    "  v_add_f32 v34, v34, v150\n"
    "  v_add_f32 v35, v35, v151\n"
    "  v_maximum3_f32 v36, v224, v225, v36\n"
    "  v_maximum3_f32 v37, v226, v227, v37\n"
    "  v_maximum3_f32 v36, v228, v229, v36\n"
    "  v_maximum3_f32 v37, v230, v231, v37\n"
    "  s_waitcnt lgkmcnt(0)\n"
    "  v_mfma_f32_32x32x16_bf16 v[240:255], v[92:95], v[124:127], v[240:255]\n"
    "  v_maximum3_f32 v36, v232, v233, v36\n"
    "  v_maximum3_f32 v37, v234, v235, v37\n"
    "  v_maximum3_f32 v36, v236, v237, v36\n"
    "  v_maximum3_f32 v37, v238, v239, v37\n"
    "  s_waitcnt lgkmcnt(0)\n"
    "  s_barrier\n"
    "  v_add_f32 v32, v32, v152\n"
    "  v_add_f32 v33, v33, v153\n"
    "  v_add_f32 v34, v34, v154\n"
    "  v_add_f32 v35, v35, v155\n"
    "  v_add_f32 v32, v32, v156\n"
    "  v_add_f32 v33, v33, v157\n"
    "  v_add_f32 v34, v34, v158\n"
    "  v_add_f32 v35, v35, v159\n"
    "  v_maximum3_f32 v36, v240, v241, v36\n"
    "  v_maximum3_f32 v37, v242, v243, v37\n"
    "  v_maximum3_f32 v36, v244, v245, v36\n"
    "  v_maximum3_f32 v37, v246, v247, v37\n"
    "  v_maximum3_f32 v36, v248, v249, v36\n"
    "  v_maximum3_f32 v37, v250, v251, v37\n"
    "  v_maximum3_f32 v36, v252, v253, v36\n"
    "  v_maximum3_f32 v37, v254, v255, v37\n"
    "  v_maximum3_f32 v36, v36, v37, v37\n"
    "  v_cmp_lt_f32 vcc, 0x42000000, v36\n"
    "  v_add_f32 v32, v32, v160\n"
    "  v_add_f32 v33, v33, v161\n"
    "  v_add_f32 v34, v34, v162\n"
    "  v_add_f32 v35, v35, v163\n"
    "  v_add_f32 v32, v32, v164\n"
    "  v_add_f32 v33, v33, v165\n"
    "  v_add_f32 v34, v34, v166\n"
    "  v_add_f32 v35, v35, v167\n"
    "  v_add_f32 v32, v32, v168\n"
    "  v_add_f32 v33, v33, v169\n"
    "  v_add_f32 v34, v34, v170\n"
    "  v_add_f32 v35, v35, v171\n"
    "  v_add_f32 v32, v32, v172\n"
    "  v_add_f32 v33, v33, v173\n"
    "  v_add_f32 v34, v34, v174\n"
    "  v_add_f32 v35, v35, v175\n"
    "  s_cbranch_vccnz .Lgqa_bigb\n"
    ".Lgqa_okb:\n"
    "  v_mov_b32 v36, s59\n"
    "  ds_write_b32 v2, v36 offset:17408\n"
    "  v_exp_f32 v192, v192\n"
    "  v_exp_f32 v193, v193\n"
    "  v_exp_f32 v194, v194\n"
    "  v_exp_f32 v195, v195\n"
    "  v_exp_f32 v196, v196\n"
    "  v_exp_f32 v197, v197\n"
    "  v_exp_f32 v198, v198\n"
    "  v_exp_f32 v199, v199\n"
    "  v_exp_f32 v200, v200\n"
    "  v_exp_f32 v201, v201\n"
    "  v_exp_f32 v202, v202\n"
    "  v_exp_f32 v203, v203\n"
    "  v_exp_f32 v204, v204\n"
    "  v_exp_f32 v205, v205\n"
    "  v_exp_f32 v206, v206\n"
    "  v_exp_f32 v207, v207\n"
    "  s_waitcnt lgkmcnt(0)\n"
    "  s_barrier\n"
    "  ds_read_b32 v10, v17 offset:17408\n"
    "  ds_read_b32 v11, v17 offset:17664\n"
    "  ds_read_b32 v12, v17 offset:17920\n"
    "  ds_read_b32 v13, v17 offset:18176\n"
    "  v_add_f32 v32, v32, v176\n"
    "  v_add_f32 v33, v33, v177\n"
    "  v_add_f32 v34, v34, v178\n"
    "  v_add_f32 v35, v35, v179\n"
    "  v_add_f32 v32, v32, v180\n"
    "  v_add_f32 v33, v33, v181\n"
    "  v_add_f32 v34, v34, v182\n"
    "  v_add_f32 v35, v35, v183\n"
    "  v_add_f32 v32, v32, v184\n"
    "  v_add_f32 v33, v33, v185\n"
    "  v_add_f32 v34, v34, v186\n"
    "  v_add_f32 v35, v35, v187\n"
    "  v_add_f32 v32, v32, v188\n"
    "  v_add_f32 v33, v33, v189\n"
    "  v_add_f32 v34, v34, v190\n"
    "  v_add_f32 v35, v35, v191\n"
    "  s_waitcnt lgkmcnt(1)\n"
    "  v_or3_b32 v10, v10, v11, v12\n"
    "  s_waitcnt lgkmcnt(0)\n"
    "  v_or_b32 v10, v10, v13\n"
    "  v_cmp_ne_u32 vcc, 0, v10\n"
    "  v_add_f32 v32, v32, v192\n"
    "  v_add_f32 v33, v33, v193\n"
    "  v_add_f32 v34, v34, v194\n"
    "  v_add_f32 v35, v35, v195\n"
    "  v_add_f32 v32, v32, v196\n"
    "  v_add_f32 v33, v33, v197\n"
    "  v_add_f32 v34, v34, v198\n"
    "  v_add_f32 v35, v35, v199\n"
    "  s_cbranch_vccnz .Lgqa_fix1\n"
    ".Lgqa_fixed1:\n"
    "  v_cvt_pk_bf16_f32 v40, v160, v161\n"
    "  v_cvt_pk_bf16_f32 v41, v162, v163\n"
    "  v_cvt_pk_bf16_f32 v42, v164, v165\n"
    "  v_cvt_pk_bf16_f32 v43, v166, v167\n"
    "  ds_write_b128 v16, v[40:43] offset:0\n"
    "  v_cvt_pk_bf16_f32 v44, v168, v169\n"
    "  v_cvt_pk_bf16_f32 v45, v170, v171\n"
    "  v_cvt_pk_bf16_f32 v46, v172, v173\n"
    "  v_cvt_pk_bf16_f32 v47, v174, v175\n"
    "  ds_write_b128 v16, v[44:47] offset:2048\n"
    "  v_exp_f32 v208, v208\n"
    "  v_exp_f32 v209, v209\n"
    "  v_exp_f32 v210, v210\n"
    "  v_exp_f32 v211, v211\n"
    "  v_exp_f32 v212, v212\n"
    "  v_exp_f32 v213, v213\n"
    "  s_waitcnt lgkmcnt(0)\n"
    "  s_barrier\n"
    "  v_cvt_pk_bf16_f32 v40, v176, v177\n"
    "  v_cvt_pk_bf16_f32 v41, v178, v179\n"
    "  v_cvt_pk_bf16_f32 v42, v180, v181\n"
    "  v_cvt_pk_bf16_f32 v43, v182, v183\n"
    "  ds_write_b128 v16, v[40:43] offset:8192\n"
    "  v_exp_f32 v214, v214\n"
    "  v_exp_f32 v215, v215\n"
    "  v_exp_f32 v216, v216\n"
    "  v_exp_f32 v217, v217\n"
    "  v_cvt_pk_bf16_f32 v44, v184, v185\n"
    "  v_cvt_pk_bf16_f32 v45, v186, v187\n"
    "  v_cvt_pk_bf16_f32 v46, v188, v189\n"
    "  v_cvt_pk_bf16_f32 v47, v190, v191\n"
    "  ds_write_b128 v16, v[44:47] offset:10240\n"
    "  v_exp_f32 v218, v218\n"
    "  v_exp_f32 v219, v219\n"
    "  v_exp_f32 v220, v220\n"
    "  v_exp_f32 v221, v221\n"
    "  v_exp_f32 v222, v222\n"
    "  v_exp_f32 v223, v223\n"
    "  v_exp_f32 v224, v224\n"
    "  v_exp_f32 v225, v225\n"
    "  v_add_f32 v32, v32, v200\n"
    "  v_add_f32 v33, v33, v201\n"
    "  v_add_f32 v34, v34, v202\n"
    "  v_add_f32 v35, v35, v203\n"
    "  v_add_f32 v32, v32, v204\n"
    "  v_add_f32 v33, v33, v205\n"
    "  v_add_f32 v34, v34, v206\n"
    "  v_add_f32 v35, v35, v207\n"
    "  s_waitcnt lgkmcnt(0)\n"
    "  s_barrier\n"
    "  v_cvt_pk_bf16_f32 v40, v192, v193\n"
    "  v_cvt_pk_bf16_f32 v41, v194, v195\n"
    "  v_cvt_pk_bf16_f32 v42, v196, v197\n"
    "  v_cvt_pk_bf16_f32 v43, v198, v199\n"
    "  ds_write_b128 v16, v[40:43] offset:0\n"
    "  v_exp_f32 v226, v226\n"
    "  v_exp_f32 v227, v227\n"
    "  v_exp_f32 v228, v228\n"
    "  v_exp_f32 v229, v229\n"
    "  v_cvt_pk_bf16_f32 v44, v200, v201\n"
    "  v_cvt_pk_bf16_f32 v45, v202, v203\n"
    "  v_cvt_pk_bf16_f32 v46, v204, v205\n"
    "  v_cvt_pk_bf16_f32 v47, v206, v207\n"
    "  ds_write_b128 v16, v[44:47] offset:2048\n"
    "  v_exp_f32 v230, v230\n"
    "  v_exp_f32 v231, v231\n"
    "  v_exp_f32 v232, v232\n"
    "  v_exp_f32 v233, v233\n"
    "  v_exp_f32 v234, v234\n"
    "  v_exp_f32 v235, v235\n"
    "  v_exp_f32 v236, v236\n"
    "  v_exp_f32 v237, v237\n"
    "  v_add_f32 v32, v32, v208\n"
    "  v_add_f32 v33, v33, v209\n"
    "  v_add_f32 v34, v34, v210\n"
    "  v_add_f32 v35, v35, v211\n"
    "  v_add_f32 v32, v32, v212\n"
    "  v_add_f32 v33, v33, v213\n"
    "  v_add_f32 v34, v34, v214\n"
    "  v_add_f32 v35, v35, v215\n"
    "  s_waitcnt lgkmcnt(0)\n"
    "  s_barrier\n"
    "  v_cvt_pk_bf16_f32 v40, v208, v209\n"
    "  v_cvt_pk_bf16_f32 v41, v210, v211\n"
    "  v_cvt_pk_bf16_f32 v42, v212, v213\n"
    "  v_cvt_pk_bf16_f32 v43, v214, v215\n"
    "  ds_write_b128 v16, v[40:43] offset:8192\n"
    "  v_exp_f32 v238, v238\n"
    "  v_exp_f32 v239, v239\n"
    "  v_exp_f32 v240, v240\n"
    "  v_exp_f32 v241, v241\n"
    "  v_cvt_pk_bf16_f32 v44, v216, v217\n"
    "  v_cvt_pk_bf16_f32 v45, v218, v219\n"
    "  v_cvt_pk_bf16_f32 v46, v220, v221\n"
    "  v_cvt_pk_bf16_f32 v47, v222, v223\n"
    "  ds_write_b128 v16, v[44:47] offset:10240\n"
    "  v_exp_f32 v242, v242\n"
    "  v_exp_f32 v243, v243\n"
    "  v_exp_f32 v244, v244\n"
    "  v_exp_f32 v245, v245\n"
    "  v_exp_f32 v246, v246\n"
    "  v_exp_f32 v247, v247\n"
    "  v_add_f32 v32, v32, v216\n"
    "  v_add_f32 v33, v33, v217\n"
    "  v_add_f32 v34, v34, v218\n"
    "  v_add_f32 v35, v35, v219\n"
    "  v_add_f32 v32, v32, v220\n"
    "  v_add_f32 v33, v33, v221\n"
    "  v_add_f32 v34, v34, v222\n"
    "  v_add_f32 v35, v35, v223\n"
    "  s_waitcnt lgkmcnt(0)\n"
    "  s_barrier\n"
    "  v_cvt_pk_bf16_f32 v40, v224, v225\n"
    "  v_cvt_pk_bf16_f32 v41, v226, v227\n"
    "  v_cvt_pk_bf16_f32 v42, v228, v229\n"
    "  v_cvt_pk_bf16_f32 v43, v230, v231\n"
    "  ds_write_b128 v16, v[40:43] offset:0\n"
    "  v_exp_f32 v248, v248\n"
    "  v_exp_f32 v249, v249\n"
    "  v_exp_f32 v250, v250\n"
    "  v_exp_f32 v251, v251\n"
    "  v_cvt_pk_bf16_f32 v44, v232, v233\n"
    "  v_cvt_pk_bf16_f32 v45, v234, v235\n"
    "  v_cvt_pk_bf16_f32 v46, v236, v237\n"
    "  v_cvt_pk_bf16_f32 v47, v238, v239\n"
    "  ds_write_b128 v16, v[44:47] offset:2048\n"
    "  v_exp_f32 v252, v252\n"
    "  v_exp_f32 v253, v253\n"
    "  v_exp_f32 v254, v254\n"
    "  v_exp_f32 v255, v255\n"
    "  v_add_f32 v32, v32, v224\n"
    "  v_add_f32 v33, v33, v225\n"
    "  v_add_f32 v34, v34, v226\n"
    "  v_add_f32 v35, v35, v227\n"
    "  v_add_f32 v32, v32, v228\n"
    "  v_add_f32 v33, v33, v229\n"
    "  v_add_f32 v34, v34, v230\n"
    "  v_add_f32 v35, v35, v231\n"
    "  v_add_f32 v32, v32, v232\n"
    "  v_add_f32 v33, v33, v233\n"
    "  v_add_f32 v34, v34, v234\n"
    "  v_add_f32 v35, v35, v235\n"
    "  v_add_f32 v32, v32, v236\n"
    "  v_add_f32 v33, v33, v237\n"
    "  v_add_f32 v34, v34, v238\n"
    "  v_add_f32 v35, v35, v239\n"
    "  s_add_u32 m0, s63, 98304\n"
    "  s_nop 0\n"
    "  buffer_load_dwordx4 v28, s[48:51], 0 offen offset:0 lds\n"
    "  buffer_load_dwordx4 v29, s[48:51], 0 offen offset:1024 lds\n"
    "  buffer_load_dwordx4 v30, s[48:51], 0 offen offset:2048 lds\n"
    "  buffer_load_dwordx4 v31, s[48:51], 0 offen offset:3072 lds\n"
    "  s_waitcnt lgkmcnt(0)\n"
    "  s_barrier\n"
    "  v_cvt_pk_bf16_f32 v40, v240, v241\n"
    "  v_cvt_pk_bf16_f32 v41, v242, v243\n"
    "  v_cvt_pk_bf16_f32 v42, v244, v245\n"
    "  v_cvt_pk_bf16_f32 v43, v246, v247\n"
    "  ds_write_b128 v16, v[40:43] offset:8192\n"
    "  v_add_f32 v32, v32, v240\n"
    "  v_add_f32 v33, v33, v241\n"
    "  v_add_f32 v34, v34, v242\n"
    "  v_add_f32 v35, v35, v243\n"
    "  v_add_f32 v32, v32, v244\n"
    "  v_add_f32 v33, v33, v245\n"
    "  v_add_f32 v34, v34, v246\n"
    "  v_add_f32 v35, v35, v247\n"
    "  v_cvt_pk_bf16_f32 v44, v248, v249\n"
    "  v_cvt_pk_bf16_f32 v45, v250, v251\n"
    "  v_cvt_pk_bf16_f32 v46, v252, v253\n"
    "  v_cvt_pk_bf16_f32 v47, v254, v255\n"
    "  ds_write_b128 v16, v[44:47] offset:10240\n"
    "  v_add_f32 v32, v32, v248\n"
    "  v_add_f32 v33, v33, v249\n"
    "  v_add_f32 v34, v34, v250\n"
    "  v_add_f32 v35, v35, v251\n"
    "  v_add_f32 v32, v32, v252\n"
    "  v_add_f32 v33, v33, v253\n"
    "  v_add_f32 v34, v34, v254\n"
    "  v_add_f32 v35, v35, v255\n"
    "  v_add_f32 v32, v32, v33\n"
    "  v_add_f32 v34, v34, v35\n"
    "  v_add_f32 v32, v32, v34\n"
    "  v_mov_b32 v33, v32\n"
    "  s_nop 1\n"
    "  v_permlane32_swap_b32 v32, v33\n"
    "  v_add_f32 v32, v32, v33\n"
    "  ds_write_b32 v2, v32 offset:16384\n"
    "  ds_write_b32 v2, v32 offset:20480\n"
    "  ds_write_b32 v2, v32 offset:24576\n"
    "  ds_write_b32 v2, v32 offset:28672\n"
    "  s_lshl1_add_u32 s69, s21, 0\n"
    "  s_and_b32 s69, s69, 7\n"
    "  s_add_u32 m0, s63, 114688\n"
    "  s_mul_i32 s69, s69, 73728\n"
    "  buffer_load_dwordx4 v20, s[48:51], s69 offen offset:0 lds\n"
    "  buffer_load_dwordx4 v21, s[48:51], s69 offen offset:1024 lds\n"
    "  buffer_load_dwordx4 v22, s[48:51], s69 offen offset:2048 lds\n"
    "  buffer_load_dwordx4 v23, s[48:51], s69 offen offset:3072 lds\n"
    "  s_waitcnt lgkmcnt(0)\n"
    "  s_barrier\n"
    "  s_lshl1_add_u32 s69, s21, 1\n"
    "  s_and_b32 s69, s69, 7\n"
    "  s_add_u32 m0, s63, 131072\n"
    "  s_mul_i32 s69, s69, 73728\n"
    "  buffer_load_dwordx4 v20, s[48:51], s69 offen offset:0 lds\n"
    "  buffer_load_dwordx4 v21, s[48:51], s69 offen offset:1024 lds\n"
    "  buffer_load_dwordx4 v22, s[48:51], s69 offen offset:2048 lds\n"
    "  buffer_load_dwordx4 v23, s[48:51], s69 offen offset:3072 lds\n"
    "  v_and_b32 v4, 15, v0\n"
    "  v_lshrrev_b32 v5, 3, v0\n"
    "  v_and_b32 v3, 7, v0\n"
    "  v_lshl_add_u32 v56, v0, 4, s63\n"
    "  s_lshl_b32 s62, s23, 8\n"
    "  s_add_u32 s62, s62, 16384\n"
    "  v_lshl_add_u32 v57, v0, 2, s62\n"
    "  s_lshl_b32 s62, %1, 8\n"
    "  v_lshl_add_u32 v58, v0, 3, s62\n"
    "  s_cmp_eq_u32 s23, 1\n"
    "  s_cselect_b32 s62, 16, 0\n"
    "  v_cmp_gt_u32 vcc, s62, v0\n"
    "  s_nop 1\n"
    "  v_cndmask_b32 v58, 2.0, v58, vcc\n"
    "  v_mov_b32 v36, 81920\n"
    "  v_mov_b32 v37, 0\n"
    "  s_lshl_b32 s62, s23, 2\n"
    "  v_add_u32 v33, s62, v36\n"
    "  v_bfe_u32 v6, v0, 3, 2\n"
    "  v_lshrrev_b32 v7, 5, v0\n"
    "  v_lshl_add_u32 v6, v6, 1, v7\n"
    "  v_lshl_add_u32 v6, v6, 3, v3\n"
    "  s_lshl_b32 s62, s23, 9\n"
    "  v_lshl_add_u32 v59, v6, 2, s62\n"
    "  s_mul_i32 s66, %1, 46080\n"
    "  s_lshl_b32 s62, s23, 11\n"
    "  s_sub_u32 s65, s66, s62\n"
    "  s_add_u32 s65, s65, 214016\n"
    "  s_add_u32 s66, s66, 222208\n"
    "  s_waitcnt vmcnt(0)\n"
    "  s_barrier\n"
    "  s_lshl1_add_u32 s69, s21, 2\n"
    "  s_and_b32 s69, s69, 7\n"
    "  s_add_u32 m0, s63, 147456\n"
    "  s_mul_i32 s69, s69, 73728\n"
    "  buffer_load_dwordx4 v20, s[48:51], s69 offen offset:0 lds\n"
    "  buffer_load_dwordx4 v21, s[48:51], s69 offen offset:1024 lds\n"
    "  buffer_load_dwordx4 v22, s[48:51], s69 offen offset:2048 lds\n"
    "  buffer_load_dwordx4 v23, s[48:51], s69 offen offset:3072 lds\n"
    "  s_lshl1_add_u32 s69, s21, 3\n"
    "  s_and_b32 s69, s69, 7\n"
    "  s_add_u32 m0, s63, 0\n"
    "  s_mul_i32 s69, s69, 73728\n"
    "  buffer_load_dwordx4 v20, s[48:51], s69 offen offset:0 lds\n"
    "  buffer_load_dwordx4 v21, s[48:51], s69 offen offset:1024 lds\n"
    "  buffer_load_dwordx4 v22, s[48:51], s69 offen offset:2048 lds\n"
    "  buffer_load_dwordx4 v23, s[48:51], s69 offen offset:3072 lds\n"
    "  s_lshl1_add_u32 s69, s21, 4\n"
    "  s_and_b32 s69, s69, 7\n"
    "  s_add_u32 m0, s63, 16384\n"
    "  s_mul_i32 s69, s69, 73728\n"
    "  buffer_load_dwordx4 v20, s[48:51], s69 offen offset:0 lds\n"
    "  buffer_load_dwordx4 v21, s[48:51], s69 offen offset:1024 lds\n"
    "  buffer_load_dwordx4 v22, s[48:51], s69 offen offset:2048 lds\n"
    "  buffer_load_dwordx4 v23, s[48:51], s69 offen offset:3072 lds\n"
    "  s_cmp_lg_u32 s23, 0\n"
    "  s_cbranch_scc1 .Lgqa_h1p\n"
    "  s_load_dword s84, s[74:75], 0x0 glc\n"
    "  s_load_dword s85, s[76:77], 0x0 glc\n"
    "  s_load_dword s86, s[78:79], 0x0 glc\n"
    ".Lgqa_h1p:\n"
    "  s_lshl1_add_u32 s69, s21, 5\n"
    "  s_and_b32 s69, s69, 7\n"
    "  s_add_u32 m0, s63, 32768\n"
    "  s_mul_i32 s69, s69, 73728\n"
    "  buffer_load_dwordx4 v20, s[48:51], s69 offen offset:0 lds\n"
    "  buffer_load_dwordx4 v21, s[48:51], s69 offen offset:1024 lds\n"
    "  buffer_load_dwordx4 v22, s[48:51], s69 offen offset:2048 lds\n"
    "  buffer_load_dwordx4 v23, s[48:51], s69 offen offset:3072 lds\n"
    "  s_cmp_lg_u32 s23, 0\n"
    "  s_cbranch_scc1 .Lgqa_h1d\n"
    "  s_waitcnt lgkmcnt(0)\n"
    "  s_xor_b32 s84, s84, s24\n"
    "  s_xor_b32 s85, s85, s24\n"
    "  s_xor_b32 s86, s86, s24\n"
    "  s_or_b32 s84, s84, s85\n"
    "  s_or_b32 s84, s84, s86\n"
    "  s_cbranch_scc1 .Lgqa_h1r\n"
    ".Lgqa_h1d:\n"
    "  s_waitcnt vmcnt(8)\n"
    "  s_barrier\n"
    "  ds_write_b32 v33, v37\n"
    "  ds_write_b32 v36, v37 offset:16\n"
    "  ds_write_b32 v36, v37 offset:24\n"
    "  s_add_u32 m0, s63, 49152\n"
    "  s_nop 0\n"
    "  buffer_load_dwordx4 v56, s[28:31], s70 offen offset:0 sc1 lds\n"
    "  buffer_load_dwordx4 v56, s[28:31], s70 offen offset:1024 sc1 lds\n"
    "  buffer_load_dwordx4 v56, s[28:31], s70 offen offset:2048 sc1 lds\n"
    "  buffer_load_dwordx4 v56, s[28:31], s70 offen offset:3072 sc1 lds\n"
    "  buffer_load_dword v60, v57, s[28:31], s70 offen sc1\n"
    "  s_add_u32 m0, s63, 65536\n"
    "  s_nop 0\n"
    "  buffer_load_dwordx4 v56, s[28:31], s71 offen offset:0 sc1 lds\n"
    "  buffer_load_dwordx4 v56, s[28:31], s71 offen offset:1024 sc1 lds\n"
    "  buffer_load_dwordx4 v56, s[28:31], s71 offen offset:2048 sc1 lds\n"
    "  buffer_load_dwordx4 v56, s[28:31], s71 offen offset:3072 sc1 lds\n"
    "  buffer_load_dword v61, v57, s[28:31], s71 offen sc1\n"
    "  s_waitcnt vmcnt(10)\n"
    "  s_barrier\n"
    "  s_add_u32 m0, s63, 98304\n"
    "  s_nop 0\n"
    "  buffer_load_dwordx4 v56, s[28:31], s72 offen offset:0 sc1 lds\n"
    "  buffer_load_dwordx4 v56, s[28:31], s72 offen offset:1024 sc1 lds\n"
    "  buffer_load_dwordx4 v56, s[28:31], s72 offen offset:2048 sc1 lds\n"
    "  buffer_load_dwordx4 v56, s[28:31], s72 offen offset:3072 sc1 lds\n"
    "  buffer_load_dword v62, v57, s[28:31], s72 offen sc1\n"
    "  s_waitcnt vmcnt(10) lgkmcnt(0)\n"
    "  s_barrier\n"
    "  s_memrealtime s[80:81]\n"
    "  s_lshl1_add_u32 s69, s21, 6\n"
    "  s_and_b32 s69, s69, 7\n"
    "  s_add_u32 m0, s63, 114688\n"
    "  s_mul_i32 s69, s69, 73728\n"
    "  buffer_load_dwordx4 v20, s[48:51], s69 offen offset:0 lds\n"
    "  buffer_load_dwordx4 v21, s[48:51], s69 offen offset:1024 lds\n"
    "  buffer_load_dwordx4 v22, s[48:51], s69 offen offset:2048 lds\n"
    "  buffer_load_dwordx4 v23, s[48:51], s69 offen offset:3072 lds\n"
    "  s_waitcnt lgkmcnt(0)\n"
    "  v_mov_b32 v34, s24\n"
    "  v_mov_b32 v35, s80\n"
    "  buffer_store_dwordx2 v[34:35], v58, s[28:31], 0 offen offset:128 sc1\n"
    "  s_waitcnt vmcnt(10)\n"
    "  v_add_u32 v34, 49152, v56\n"
    "  ds_read_b128 v[64:67], v34 offset:0\n"
    "  ds_read_b128 v[68:71], v34 offset:1024\n"
    "  ds_read_b128 v[72:75], v34 offset:2048\n"
    "  ds_read_b128 v[76:79], v34 offset:3072\n"
    "  v_add_u32 v34, 65536, v56\n"
    "  ds_read_b128 v[80:83], v34 offset:0\n"
    "  ds_read_b128 v[84:87], v34 offset:1024\n"
    "  ds_read_b128 v[88:91], v34 offset:2048\n"
    "  ds_read_b128 v[92:95], v34 offset:3072\n"
    "  s_waitcnt vmcnt(10)\n"
    "  s_barrier\n"
    "  s_lshl1_add_u32 s69, s21, 7\n"
    "  s_and_b32 s69, s69, 7\n"
    "  s_add_u32 m0, s63, 131072\n"
    "  s_mul_i32 s69, s69, 73728\n"
    "  buffer_load_dwordx4 v20, s[48:51], s69 offen offset:0 lds\n"
    "  buffer_load_dwordx4 v21, s[48:51], s69 offen offset:1024 lds\n"
    "  buffer_load_dwordx4 v22, s[48:51], s69 offen offset:2048 lds\n"
    "  buffer_load_dwordx4 v23, s[48:51], s69 offen offset:3072 lds\n"
    "  s_waitcnt lgkmcnt(7)\n"
    "  v_add3_u32 v34, v64, v65, s24\n"
    "  s_waitcnt lgkmcnt(6)\n"
    "  v_add3_u32 v35, v66, v67, v68\n"
    "  v_add3_u32 v34, v34, v69, v70\n"
    "  s_waitcnt lgkmcnt(5)\n"
    "  v_add3_u32 v35, v35, v71, v72\n"
    "  v_add3_u32 v34, v34, v73, v74\n"
    "  s_waitcnt lgkmcnt(4)\n"
    "  v_add3_u32 v35, v35, v75, v76\n"
    "  v_add3_u32 v34, v34, v77, v78\n"
    "  v_add3_u32 v34, v34, v35, v79\n"
    "  v_xor_b32 v60, v34, v60\n"
    "  s_waitcnt lgkmcnt(3)\n"
    "  v_add3_u32 v34, v80, v81, s24\n"
    "  s_waitcnt lgkmcnt(2)\n"
    "  v_add3_u32 v35, v82, v83, v84\n"
    "  v_add3_u32 v34, v34, v85, v86\n"
    "  s_waitcnt lgkmcnt(1)\n"
    "  v_add3_u32 v35, v35, v87, v88\n"
    "  v_add3_u32 v34, v34, v89, v90\n"
    "  s_waitcnt lgkmcnt(0)\n"
    "  v_add3_u32 v35, v35, v91, v92\n"
    "  v_add3_u32 v34, v34, v93, v94\n"
    "  v_add3_u32 v34, v34, v35, v95\n"
    "  v_xor_b32 v61, v34, v61\n"
    "  s_waitcnt vmcnt(9)\n"
    "  v_add_u32 v34, 98304, v56\n"
    "  ds_read_b128 v[96:99], v34 offset:0\n"
    "  ds_read_b128 v[100:103], v34 offset:1024\n"
    "  ds_read_b128 v[104:107], v34 offset:2048\n"
    "  ds_read_b128 v[108:111], v34 offset:3072\n"
    "  v_add_u32 v34, 83968, v56\n"
    "  ds_read_b128 v[112:115], v34\n"
    "  ds_read_b128 v[116:119], v34 offset:1024\n"
    "  s_waitcnt lgkmcnt(1)\n"
    "  buffer_store_dwordx4 v[112:115], v56, s[28:31], s65 offen offset:0 sc1\n"
    "  s_waitcnt lgkmcnt(0)\n"
    "  buffer_store_dwordx4 v[116:119], v56, s[28:31], s65 offen offset:1024 sc1\n"
    "  v_add3_u32 v34, v112, v113, s24\n"
    "  v_add3_u32 v34, v114, v115, v34\n"
    "  v_add3_u32 v35, v116, v117, s24\n"
    "  v_add3_u32 v35, v118, v119, v35\n"
    "  buffer_store_dword v34, v59, s[28:31], s66 offen offset:0 sc1\n"
    "  buffer_store_dword v35, v59, s[28:31], s66 offen offset:256 sc1\n"
    "  s_waitcnt vmcnt(9)\n"
    "  s_barrier\n"
    "  s_waitcnt vmcnt(4)\n"
    "  s_barrier\n"
    "  v_add3_u32 v34, v96, v97, s24\n"
    "  v_add3_u32 v35, v98, v99, v100\n"
    "  v_add3_u32 v34, v34, v101, v102\n"
    "  v_add3_u32 v35, v35, v103, v104\n"
    "  v_add3_u32 v34, v34, v105, v106\n"
    "  v_add3_u32 v35, v35, v107, v108\n"
    "  v_add3_u32 v34, v34, v109, v110\n"
    "  v_add3_u32 v34, v34, v35, v111\n"
    "  v_xor_b32 v62, v34, v62\n"
    "  v_or3_b32 v34, v60, v61, v62\n"
    "  v_cmp_ne_u32 vcc, 0, v34\n"
    "  s_cmp_lg_u64 vcc, 0\n"
    "  s_cselect_b32 s62, 64, 1\n"
    "  v_mov_b32 v37, s62\n"
    "  ds_write_b32 v33, v37\n"
    ".Lgqa_t5:\n"
    "  s_mov_b32 s82, 1048576\n"
    ".Lgqa_t5w:\n"
    "  ds_read_b128 v[40:43], v36\n"
    "  s_sub_u32 s82, s82, 1\n"
    "  s_cbranch_scc1 .Lgqa_t5d\n"
    "  s_waitcnt lgkmcnt(0)\n"
    "  v_min_u32 v44, v40, v41\n"
    "  v_min3_u32 v44, v44, v42, v43\n"
    "  v_cmp_eq_u32 vcc, 0, v44\n"
    "  s_cbranch_vccnz .Lgqa_t5w\n"
    "  v_add3_u32 v44, v40, v41, v42\n"
    "  v_add_u32 v44, v44, v43\n"
    "  v_cmp_ne_u32 vcc, 4, v44\n"
    "  s_nop 4\n"
    "  s_cbranch_vccnz .Lgqa_redo\n"
    ".Lgqa_t5d:\n"
    "  s_cmp_eq_u32 s23, 0\n"
    "  s_cbranch_scc1 .Lgqa_tail\n"
    "  s_cmp_lg_u32 s23, 2\n"
    "  s_cbranch_scc1 .Lgqa_end\n"
    "  s_lshl_b32 s58, s19, 11\n"
    "  s_add_u32 s56, s16, s58\n"
    "  s_addc_u32 s57, s17, 0\n"
    "  s_movk_i32 s82, 4096\n"
    "  s_mov_b32 s83, 1\n"
    ".Lgqa_h2l:\n"
    "  s_load_dwordx2 s[32:33], s[56:57], 128 glc\n"
    "  s_load_dwordx2 s[34:35], s[56:57], 384 glc\n"
    "  s_load_dwordx2 s[36:37], s[56:57], 640 glc\n"
    "  s_load_dwordx2 s[38:39], s[56:57], 896 glc\n"
    "  s_load_dwordx2 s[40:41], s[56:57], 1152 glc\n"
    "  s_load_dwordx2 s[42:43], s[56:57], 1408 glc\n"
    "  s_load_dwordx2 s[44:45], s[56:57], 1664 glc\n"
    "  s_load_dwordx2 s[46:47], s[56:57], 1920 glc\n"
    "  s_sub_u32 s82, s82, 1\n"
    "  s_cbranch_scc1 .Lgqa_h2d\n"
    "  s_waitcnt lgkmcnt(0)\n"
    "  s_xor_b32 s58, s32, s24\n"
    "  s_xor_b32 s59, s34, s24\n"
    "  s_or_b32 s58, s58, s59\n"
    "  s_xor_b32 s59, s36, s24\n"
    "  s_or_b32 s58, s58, s59\n"
    "  s_xor_b32 s59, s38, s24\n"
    "  s_or_b32 s58, s58, s59\n"
    "  s_xor_b32 s59, s40, s24\n"
    "  s_or_b32 s58, s58, s59\n"
    "  s_xor_b32 s59, s42, s24\n"
    "  s_or_b32 s58, s58, s59\n"
    "  s_xor_b32 s59, s44, s24\n"
    "  s_or_b32 s58, s58, s59\n"
    "  s_xor_b32 s59, s46, s24\n"
    "  s_or_b32 s58, s58, s59\n"
    "  s_cbranch_scc0 .Lgqa_h2k\n"
    "  s_sleep 0\n"
    "  s_branch .Lgqa_h2l\n"
    ".Lgqa_h2k:\n"
    "  s_sub_u32 s59, s33, s80\n"
    "  s_max_i32 s83, s83, s59\n"
    "  s_sub_u32 s59, s35, s80\n"
    "  s_max_i32 s83, s83, s59\n"
    "  s_sub_u32 s59, s37, s80\n"
    "  s_max_i32 s83, s83, s59\n"
    "  s_sub_u32 s59, s39, s80\n"
    "  s_max_i32 s83, s83, s59\n"
    "  s_sub_u32 s59, s41, s80\n"
    "  s_max_i32 s83, s83, s59\n"
    "  s_sub_u32 s59, s43, s80\n"
    "  s_max_i32 s83, s83, s59\n"
    "  s_sub_u32 s59, s45, s80\n"
    "  s_max_i32 s83, s83, s59\n"
    "  s_sub_u32 s59, s47, s80\n"
    "  s_max_i32 s83, s83, s59\n"
    ".Lgqa_h2d:\n"
    "  s_add_u32 s59, s83, s80\n"
    "  v_mov_b32 v34, s83\n"
    "  v_mov_b32 v35, s59\n"
    "  ds_write_b64 v36, v[34:35] offset:16\n"
    "  s_waitcnt lgkmcnt(0)\n"
    ".Lgqa_end:\n"
    "  s_endpgm\n"
    ".Lgqa_tail:\n"
    "  s_lshr_b32 s56, s21, 1\n"
    "  s_lshr_b32 s57, s18, 2\n"
    "  s_lshl1_add_u32 s56, s56, s57\n"
    "  s_and_b32 s57, s21, 1\n"
    "  s_lshl1_add_u32 s56, s56, s57\n"
    "  s_and_b32 s57, s18, 3\n"
    "  s_mul_i32 s58, s20, 1474560\n"
    "  s_lshl_b32 s59, s56, 10\n"
    "  s_add_u32 s59, s59, s58\n"
    "  s_lshl_b32 s60, s57, 7\n"
    "  s_add_u32 s59, s59, s60\n"
    "  s_add_u32 s59, s59, 214016\n"
    "  s_lshl_b32 s60, s56, 8\n"
    "  s_add_u32 s60, s60, s58\n"
    "  s_lshl_b32 s61, s57, 6\n"
    "  s_add_u32 s60, s60, s61\n"
    "  s_add_u32 s60, s60, 222208\n"
    "  s_mov_b32 s61, 368640\n"
    "  v_and_b32 v5, 1, v5\n"
    "  v_lshlrev_b32 v6, 9, v5\n"
    "  v_lshl_add_u32 v6, v3, 4, v6\n"
    "  v_mad_u32_u24 v6, v1, s61, v6\n"
    "  v_add_u32 v6, s59, v6\n"
    "  v_lshlrev_b32 v7, 2, v4\n"
    "  v_mad_u32_u24 v7, v1, s61, v7\n"
    "  v_add_u32 v7, s60, v7\n"
    "  s_lshl3_add_u32 s56, s20, s18\n"
    "  s_mul_i32 s56, s56, 9216\n"
    "  s_lshl_b32 s57, s21, 5\n"
    "  s_add_u32 s56, s56, s57\n"
    "  s_add_u32 s56, s56, 1024\n"
    "  v_and_b32 v8, 1, v1\n"
    "  v_lshlrev_b32 v8, 4, v8\n"
    "  v_lshl_add_u32 v8, v5, 3, v8\n"
    "  v_lshrrev_b32 v9, 5, v0\n"
    "  v_lshl_add_u32 v8, v9, 2, v8\n"
    "  v_mad_u32_u24 v8, v3, s64, v8\n"
    "  v_mov_b32 v10, 0x3f80\n"
    "  v_mov_b32 v11, 0x3f800000\n"
    "  s_lshl_b32 s62, s24, 3\n"
    "  s_mov_b32 s82, 1048576\n"
    ".Lgqa_tw0:\n"
    "  ds_read_b32 v12, v36 offset:16\n"
    "  s_sub_u32 s82, s82, 1\n"
    "  s_cbranch_scc1 .Lgqa_tw2\n"
    "  s_waitcnt lgkmcnt(0)\n"
    "  v_readfirstlane_b32 s57, v12\n"
    "  s_cmp_eq_u32 s57, 0\n"
    "  s_cbranch_scc1 .Lgqa_tw0\n"
    "  s_add_u32 s57, s57, s80\n"
    "  s_add_u32 s57, s57, 120\n"
    "  s_movk_i32 s82, 256\n"
    ".Lgqa_tw1:\n"
    "  s_memrealtime s[58:59]\n"
    "  s_sub_u32 s82, s82, 1\n"
    "  s_cbranch_scc1 .Lgqa_tw2\n"
    "  s_waitcnt lgkmcnt(0)\n"
    "  s_sub_u32 s58, s58, s57\n"
    "  s_cmp_lt_i32 s58, 0\n"
    "  s_cbranch_scc1 .Lgqa_tw1\n"
    ".Lgqa_tw2:\n"
    "  s_waitcnt lgkmcnt(0)\n"
    "  s_mov_b32 s82, 1048576\n"
    ".Lgqa_tl:\n"
    "  s_mov_b32 s61, 0\n"
    "  buffer_load_dwordx4 v[64:67], v6, s[28:31], s61 offen sc1\n"
    "  buffer_load_dword v96, v7, s[28:31], s61 offen sc1\n"
    "  s_add_u32 s61, s61, 46080\n"
    "  buffer_load_dwordx4 v[68:71], v6, s[28:31], s61 offen sc1\n"
    "  buffer_load_dword v97, v7, s[28:31], s61 offen sc1\n"
    "  s_add_u32 s61, s61, 46080\n"
    "  buffer_load_dwordx4 v[72:75], v6, s[28:31], s61 offen sc1\n"
    "  buffer_load_dword v98, v7, s[28:31], s61 offen sc1\n"
    "  s_add_u32 s61, s61, 46080\n"
    "  buffer_load_dwordx4 v[76:79], v6, s[28:31], s61 offen sc1\n"
    "  buffer_load_dword v99, v7, s[28:31], s61 offen sc1\n"
    "  s_add_u32 s61, s61, 46080\n"
    "  buffer_load_dwordx4 v[80:83], v6, s[28:31], s61 offen sc1\n"
    "  buffer_load_dword v100, v7, s[28:31], s61 offen sc1\n"
    "  s_add_u32 s61, s61, 46080\n"
    "  buffer_load_dwordx4 v[84:87], v6, s[28:31], s61 offen sc1\n"
    "  buffer_load_dword v101, v7, s[28:31], s61 offen sc1\n"
    "  s_add_u32 s61, s61, 46080\n"
    "  buffer_load_dwordx4 v[88:91], v6, s[28:31], s61 offen sc1\n"
    "  buffer_load_dword v102, v7, s[28:31], s61 offen sc1\n"
    "  s_add_u32 s61, s61, 46080\n"
    "  buffer_load_dwordx4 v[92:95], v6, s[28:31], s61 offen sc1\n"
    "  buffer_load_dword v103, v7, s[28:31], s61 offen sc1\n"
    "  s_waitcnt vmcnt(14)\n"
    "  v_add3_u32 v12, v64, v65, s62\n"
    "  v_sub_u32 v13, v66, v96\n"
    "  v_lshlrev_b32 v20, 16, v64\n"
    "  v_and_b32 v21, 0xffff0000, v64\n"
    "  v_lshlrev_b32 v22, 16, v65\n"
    "  v_and_b32 v23, 0xffff0000, v65\n"
    "  v_lshlrev_b32 v24, 16, v66\n"
    "  v_and_b32 v25, 0xffff0000, v66\n"
    "  v_lshlrev_b32 v26, 16, v67\n"
    "  v_and_b32 v27, 0xffff0000, v67\n"
    "  v_add_u32 v13, v13, v67\n"
    "  s_waitcnt vmcnt(12)\n"
    "  v_add3_u32 v12, v12, v68, v69\n"
    "  v_add3_u32 v13, v13, v70, v71\n"
    "  v_dot2c_f32_bf16 v20, v10, v68\n"
    "  v_dot2c_f32_bf16 v21, v11, v68\n"
    "  v_dot2c_f32_bf16 v22, v10, v69\n"
    "  v_dot2c_f32_bf16 v23, v11, v69\n"
    "  v_dot2c_f32_bf16 v24, v10, v70\n"
    "  v_dot2c_f32_bf16 v25, v11, v70\n"
    "  v_dot2c_f32_bf16 v26, v10, v71\n"
    "  v_dot2c_f32_bf16 v27, v11, v71\n"
    "  v_sub_u32 v12, v12, v97\n"
    "  s_waitcnt vmcnt(10)\n"
    "  v_add3_u32 v12, v12, v72, v73\n"
    "  v_add3_u32 v13, v13, v74, v75\n"
    "  v_dot2c_f32_bf16 v20, v10, v72\n"
    "  v_dot2c_f32_bf16 v21, v11, v72\n"
    "  v_dot2c_f32_bf16 v22, v10, v73\n"
    "  v_dot2c_f32_bf16 v23, v11, v73\n"
    "  v_dot2c_f32_bf16 v24, v10, v74\n"
    "  v_dot2c_f32_bf16 v25, v11, v74\n"
    "  v_dot2c_f32_bf16 v26, v10, v75\n"
    "  v_dot2c_f32_bf16 v27, v11, v75\n"
    "  v_sub_u32 v12, v12, v98\n"
    "  s_waitcnt vmcnt(8)\n"
    "  v_add3_u32 v12, v12, v76, v77\n"
    "  v_add3_u32 v13, v13, v78, v79\n"
    "  v_dot2c_f32_bf16 v20, v10, v76\n"
    "  v_dot2c_f32_bf16 v21, v11, v76\n"
    "  v_dot2c_f32_bf16 v22, v10, v77\n"
    "  v_dot2c_f32_bf16 v23, v11, v77\n"
    "  v_dot2c_f32_bf16 v24, v10, v78\n"
    "  v_dot2c_f32_bf16 v25, v11, v78\n"
    "  v_dot2c_f32_bf16 v26, v10, v79\n"
    "  v_dot2c_f32_bf16 v27, v11, v79\n"
    "  v_sub_u32 v12, v12, v99\n"
    "  s_waitcnt vmcnt(6)\n"
    "  v_add3_u32 v12, v12, v80, v81\n"
    "  v_add3_u32 v13, v13, v82, v83\n"
    "  v_dot2c_f32_bf16 v20, v10, v80\n"
    "  v_dot2c_f32_bf16 v21, v11, v80\n"
    "  v_dot2c_f32_bf16 v22, v10, v81\n"
    "  v_dot2c_f32_bf16 v23, v11, v81\n"
    "  v_dot2c_f32_bf16 v24, v10, v82\n"
    "  v_dot2c_f32_bf16 v25, v11, v82\n"
    "  v_dot2c_f32_bf16 v26, v10, v83\n"
    "  v_dot2c_f32_bf16 v27, v11, v83\n"
    "  v_sub_u32 v12, v12, v100\n"
    "  s_waitcnt vmcnt(4)\n"
    "  v_add3_u32 v12, v12, v84, v85\n"
    "  v_add3_u32 v13, v13, v86, v87\n"
    "  v_dot2c_f32_bf16 v20, v10, v84\n"
    "  v_dot2c_f32_bf16 v21, v11, v84\n"
    "  v_dot2c_f32_bf16 v22, v10, v85\n"
    "  v_dot2c_f32_bf16 v23, v11, v85\n"
    "  v_dot2c_f32_bf16 v24, v10, v86\n"
    "  v_dot2c_f32_bf16 v25, v11, v86\n"
    "  v_dot2c_f32_bf16 v26, v10, v87\n"
    "  v_dot2c_f32_bf16 v27, v11, v87\n"
    "  v_sub_u32 v12, v12, v101\n"
    "  s_waitcnt vmcnt(2)\n"
    "  v_add3_u32 v12, v12, v88, v89\n"
    "  v_add3_u32 v13, v13, v90, v91\n"
    "  v_dot2c_f32_bf16 v20, v10, v88\n"
    "  v_dot2c_f32_bf16 v21, v11, v88\n"
    "  v_dot2c_f32_bf16 v22, v10, v89\n"
    "  v_dot2c_f32_bf16 v23, v11, v89\n"
    "  v_dot2c_f32_bf16 v24, v10, v90\n"
    "  v_dot2c_f32_bf16 v25, v11, v90\n"
    "  v_dot2c_f32_bf16 v26, v10, v91\n"
    "  v_dot2c_f32_bf16 v27, v11, v91\n"
    "  v_sub_u32 v12, v12, v102\n"
    "  s_waitcnt vmcnt(0)\n"
    "  v_add3_u32 v12, v12, v92, v93\n"
    "  v_add3_u32 v13, v13, v94, v95\n"
    "  v_dot2c_f32_bf16 v20, v10, v92\n"
    "  v_dot2c_f32_bf16 v21, v11, v92\n"
    "  v_dot2c_f32_bf16 v22, v10, v93\n"
    "  v_dot2c_f32_bf16 v23, v11, v93\n"
    "  v_dot2c_f32_bf16 v24, v10, v94\n"
    "  v_dot2c_f32_bf16 v25, v11, v94\n"
    "  v_dot2c_f32_bf16 v26, v10, v95\n"
    "  v_dot2c_f32_bf16 v27, v11, v95\n"
    "  v_sub_u32 v12, v12, v103\n"
    "  v_add_u32 v12, v12, v13\n"
    "  v_cmp_ne_u32 vcc, 0, v12\n"
    "  s_cbranch_vccz .Lgqa_tg\n"
    "  s_sub_u32 s82, s82, 1\n"
    "  s_sleep 4\n"
    "  s_cbranch_scc0 .Lgqa_tl\n"
    ".Lgqa_tg:\n"
    "  v_permlane16_swap_b32 v20, v24\n"
    "  v_permlane16_swap_b32 v21, v25\n"
    "  v_permlane16_swap_b32 v22, v26\n"
    "  v_permlane16_swap_b32 v23, v27\n"
    "  v_add_f32 v20, v20, v24\n"
    "  v_add_f32 v21, v21, v25\n"
    "  v_add_f32 v22, v22, v26\n"
    "  v_add_f32 v23, v23, v27\n"
    "  s_nop 0\n"
    "  v_permlane32_swap_b32 v20, v22\n"
    "  v_permlane32_swap_b32 v21, v23\n"
    "  v_add_f32 v20, v20, v22\n"
    "  v_add_f32 v21, v21, v23\n"
    "  v_cvt_pk_bf16_f32 v20, v20, v21\n"
    "  buffer_store_dword v20, v8, s[52:55], s56 offen\n"
    "  s_and_b32 s57, %1, 31\n"
    "  s_cbranch_scc1 .Lgqa_te\n"
    "  s_lshl_b32 s57, s20, 7\n"
    "  s_add_u32 s57, s57, 65536\n"
    "  v_mov_b32 v12, s24\n"
    "  v_mov_b32 v13, 0\n"
    "  buffer_store_dword v12, v13, s[28:31], s57 offen sc1\n"
    ".Lgqa_te:\n"
    "  s_endpgm\n"
    ".Lgqa_h1r:\n"
    "  s_movk_i32 s82, 4096\n"
    ".Lgqa_h1l:\n"
    "  s_sub_u32 s82, s82, 1\n"
    "  s_cbranch_scc1 .Lgqa_h1d\n"
    "  s_sleep 0\n"
    "  s_load_dword s84, s[74:75], 0x0 glc\n"
    "  s_load_dword s85, s[76:77], 0x0 glc\n"
    "  s_load_dword s86, s[78:79], 0x0 glc\n"
    "  s_waitcnt lgkmcnt(0)\n"
    "  s_xor_b32 s84, s84, s24\n"
    "  s_xor_b32 s85, s85, s24\n"
    "  s_xor_b32 s86, s86, s24\n"
    "  s_or_b32 s84, s84, s85\n"
    "  s_or_b32 s84, s84, s86\n"
    "  s_cbranch_scc1 .Lgqa_h1l\n"
    "  s_sleep 0\n"
    "  s_branch .Lgqa_h1d\n"
    ".Lgqa_last:\n"
    ".Lgqa_verd:\n"
    "  s_mov_b32 s59, 1048576\n"
    ".Lgqa_verd1:\n"
    "  ds_read_b128 v[192:195], v223\n"
    "  s_waitcnt lgkmcnt(0)\n"
    "  v_min_u32 v196, v192, v193\n"
    "  v_min3_u32 v196, v196, v194, v195\n"
    "  v_cmp_eq_u32 vcc, 0, v196\n"
    "  s_sub_u32 s59, s59, 1\n"
    "  s_cbranch_scc1 .Lgqa_good\n"
    "  s_cbranch_vccnz .Lgqa_verd1\n"
    "  v_add3_u32 v196, v192, v193, v194\n"
    "  v_add_u32 v196, v196, v195\n"
    "  v_cmp_ne_u32 vcc, 4, v196\n"
    "  s_nop 4\n"
    "  s_cbranch_vccz .Lgqa_good\n"
    "  s_nop 11\n"
    "  v_mov_b64 v[96:97], v[128:129]\n"
    "  v_mov_b64 v[98:99], v[130:131]\n"
    "  v_mov_b64 v[100:101], v[132:133]\n"
    "  v_mov_b64 v[102:103], v[134:135]\n"
    "  v_mov_b64 v[104:105], v[136:137]\n"
    "  v_mov_b64 v[106:107], v[138:139]\n"
    "  v_mov_b64 v[108:109], v[140:141]\n"
    "  v_mov_b64 v[110:111], v[142:143]\n"
    "  v_mov_b64 v[112:113], v[144:145]\n"
    "  v_mov_b64 v[114:115], v[146:147]\n"
    "  v_mov_b64 v[116:117], v[148:149]\n"
    "  v_mov_b64 v[118:119], v[150:151]\n"
    "  v_mov_b64 v[120:121], v[152:153]\n"
    "  v_mov_b64 v[122:123], v[154:155]\n"
    "  v_mov_b64 v[124:125], v[156:157]\n"
    "  v_mov_b64 v[126:127], v[158:159]\n"
    "  ds_read_b64_tr_b16 v[160:161], v209 offset:49152\n"
    "  ds_read_b64_tr_b16 v[162:163], v209 offset:51200\n"
    "  ds_read_b64_tr_b16 v[164:165], v209 offset:53248\n"
    "  ds_read_b64_tr_b16 v[166:167], v209 offset:55296\n"
    "  ds_read_b64_tr_b16 v[168:169], v209 offset:57344\n"
    "  ds_read_b64_tr_b16 v[170:171], v209 offset:59392\n"
    "  ds_read_b64_tr_b16 v[172:173], v209 offset:61440\n"
    "  ds_read_b64_tr_b16 v[174:175], v209 offset:63488\n"
    "  ds_read_b64_tr_b16 v[176:177], v210 offset:0\n"
    "  ds_read_b64_tr_b16 v[178:179], v210 offset:2048\n"
    "  ds_read_b64_tr_b16 v[180:181], v210 offset:4096\n"
    "  ds_read_b64_tr_b16 v[182:183], v210 offset:6144\n"
    "  ds_read_b64_tr_b16 v[184:185], v210 offset:8192\n"
    "  ds_read_b64_tr_b16 v[186:187], v210 offset:10240\n"
    "  ds_read_b64_tr_b16 v[188:189], v210 offset:12288\n"
    "  ds_read_b64_tr_b16 v[190:191], v210 offset:14336\n"
    "  s_waitcnt lgkmcnt(0)\n"
    "  s_branch .Lgqa_again\n"
    ".Lgqa_redo:\n"
    "  s_mov_b32 s82, 1048576\n"
    ".Lgqa_redo1:\n"
    "  s_sleep 8\n"
    "  s_add_u32 m0, s63, 49152\n"
    "  s_nop 0\n"
    "  buffer_load_dwordx4 v56, s[28:31], s70 offen offset:0 sc1 lds\n"
    "  buffer_load_dwordx4 v56, s[28:31], s70 offen offset:1024 sc1 lds\n"
    "  buffer_load_dwordx4 v56, s[28:31], s70 offen offset:2048 sc1 lds\n"
    "  buffer_load_dwordx4 v56, s[28:31], s70 offen offset:3072 sc1 lds\n"
    "  buffer_load_dword v60, v57, s[28:31], s70 offen sc1\n"
    "  s_add_u32 m0, s63, 65536\n"
    "  s_nop 0\n"
    "  buffer_load_dwordx4 v56, s[28:31], s71 offen offset:0 sc1 lds\n"
    "  buffer_load_dwordx4 v56, s[28:31], s71 offen offset:1024 sc1 lds\n"
    "  buffer_load_dwordx4 v56, s[28:31], s71 offen offset:2048 sc1 lds\n"
    "  buffer_load_dwordx4 v56, s[28:31], s71 offen offset:3072 sc1 lds\n"
    "  buffer_load_dword v61, v57, s[28:31], s71 offen sc1\n"
    "  s_add_u32 m0, s63, 98304\n"
    "  s_nop 0\n"
    "  buffer_load_dwordx4 v56, s[28:31], s72 offen offset:0 sc1 lds\n"
    "  buffer_load_dwordx4 v56, s[28:31], s72 offen offset:1024 sc1 lds\n"
    "  buffer_load_dwordx4 v56, s[28:31], s72 offen offset:2048 sc1 lds\n"
    "  buffer_load_dwordx4 v56, s[28:31], s72 offen offset:3072 sc1 lds\n"
    "  buffer_load_dword v62, v57, s[28:31], s72 offen sc1\n"
    "  s_waitcnt vmcnt(0)\n"
    "  v_add_u32 v34, 49152, v56\n"
    "  ds_read_b128 v[64:67], v34 offset:0\n"
    "  ds_read_b128 v[68:71], v34 offset:1024\n"
    "  ds_read_b128 v[72:75], v34 offset:2048\n"
    "  ds_read_b128 v[76:79], v34 offset:3072\n"
    "  v_add_u32 v34, 65536, v56\n"
    "  ds_read_b128 v[80:83], v34 offset:0\n"
    "  ds_read_b128 v[84:87], v34 offset:1024\n"
    "  ds_read_b128 v[88:91], v34 offset:2048\n"
    "  ds_read_b128 v[92:95], v34 offset:3072\n"
    "  v_add_u32 v34, 98304, v56\n"
    "  ds_read_b128 v[96:99], v34 offset:0\n"
    "  ds_read_b128 v[100:103], v34 offset:1024\n"
    "  ds_read_b128 v[104:107], v34 offset:2048\n"
    "  ds_read_b128 v[108:111], v34 offset:3072\n"
    "  s_waitcnt lgkmcnt(11)\n"
    "  v_add3_u32 v34, v64, v65, s24\n"
    "  s_waitcnt lgkmcnt(10)\n"
    "  v_add3_u32 v35, v66, v67, v68\n"
    "  v_add3_u32 v34, v34, v69, v70\n"
    "  s_waitcnt lgkmcnt(9)\n"
    "  v_add3_u32 v35, v35, v71, v72\n"
    "  v_add3_u32 v34, v34, v73, v74\n"
    "  s_waitcnt lgkmcnt(8)\n"
    "  v_add3_u32 v35, v35, v75, v76\n"
    "  v_add3_u32 v34, v34, v77, v78\n"
    "  v_add3_u32 v34, v34, v35, v79\n"
    "  v_xor_b32 v60, v34, v60\n"
    "  s_waitcnt lgkmcnt(7)\n"
    "  v_add3_u32 v34, v80, v81, s24\n"
    "  s_waitcnt lgkmcnt(6)\n"
    "  v_add3_u32 v35, v82, v83, v84\n"
    "  v_add3_u32 v34, v34, v85, v86\n"
    "  s_waitcnt lgkmcnt(5)\n"
    "  v_add3_u32 v35, v35, v87, v88\n"
    "  v_add3_u32 v34, v34, v89, v90\n"
    "  s_waitcnt lgkmcnt(4)\n"
    "  v_add3_u32 v35, v35, v91, v92\n"
    "  v_add3_u32 v34, v34, v93, v94\n"
    "  v_add3_u32 v34, v34, v35, v95\n"
    "  v_xor_b32 v61, v34, v61\n"
    "  s_waitcnt lgkmcnt(3)\n"
    "  v_add3_u32 v34, v96, v97, s24\n"
    "  s_waitcnt lgkmcnt(2)\n"
    "  v_add3_u32 v35, v98, v99, v100\n"
    "  v_add3_u32 v34, v34, v101, v102\n"
    "  s_waitcnt lgkmcnt(1)\n"
    "  v_add3_u32 v35, v35, v103, v104\n"
    "  v_add3_u32 v34, v34, v105, v106\n"
    "  s_waitcnt lgkmcnt(0)\n"
    "  v_add3_u32 v35, v35, v107, v108\n"
    "  v_add3_u32 v34, v34, v109, v110\n"
    "  v_add3_u32 v34, v34, v35, v111\n"
    "  v_xor_b32 v62, v34, v62\n"
    "  v_or3_b32 v34, v60, v61, v62\n"
    "  v_cmp_ne_u32 vcc, 0, v34\n"
    "  s_sub_u32 s82, s82, 1\n"
    "  s_cbranch_scc1 .Lgqa_redo2\n"
    "  s_cbranch_vccnz .Lgqa_redo1\n"
    ".Lgqa_redo2:\n"
    "  s_barrier\n"
    "  v_mov_b32 v37, 1\n"
    "  ds_write_b32 v33, v37\n"
    "  s_waitcnt lgkmcnt(0)\n"
    "  s_barrier\n"
    "  s_barrier\n"
    "  s_barrier\n"
    "  s_branch .Lgqa_t5\n"
    ".Lgqa_biga:\n"
    "  v_max_f32 v24, 0, v36\n"
    "  v_sub_f32 v36, 0, v24\n"
    "  s_mov_b32 s59, 1\n"
    "  v_pk_add_f32 v[144:145], v[144:145], v[36:37] op_sel:[0,0] op_sel_hi:[1,0]\n"
    "  v_pk_add_f32 v[146:147], v[146:147], v[36:37] op_sel:[0,0] op_sel_hi:[1,0]\n"
    "  v_pk_add_f32 v[148:149], v[148:149], v[36:37] op_sel:[0,0] op_sel_hi:[1,0]\n"
    "  v_pk_add_f32 v[150:151], v[150:151], v[36:37] op_sel:[0,0] op_sel_hi:[1,0]\n"
    "  v_pk_add_f32 v[152:153], v[152:153], v[36:37] op_sel:[0,0] op_sel_hi:[1,0]\n"
    "  v_pk_add_f32 v[154:155], v[154:155], v[36:37] op_sel:[0,0] op_sel_hi:[1,0]\n"
    "  v_pk_add_f32 v[156:157], v[156:157], v[36:37] op_sel:[0,0] op_sel_hi:[1,0]\n"
    "  v_pk_add_f32 v[158:159], v[158:159], v[36:37] op_sel:[0,0] op_sel_hi:[1,0]\n"
    "  v_pk_add_f32 v[160:161], v[160:161], v[36:37] op_sel:[0,0] op_sel_hi:[1,0]\n"
    "  v_pk_add_f32 v[162:163], v[162:163], v[36:37] op_sel:[0,0] op_sel_hi:[1,0]\n"
    "  v_pk_add_f32 v[164:165], v[164:165], v[36:37] op_sel:[0,0] op_sel_hi:[1,0]\n"
    "  v_pk_add_f32 v[166:167], v[166:167], v[36:37] op_sel:[0,0] op_sel_hi:[1,0]\n"
    "  v_pk_add_f32 v[168:169], v[168:169], v[36:37] op_sel:[0,0] op_sel_hi:[1,0]\n"
    "  v_pk_add_f32 v[170:171], v[170:171], v[36:37] op_sel:[0,0] op_sel_hi:[1,0]\n"
    "  v_pk_add_f32 v[172:173], v[172:173], v[36:37] op_sel:[0,0] op_sel_hi:[1,0]\n"
    "  v_pk_add_f32 v[174:175], v[174:175], v[36:37] op_sel:[0,0] op_sel_hi:[1,0]\n"
    "  v_pk_add_f32 v[176:177], v[176:177], v[36:37] op_sel:[0,0] op_sel_hi:[1,0]\n"
    "  v_pk_add_f32 v[178:179], v[178:179], v[36:37] op_sel:[0,0] op_sel_hi:[1,0]\n"
    "  v_pk_add_f32 v[180:181], v[180:181], v[36:37] op_sel:[0,0] op_sel_hi:[1,0]\n"
    "  v_pk_add_f32 v[182:183], v[182:183], v[36:37] op_sel:[0,0] op_sel_hi:[1,0]\n"
    "  v_pk_add_f32 v[184:185], v[184:185], v[36:37] op_sel:[0,0] op_sel_hi:[1,0]\n"
    "  v_pk_add_f32 v[186:187], v[186:187], v[36:37] op_sel:[0,0] op_sel_hi:[1,0]\n"
    "  v_pk_add_f32 v[188:189], v[188:189], v[36:37] op_sel:[0,0] op_sel_hi:[1,0]\n"
    "  v_pk_add_f32 v[190:191], v[190:191], v[36:37] op_sel:[0,0] op_sel_hi:[1,0]\n"
    "  s_nop 1\n"
    "  s_branch .Lgqa_oka\n"
    ".Lgqa_bigb:\n"
    "  v_max_f32 v26, 0, v36\n"
    "  v_sub_f32 v36, 0, v26\n"
    "  s_mov_b32 s59, 1\n"
    "  v_pk_add_f32 v[192:193], v[192:193], v[36:37] op_sel:[0,0] op_sel_hi:[1,0]\n"
    "  v_pk_add_f32 v[194:195], v[194:195], v[36:37] op_sel:[0,0] op_sel_hi:[1,0]\n"
    "  v_pk_add_f32 v[196:197], v[196:197], v[36:37] op_sel:[0,0] op_sel_hi:[1,0]\n"
    "  v_pk_add_f32 v[198:199], v[198:199], v[36:37] op_sel:[0,0] op_sel_hi:[1,0]\n"
    "  v_pk_add_f32 v[200:201], v[200:201], v[36:37] op_sel:[0,0] op_sel_hi:[1,0]\n"
    "  v_pk_add_f32 v[202:203], v[202:203], v[36:37] op_sel:[0,0] op_sel_hi:[1,0]\n"
    "  v_pk_add_f32 v[204:205], v[204:205], v[36:37] op_sel:[0,0] op_sel_hi:[1,0]\n"
    "  v_pk_add_f32 v[206:207], v[206:207], v[36:37] op_sel:[0,0] op_sel_hi:[1,0]\n"
    "  v_pk_add_f32 v[208:209], v[208:209], v[36:37] op_sel:[0,0] op_sel_hi:[1,0]\n"
    "  v_pk_add_f32 v[210:211], v[210:211], v[36:37] op_sel:[0,0] op_sel_hi:[1,0]\n"
    "  v_pk_add_f32 v[212:213], v[212:213], v[36:37] op_sel:[0,0] op_sel_hi:[1,0]\n"
    "  v_pk_add_f32 v[214:215], v[214:215], v[36:37] op_sel:[0,0] op_sel_hi:[1,0]\n"
    "  v_pk_add_f32 v[216:217], v[216:217], v[36:37] op_sel:[0,0] op_sel_hi:[1,0]\n"
    "  v_pk_add_f32 v[218:219], v[218:219], v[36:37] op_sel:[0,0] op_sel_hi:[1,0]\n"
    "  v_pk_add_f32 v[220:221], v[220:221], v[36:37] op_sel:[0,0] op_sel_hi:[1,0]\n"
    "  v_pk_add_f32 v[222:223], v[222:223], v[36:37] op_sel:[0,0] op_sel_hi:[1,0]\n"
    "  v_pk_add_f32 v[224:225], v[224:225], v[36:37] op_sel:[0,0] op_sel_hi:[1,0]\n"
    "  v_pk_add_f32 v[226:227], v[226:227], v[36:37] op_sel:[0,0] op_sel_hi:[1,0]\n"
    "  v_pk_add_f32 v[228:229], v[228:229], v[36:37] op_sel:[0,0] op_sel_hi:[1,0]\n"
    "  v_pk_add_f32 v[230:231], v[230:231], v[36:37] op_sel:[0,0] op_sel_hi:[1,0]\n"
    "  v_pk_add_f32 v[232:233], v[232:233], v[36:37] op_sel:[0,0] op_sel_hi:[1,0]\n"
    "  v_pk_add_f32 v[234:235], v[234:235], v[36:37] op_sel:[0,0] op_sel_hi:[1,0]\n"
    "  v_pk_add_f32 v[236:237], v[236:237], v[36:37] op_sel:[0,0] op_sel_hi:[1,0]\n"
    "  v_pk_add_f32 v[238:239], v[238:239], v[36:37] op_sel:[0,0] op_sel_hi:[1,0]\n"
    "  v_pk_add_f32 v[240:241], v[240:241], v[36:37] op_sel:[0,0] op_sel_hi:[1,0]\n"
    "  v_pk_add_f32 v[242:243], v[242:243], v[36:37] op_sel:[0,0] op_sel_hi:[1,0]\n"
    "  v_pk_add_f32 v[244:245], v[244:245], v[36:37] op_sel:[0,0] op_sel_hi:[1,0]\n"
    "  v_pk_add_f32 v[246:247], v[246:247], v[36:37] op_sel:[0,0] op_sel_hi:[1,0]\n"
    "  v_pk_add_f32 v[248:249], v[248:249], v[36:37] op_sel:[0,0] op_sel_hi:[1,0]\n"
    "  v_pk_add_f32 v[250:251], v[250:251], v[36:37] op_sel:[0,0] op_sel_hi:[1,0]\n"
    "  v_pk_add_f32 v[252:253], v[252:253], v[36:37] op_sel:[0,0] op_sel_hi:[1,0]\n"
    "  v_pk_add_f32 v[254:255], v[254:255], v[36:37] op_sel:[0,0] op_sel_hi:[1,0]\n"
    "  s_nop 1\n"
    "  s_branch .Lgqa_okb\n"
    ".Lgqa_fix1:\n"
    "  v_max_f32 v36, v24, v26\n"
    "  v_mov_b32 v37, v36\n"
    "  s_nop 1\n"
    "  v_permlane32_swap_b32 v36, v37\n"
    "  v_max_f32 v36, v36, v37\n"
    "  ds_write_b32 v2, v36 offset:18432\n"
    "  s_waitcnt lgkmcnt(0)\n"
    "  s_barrier\n"
    "  ds_read_b32 v37, v18 offset:18432\n"
    "  s_waitcnt lgkmcnt(0)\n"
    "  v_max_f32 v36, v36, v37\n"
    "  v_sub_f32 v24, v24, v36\n"
    "  v_sub_f32 v26, v26, v36\n"
    "  v_exp_f32 v10, -v36\n"
    "  v_exp_f32 v11, v24\n"
    "  s_nop 0\n"
    "  v_pk_mul_f32 v[128:129], v[128:129], v[10:11] op_sel:[0,0] op_sel_hi:[1,0]\n"
    "  v_pk_mul_f32 v[130:131], v[130:131], v[10:11] op_sel:[0,0] op_sel_hi:[1,0]\n"
    "  v_pk_mul_f32 v[132:133], v[132:133], v[10:11] op_sel:[0,0] op_sel_hi:[1,0]\n"
    "  v_pk_mul_f32 v[134:135], v[134:135], v[10:11] op_sel:[0,0] op_sel_hi:[1,0]\n"
    "  v_pk_mul_f32 v[136:137], v[136:137], v[10:11] op_sel:[0,0] op_sel_hi:[1,0]\n"
    "  v_pk_mul_f32 v[138:139], v[138:139], v[10:11] op_sel:[0,0] op_sel_hi:[1,0]\n"
    "  v_pk_mul_f32 v[140:141], v[140:141], v[10:11] op_sel:[0,0] op_sel_hi:[1,0]\n"
    "  v_pk_mul_f32 v[142:143], v[142:143], v[10:11] op_sel:[0,0] op_sel_hi:[1,0]\n"
    "  v_pk_mul_f32 v[144:145], v[144:145], v[10:11] op_sel:[0,1] op_sel_hi:[1,1]\n"
    "  v_pk_mul_f32 v[146:147], v[146:147], v[10:11] op_sel:[0,1] op_sel_hi:[1,1]\n"
    "  v_pk_mul_f32 v[148:149], v[148:149], v[10:11] op_sel:[0,1] op_sel_hi:[1,1]\n"
    "  v_pk_mul_f32 v[150:151], v[150:151], v[10:11] op_sel:[0,1] op_sel_hi:[1,1]\n"
    "  v_pk_mul_f32 v[152:153], v[152:153], v[10:11] op_sel:[0,1] op_sel_hi:[1,1]\n"
    "  v_pk_mul_f32 v[154:155], v[154:155], v[10:11] op_sel:[0,1] op_sel_hi:[1,1]\n"
    "  v_pk_mul_f32 v[156:157], v[156:157], v[10:11] op_sel:[0,1] op_sel_hi:[1,1]\n"
    "  v_pk_mul_f32 v[158:159], v[158:159], v[10:11] op_sel:[0,1] op_sel_hi:[1,1]\n"
    "  v_pk_mul_f32 v[160:161], v[160:161], v[10:11] op_sel:[0,1] op_sel_hi:[1,1]\n"
    "  v_pk_mul_f32 v[162:163], v[162:163], v[10:11] op_sel:[0,1] op_sel_hi:[1,1]\n"
    "  v_pk_mul_f32 v[164:165], v[164:165], v[10:11] op_sel:[0,1] op_sel_hi:[1,1]\n"
    "  v_pk_mul_f32 v[166:167], v[166:167], v[10:11] op_sel:[0,1] op_sel_hi:[1,1]\n"
    "  v_pk_mul_f32 v[168:169], v[168:169], v[10:11] op_sel:[0,1] op_sel_hi:[1,1]\n"
    "  v_pk_mul_f32 v[170:171], v[170:171], v[10:11] op_sel:[0,1] op_sel_hi:[1,1]\n"
    "  v_pk_mul_f32 v[172:173], v[172:173], v[10:11] op_sel:[0,1] op_sel_hi:[1,1]\n"
    "  v_pk_mul_f32 v[174:175], v[174:175], v[10:11] op_sel:[0,1] op_sel_hi:[1,1]\n"
    "  v_pk_mul_f32 v[176:177], v[176:177], v[10:11] op_sel:[0,1] op_sel_hi:[1,1]\n"
    "  v_pk_mul_f32 v[178:179], v[178:179], v[10:11] op_sel:[0,1] op_sel_hi:[1,1]\n"
    "  v_pk_mul_f32 v[180:181], v[180:181], v[10:11] op_sel:[0,1] op_sel_hi:[1,1]\n"
    "  v_pk_mul_f32 v[182:183], v[182:183], v[10:11] op_sel:[0,1] op_sel_hi:[1,1]\n"
    "  v_pk_mul_f32 v[184:185], v[184:185], v[10:11] op_sel:[0,1] op_sel_hi:[1,1]\n"
    "  v_pk_mul_f32 v[186:187], v[186:187], v[10:11] op_sel:[0,1] op_sel_hi:[1,1]\n"
    "  v_pk_mul_f32 v[188:189], v[188:189], v[10:11] op_sel:[0,1] op_sel_hi:[1,1]\n"
    "  v_pk_mul_f32 v[190:191], v[190:191], v[10:11] op_sel:[0,1] op_sel_hi:[1,1]\n"
    "  v_exp_f32 v10, v26\n"
    "  s_nop 0\n"
    "  v_pk_mul_f32 v[192:193], v[192:193], v[10:11] op_sel:[0,0] op_sel_hi:[1,0]\n"
    "  v_pk_mul_f32 v[194:195], v[194:195], v[10:11] op_sel:[0,0] op_sel_hi:[1,0]\n"
    "  v_pk_mul_f32 v[196:197], v[196:197], v[10:11] op_sel:[0,0] op_sel_hi:[1,0]\n"
    "  v_pk_mul_f32 v[198:199], v[198:199], v[10:11] op_sel:[0,0] op_sel_hi:[1,0]\n"
    "  v_pk_mul_f32 v[200:201], v[200:201], v[10:11] op_sel:[0,0] op_sel_hi:[1,0]\n"
    "  v_pk_mul_f32 v[202:203], v[202:203], v[10:11] op_sel:[0,0] op_sel_hi:[1,0]\n"
    "  v_pk_mul_f32 v[204:205], v[204:205], v[10:11] op_sel:[0,0] op_sel_hi:[1,0]\n"
    "  v_pk_mul_f32 v[206:207], v[206:207], v[10:11] op_sel:[0,0] op_sel_hi:[1,0]\n"
    "  v_pk_add_f32 v[208:209], v[208:209], v[26:27] op_sel:[0,0] op_sel_hi:[1,0]\n"
    "  v_pk_add_f32 v[210:211], v[210:211], v[26:27] op_sel:[0,0] op_sel_hi:[1,0]\n"
    "  v_pk_add_f32 v[212:213], v[212:213], v[26:27] op_sel:[0,0] op_sel_hi:[1,0]\n"
    "  v_pk_add_f32 v[214:215], v[214:215], v[26:27] op_sel:[0,0] op_sel_hi:[1,0]\n"
    "  v_pk_add_f32 v[216:217], v[216:217], v[26:27] op_sel:[0,0] op_sel_hi:[1,0]\n"
    "  v_pk_add_f32 v[218:219], v[218:219], v[26:27] op_sel:[0,0] op_sel_hi:[1,0]\n"
    "  v_pk_add_f32 v[220:221], v[220:221], v[26:27] op_sel:[0,0] op_sel_hi:[1,0]\n"
    "  v_pk_add_f32 v[222:223], v[222:223], v[26:27] op_sel:[0,0] op_sel_hi:[1,0]\n"
    "  v_pk_add_f32 v[224:225], v[224:225], v[26:27] op_sel:[0,0] op_sel_hi:[1,0]\n"
    "  v_pk_add_f32 v[226:227], v[226:227], v[26:27] op_sel:[0,0] op_sel_hi:[1,0]\n"
    "  v_pk_add_f32 v[228:229], v[228:229], v[26:27] op_sel:[0,0] op_sel_hi:[1,0]\n"
    "  v_pk_add_f32 v[230:231], v[230:231], v[26:27] op_sel:[0,0] op_sel_hi:[1,0]\n"
    "  v_pk_add_f32 v[232:233], v[232:233], v[26:27] op_sel:[0,0] op_sel_hi:[1,0]\n"
    "  v_pk_add_f32 v[234:235], v[234:235], v[26:27] op_sel:[0,0] op_sel_hi:[1,0]\n"
    "  v_pk_add_f32 v[236:237], v[236:237], v[26:27] op_sel:[0,0] op_sel_hi:[1,0]\n"
    "  v_pk_add_f32 v[238:239], v[238:239], v[26:27] op_sel:[0,0] op_sel_hi:[1,0]\n"
    "  v_pk_add_f32 v[240:241], v[240:241], v[26:27] op_sel:[0,0] op_sel_hi:[1,0]\n"
    "  v_pk_add_f32 v[242:243], v[242:243], v[26:27] op_sel:[0,0] op_sel_hi:[1,0]\n"
    "  v_pk_add_f32 v[244:245], v[244:245], v[26:27] op_sel:[0,0] op_sel_hi:[1,0]\n"
    "  v_pk_add_f32 v[246:247], v[246:247], v[26:27] op_sel:[0,0] op_sel_hi:[1,0]\n"
    "  v_pk_add_f32 v[248:249], v[248:249], v[26:27] op_sel:[0,0] op_sel_hi:[1,0]\n"
    "  v_pk_add_f32 v[250:251], v[250:251], v[26:27] op_sel:[0,0] op_sel_hi:[1,0]\n"
    "  v_pk_add_f32 v[252:253], v[252:253], v[26:27] op_sel:[0,0] op_sel_hi:[1,0]\n"
    "  v_pk_add_f32 v[254:255], v[254:255], v[26:27] op_sel:[0,0] op_sel_hi:[1,0]\n"
    "  v_add_f32 v32, v128, v132\n"
    "  v_add_f32 v33, v129, v133\n"
    "  v_add_f32 v34, v130, v134\n"
    "  v_add_f32 v35, v131, v135\n"
    "  v_add_f32 v32, v32, v136\n"
    "  v_add_f32 v33, v33, v137\n"
    "  v_add_f32 v34, v34, v138\n"
    "  v_add_f32 v35, v35, v139\n"
    "  v_add_f32 v32, v32, v140\n"
    "  v_add_f32 v33, v33, v141\n"
    "  v_add_f32 v34, v34, v142\n"
    "  v_add_f32 v35, v35, v143\n"
    "  v_add_f32 v32, v32, v144\n"
    "  v_add_f32 v33, v33, v145\n"
    "  v_add_f32 v34, v34, v146\n"
    "  v_add_f32 v35, v35, v147\n"
    "  v_add_f32 v32, v32, v148\n"
    "  v_add_f32 v33, v33, v149\n"
    "  v_add_f32 v34, v34, v150\n"
    "  v_add_f32 v35, v35, v151\n"
    "  v_add_f32 v32, v32, v152\n"
    "  v_add_f32 v33, v33, v153\n"
    "  v_add_f32 v34, v34, v154\n"
    "  v_add_f32 v35, v35, v155\n"
    "  v_add_f32 v32, v32, v156\n"
    "  v_add_f32 v33, v33, v157\n"
    "  v_add_f32 v34, v34, v158\n"
    "  v_add_f32 v35, v35, v159\n"
    "  v_add_f32 v32, v32, v160\n"
    "  v_add_f32 v33, v33, v161\n"
    "  v_add_f32 v34, v34, v162\n"
    "  v_add_f32 v35, v35, v163\n"
    "  v_add_f32 v32, v32, v164\n"
    "  v_add_f32 v33, v33, v165\n"
    "  v_add_f32 v34, v34, v166\n"
    "  v_add_f32 v35, v35, v167\n"
    "  v_add_f32 v32, v32, v168\n"
    "  v_add_f32 v33, v33, v169\n"
    "  v_add_f32 v34, v34, v170\n"
    "  v_add_f32 v35, v35, v171\n"
    "  v_add_f32 v32, v32, v172\n"
    "  v_add_f32 v33, v33, v173\n"
    "  v_add_f32 v34, v34, v174\n"
    "  v_add_f32 v35, v35, v175\n"
    "  v_add_f32 v32, v32, v176\n"
    "  v_add_f32 v33, v33, v177\n"
    "  v_add_f32 v34, v34, v178\n"
    "  v_add_f32 v35, v35, v179\n"
    "  v_add_f32 v32, v32, v180\n"
    "  v_add_f32 v33, v33, v181\n"
    "  v_add_f32 v34, v34, v182\n"
    "  v_add_f32 v35, v35, v183\n"
    "  v_add_f32 v32, v32, v184\n"
    "  v_add_f32 v33, v33, v185\n"
    "  v_add_f32 v34, v34, v186\n"
    "  v_add_f32 v35, v35, v187\n"
    "  v_add_f32 v32, v32, v188\n"
    "  v_add_f32 v33, v33, v189\n"
    "  v_add_f32 v34, v34, v190\n"
    "  v_add_f32 v35, v35, v191\n"
    "  v_add_f32 v32, v32, v192\n"
    "  v_add_f32 v33, v33, v193\n"
    "  v_add_f32 v34, v34, v194\n"
    "  v_add_f32 v35, v35, v195\n"
    "  v_add_f32 v32, v32, v196\n"
    "  v_add_f32 v33, v33, v197\n"
    "  v_add_f32 v34, v34, v198\n"
    "  v_add_f32 v35, v35, v199\n"
    "  v_cvt_pk_bf16_f32 v40, v144, v145\n"
    "  v_cvt_pk_bf16_f32 v41, v146, v147\n"
    "  v_cvt_pk_bf16_f32 v42, v148, v149\n"
    "  v_cvt_pk_bf16_f32 v43, v150, v151\n"
    "  v_cvt_pk_bf16_f32 v44, v152, v153\n"
    "  v_cvt_pk_bf16_f32 v45, v154, v155\n"
    "  v_cvt_pk_bf16_f32 v46, v156, v157\n"
    "  v_cvt_pk_bf16_f32 v47, v158, v159\n"
    "  ds_write_b128 v16, v[40:43] offset:8192\n"
    "  ds_write_b128 v16, v[44:47] offset:10240\n"
    "  s_waitcnt lgkmcnt(0)\n"
    "  s_barrier\n"
    "  s_branch .Lgqa_fixed1\n"
    ".Lgqa_fix0:\n"
    "  s_waitcnt lgkmcnt(0)\n"
    "  s_barrier\n"
    "  ds_read_b32 v10, v214 offset:18432\n"
    "  ds_read_b32 v11, v214 offset:18688\n"
    "  ds_read_b32 v12, v214 offset:18944\n"
    "  ds_read_b32 v13, v214 offset:19200\n"
    "  s_waitcnt lgkmcnt(1)\n"
    "  v_max_f32 v10, v10, v12\n"
    "  s_waitcnt lgkmcnt(0)\n"
    "  v_max_f32 v11, v11, v13\n"
    "  v_exp_f32 v10, -v10\n"
    "  v_exp_f32 v11, -v11\n"
    "  s_nop 7\n"
    "  v_pk_mul_f32 v[96:97], v[96:97], v[10:11] op_sel:[0,0] op_sel_hi:[1,0]\n"
    "  v_pk_mul_f32 v[98:99], v[98:99], v[10:11] op_sel:[0,0] op_sel_hi:[1,0]\n"
    "  v_pk_mul_f32 v[100:101], v[100:101], v[10:11] op_sel:[0,0] op_sel_hi:[1,0]\n"
    "  v_pk_mul_f32 v[102:103], v[102:103], v[10:11] op_sel:[0,0] op_sel_hi:[1,0]\n"
    "  v_pk_mul_f32 v[104:105], v[104:105], v[10:11] op_sel:[0,0] op_sel_hi:[1,0]\n"
    "  v_pk_mul_f32 v[106:107], v[106:107], v[10:11] op_sel:[0,0] op_sel_hi:[1,0]\n"
    "  v_pk_mul_f32 v[108:109], v[108:109], v[10:11] op_sel:[0,0] op_sel_hi:[1,0]\n"
    "  v_pk_mul_f32 v[110:111], v[110:111], v[10:11] op_sel:[0,0] op_sel_hi:[1,0]\n"
    "  v_pk_mul_f32 v[112:113], v[112:113], v[10:11] op_sel:[0,1] op_sel_hi:[1,1]\n"
    "  v_pk_mul_f32 v[114:115], v[114:115], v[10:11] op_sel:[0,1] op_sel_hi:[1,1]\n"
    "  v_pk_mul_f32 v[116:117], v[116:117], v[10:11] op_sel:[0,1] op_sel_hi:[1,1]\n"
    "  v_pk_mul_f32 v[118:119], v[118:119], v[10:11] op_sel:[0,1] op_sel_hi:[1,1]\n"
    "  v_pk_mul_f32 v[120:121], v[120:121], v[10:11] op_sel:[0,1] op_sel_hi:[1,1]\n"
    "  v_pk_mul_f32 v[122:123], v[122:123], v[10:11] op_sel:[0,1] op_sel_hi:[1,1]\n"
    "  v_pk_mul_f32 v[124:125], v[124:125], v[10:11] op_sel:[0,1] op_sel_hi:[1,1]\n"
    "  v_pk_mul_f32 v[126:127], v[126:127], v[10:11] op_sel:[0,1] op_sel_hi:[1,1]\n"
    "  s_nop 1\n"
    "  v_mov_b32 v10, 0\n"
    "  v_mov_b32 v11, 0\n"
    "  v_mov_b32 v12, 0\n"
    "  v_mov_b32 v13, 0\n"
    "  s_barrier\n"
    "  s_branch .Lgqa_fixed0\n"
    :: "s"(__builtin_amdgcn_kernarg_segment_ptr()), "s"(blockIdx.x), "v"(threadIdx.x), "s"((unsigned)(size_t)(__attribute__((address_space(3))) char*)smem)
    : "v1", "v2", "v3", "v4", "v5", "v6", "v7", "v8", "v9", "v10", "v11", "v12", "v13", "v14", "v15", "v16", "v17", "v18", "v19", "v20", "v21", "v22", "v23", "v24", "v25", "v26", "v27", "v28", "v29", "v30", "v31", "v32", "v33", "v34", "v35", "v36", "v37", "v38", "v39", "v40", "v41", "v42", "v43", "v44", "v45", "v46", "v47", "v48", "v49", "v50", "v51", "v52", "v53", "v54", "v55", "v56", "v57", "v58", "v59", "v60", "v61", "v62", "v63", "v64", "v65", "v66", "v67", "v68", "v69", "v70", "v71", "v72", "v73", "v74", "v75", "v76", "v77", "v78", "v79", "v80", "v81", "v82", "v83", "v84", "v85", "v86", "v87", "v88", "v89", "v90", "v91", "v92", "v93", "v94", "v95", "v96", "v97", "v98", "v99", "v100", "v101", "v102", "v103", "v104", "v105", "v106", "v107", "v108", "v109", "v110", "v111", "v112", "v113", "v114", "v115", "v116", "v117", "v118", "v119", "v120", "v121", "v122", "v123", "v124", "v125", "v126", "v127", "v128", "v129", "v130", "v131", "v132", "v133", "v134", "v135", "v136", "v137", "v138", "v139", "v140", "v141", "v142", "v143", "v144", "v145", "v146", "v147", "v148", "v149", "v150", "v151", "v152", "v153", "v154", "v155", "v156", "v157", "v158", "v159", "v160", "v161", "v162", "v163", "v164", "v165", "v166", "v167", "v168", "v169", "v170", "v171", "v172", "v173", "v174", "v175", "v176", "v177", "v178", "v179", "v180", "v181", "v182", "v183", "v184", "v185", "v186", "v187", "v188", "v189", "v190", "v191", "v192", "v193", "v194", "v195", "v196", "v197", "v198", "v199", "v200", "v201", "v202", "v203", "v204", "v205", "v206", "v207", "v208", "v209", "v210", "v211", "v212", "v213", "v214", "v215", "v216", "v217", "v218", "v219", "v220", "v221", "v222", "v223", "v224", "v225", "v226", "v227", "v228", "v229", "v230", "v231", "v232", "v233", "v234", "v235", "v236", "v237", "v238", "v239", "v240", "v241", "v242", "v243", "v244", "v245", "v246", "v247", "v248", "v249", "v250", "v251", "v252", "v253", "v254", "v255", "s4", "s5", "s6", "s7", "s8", "s9", "s10", "s11", "s12", "s13", "s14", "s15", "s16", "s17", "s18", "s19", "s20", "s21", "s22", "s23", "s24", "s25", "s26", "s27", "s28", "s29", "s30", "s31", "s33", "s34", "s35", "s36", "s37", "s38", "s39", "s40", "s41", "s42", "s43", "s44", "s45", "s46", "s47", "s48", "s49", "s50", "s51", "s52", "s53", "s54", "s55", "s56", "s57", "s58", "s59", "s60", "s61", "s62", "s63", "s64", "s65", "s66", "s67", "s68", "s69", "s70", "s71", "s72", "s73", "s74", "s75", "s76", "s77", "s78", "s79", "s80", "s81", "s82", "s83", "s84", "s85", "s86", "s87", "s88", "s89", "s90", "s91", "s92", "s93", "s94", "s95", "s96", "s97", "s98", "s99", "vcc", "scc", "memory");
}

extern "C" void kernel_launch(void* const* d_in, const int* in_sizes, int n_in, void* d_out, int out_size, void* d_ws, size_t ws_size, hipStream_t stream) {
  gqa_fused<<<256, 512, 0, stream>>>(d_in[0], d_in[1], d_in[2], d_in[3], d_in[4], d_out, d_ws);
}
```
